```python
import torch

def _softmax(x, axis=-1):
    return torch.softmax(x, dim=axis)
def _gelu(x):
    return torch.nn.functional.gelu(x, approximate="tanh")
def _silu(x):
    return torch.nn.functional.silu(x)
def _sigmoid(x):
    return torch.sigmoid(x)
def _layernorm(x, axis=-1, eps=1e-5):
    m = x.mean(dim=axis, keepdim=True)
    v = x.var(dim=axis, keepdim=True, unbiased=False)
    return (x - m) / torch.sqrt(v + eps)
def _scatter_add(tgt, idx, src, axis=0):
    return tgt.clone().index_add_(axis, idx, src)
def _segment_sum(src, seg, n):
    out = torch.zeros((n,) + src.shape[1:], dtype=src.dtype)
    return out.index_add_(0, seg, src)
def _pad(x, pw):
    flat = tuple(v for pair in reversed(pw) for v in pair)
    return torch.nn.functional.pad(x, flat)

kernel_name = 'sequential_preLN__grouped_kv_mqa__geglu_ffn__prompt'

def setup_inputs():

    _w = (lambda a: (lambda t: t.to(torch.bfloat16) if t.is_floating_point() else t)(torch.as_tensor(a)))
    def _A_setup():
        B, S, D, H = 1, 2048, 512, 32
        Dh = D // H
        return {
            "x": _w((lambda *s: torch.randn(*s, dtype=torch.bfloat16) * 0.05)(B, S, D)),
            "wq": _w((lambda *s: torch.randn(*s, dtype=torch.bfloat16) * 0.05)(D, H * Dh)),
            "wk": _w((lambda *s: torch.randn(*s, dtype=torch.bfloat16) * 0.05)(D, Dh)),
            "wv": _w((lambda *s: torch.randn(*s, dtype=torch.bfloat16) * 0.05)(D, Dh)),
            "wo": _w((lambda *s: torch.randn(*s, dtype=torch.bfloat16) * 0.05)(H * Dh, D)),
        }
    _A_in = _A_setup()

    def _B_setup():
        B, S, D, F = 1, 2048, 512, 1792
        return {
            "x": _w((lambda *s: torch.randn(*s, dtype=torch.bfloat16) * 0.05)(B, S, D)),
            "w_gate": _w((lambda *s: torch.randn(*s, dtype=torch.bfloat16) * 0.05)(D, F)),
            "w_up": _w((lambda *s: torch.randn(*s, dtype=torch.bfloat16) * 0.05)(D, F)),
            "w_down": _w((lambda *s: torch.randn(*s, dtype=torch.bfloat16) * 0.05)(F, D)),
        }
    _B_in = _B_setup()

    return {
        "A_x": _A_in["x"],
        "A_wq": _A_in["wq"],
        "A_wk": _A_in["wk"],
        "A_wv": _A_in["wv"],
        "A_wo": _A_in["wo"],
        "B_x": _B_in["x"],
        "B_w_gate": _B_in["w_gate"],
        "B_w_up": _B_in["w_up"],
        "B_w_down": _B_in["w_down"],
    }

def reference(A_x, A_wq, A_wk, A_wv, A_wo, B_x, B_w_gate, B_w_up, B_w_down):
    def _A_ref(x, wq, wk, wv, wo):
        B, S, D = x.shape
        H = 32
        Dh = D // H
        q = (lambda x, *d: x.permute(*d))((x @ wq).reshape(B, S, H, Dh), 0, 2, 1, 3)
        k = x @ wk
        v = x @ wv
        scores = torch.einsum("bhqd,bkd->bhqk", q, k) * (Dh ** -0.5)
        p = _softmax(scores, axis=-1)
        o = torch.einsum("bhqk,bkd->bhqd", p, v)
        return (lambda x, *d: x.permute(*d))(o, 0, 2, 1, 3).reshape(B, S, H * Dh) @ wo

    def _B_ref(x, w_gate, w_up, w_down):
        return (_gelu(x @ w_gate) * (x @ w_up)) @ w_down

    x = A_x
    h = x + _A_ref(_layernorm(x, axis=-1), A_wq, A_wk, A_wv, A_wo)
    h2 = h + _B_ref(_layernorm(h, axis=-1), B_w_gate, B_w_up, B_w_down)
    return h2
```

```cpp
#include <hip/hip_runtime.h>
#include <hip/hip_bf16.h>
#include <stdint.h>

constexpr int S = 2048, D = 512, H = 32, DH = 16, F = 1792;
typedef unsigned short u16;
__device__ __forceinline__ float bf2f(u16 v) { return __uint_as_float(((unsigned)v) << 16); }
__device__ __forceinline__ u16 f2bf(float f) { __hip_bfloat16 b = __float2bfloat16(f); return *(u16*)&b; }

template <typename T> __global__ void ln_kernel(const T* in, float* out) {
  int row = blockIdx.x * 4 + (threadIdx.x >> 6), lane = threadIdx.x & 63;
  float v[8]; float s = 0;
  for (int i = 0; i < 8; i++) { float x; if constexpr (sizeof(T) == 2) x = bf2f(((const u16*)in)[row * D + lane + 64 * i]); else x = ((const float*)in)[row * D + lane + 64 * i]; v[i] = x; s += x; }
  for (int o = 32; o; o >>= 1) s += __shfl_xor(s, o);
  float m = s / D; float q = 0;
  for (int i = 0; i < 8; i++) { v[i] -= m; q += v[i] * v[i]; }
  for (int o = 32; o; o >>= 1) q += __shfl_xor(q, o);
  float r = 1.0f / sqrtf(q / D + 1e-5f);
  for (int i = 0; i < 8; i++) out[row * D + lane + 64 * i] = bf2f(f2bf(v[i] * r));
}
__global__ void gemm_naive(const float* A, const u16* W, float* C, int M, int N, int K) {
  __shared__ float As[16][65]; __shared__ float Ws[16][65];
  int tx = threadIdx.x & 15, ty = threadIdx.x >> 4;
  int m0 = blockIdx.y * 64, n0 = blockIdx.x * 64;
  float acc[4][4] = {};
  for (int k0 = 0; k0 < K; k0 += 16) {
    for (int i = threadIdx.x; i < 64 * 16; i += 256) { int m = i / 16, k = i % 16; As[k][m] = A[(m0 + m) * K + k0 + k]; }
    for (int i = threadIdx.x; i < 64 * 16; i += 256) { int k = i / 64, n = i % 64; Ws[k][n] = (n0 + n < N) ? bf2f(W[(k0 + k) * N + n0 + n]) : 0.f; }
    __syncthreads();
    for (int k = 0; k < 16; k++) { float a[4], b[4]; for (int i = 0; i < 4; i++) { a[i] = As[k][ty * 4 + i]; b[i] = Ws[k][tx * 4 + i]; }
      for (int i = 0; i < 4; i++) for (int j = 0; j < 4; j++) acc[i][j] += a[i] * b[j]; }
    __syncthreads();
  }
  for (int i = 0; i < 4; i++) for (int j = 0; j < 4; j++) if (n0 + tx * 4 + j < N) C[(m0 + ty * 4 + i) * N + n0 + tx * 4 + j] = acc[i][j];
}
__global__ void attn_naive(const float* q, const float* k, const float* v, float* o) {
  __shared__ float ks[256][DH], vs[256][DH];
  int idx = blockIdx.x * 256 + threadIdx.x; int t = idx / H, h = idx % H;
  float qq[DH]; for (int d = 0; d < DH; d++) qq[d] = bf2f(f2bf(q[t * D + h * DH + d])) * 0.25f;
  float m = -INFINITY, l = 0, acc[DH] = {};
  for (int j0 = 0; j0 < S; j0 += 256) {
    __syncthreads();
    for (int d = 0; d < DH; d++) { ks[threadIdx.x][d] = bf2f(f2bf(k[(j0 + threadIdx.x) * DH + d])); vs[threadIdx.x][d] = bf2f(f2bf(v[(j0 + threadIdx.x) * DH + d])); }
    __syncthreads();
    for (int j = 0; j < 256; j++) { float s = 0; for (int d = 0; d < DH; d++) s += qq[d] * ks[j][d];
      float mn = fmaxf(m, s); float a = __expf(m - mn), p = __expf(s - mn); l = l * a + p; for (int d = 0; d < DH; d++) acc[d] = acc[d] * a + p * vs[j][d]; m = mn; }
  }
  for (int d = 0; d < DH; d++) o[t * D + h * DH + d] = bf2f(f2bf(acc[d] / l));
}
__global__ void add_res(const u16* x, const float* a, float* h) { int i = blockIdx.x * 256 + threadIdx.x; h[i] = bf2f(f2bf(bf2f(x[i]) + a[i])); }
__global__ void geglu(const float* g, const float* u, float* a) { int i = blockIdx.x * 256 + threadIdx.x; float x = g[i]; float t = tanhf(0.7978845608f * (x + 0.044715f * x * x * x)); a[i] = bf2f(f2bf(0.5f * x * (1.f + t) * u[i])); }
__global__ void add_out(const float* h, const float* a, u16* out) { int i = blockIdx.x * 256 + threadIdx.x; out[i] = f2bf(h[i] + a[i]); }

extern "C" void kernel_launch(void* const* d_in, const int* in_sizes, int n_in, void* d_out, int out_size, void* d_ws, size_t ws_size, hipStream_t stream) {
  const u16 *x = (const u16*)d_in[0], *wq = (const u16*)d_in[1], *wk = (const u16*)d_in[2], *wv = (const u16*)d_in[3], *wo = (const u16*)d_in[4];
  const u16 *wg = (const u16*)d_in[6], *wu = (const u16*)d_in[7], *wd = (const u16*)d_in[8];
  float* ws = (float*)d_ws;
  float *ln = ws, *q = ln + S * D, *k = q + S * D, *v = k + S * DH, *o = v + S * DH, *a = o + S * D, *h = a + S * D, *g = h + S * D, *u = g + S * F, *act = u + S * F;
  ln_kernel<u16><<<S / 4, 256, 0, stream>>>(x, ln);
  gemm_naive<<<dim3(D / 64, S / 64), 256, 0, stream>>>(ln, wq, q, S, D, D);
  gemm_naive<<<dim3(1, S / 64), 256, 0, stream>>>(ln, wk, k, S, DH, D);
  gemm_naive<<<dim3(1, S / 64), 256, 0, stream>>>(ln, wv, v, S, DH, D);
  attn_naive<<<S * H / 256, 256, 0, stream>>>(q, k, v, o);
  gemm_naive<<<dim3(D / 64, S / 64), 256, 0, stream>>>(o, wo, a, S, D, D);
  add_res<<<S * D / 256, 256, 0, stream>>>(x, a, h);
  ln_kernel<float><<<S / 4, 256, 0, stream>>>(h, ln);
  gemm_naive<<<dim3(F / 64, S / 64), 256, 0, stream>>>(ln, wg, g, S, F, D);
  gemm_naive<<<dim3(F / 64, S / 64), 256, 0, stream>>>(ln, wu, u, S, F, D);
  geglu<<<S * F / 256, 256, 0, stream>>>(g, u, act);
  gemm_naive<<<dim3(D / 64, S / 64), 256, 0, stream>>>(act, wd, a, S, D, F);
  add_out<<<S * D / 256, 256, 0, stream>>>(h, a, (u16*)d_out);
}
```

```cpp
#include <hip/hip_runtime.h>
#include <stdint.h>

struct KArgs {
  const void *x, *wq, *wk, *wv;
  void* ws;
  const void *wo, *wg, *wu, *wd;
  void* out;
};

__global__ void __launch_bounds__(256) fused_block(KArgs args) {
  extern __shared__ __attribute__((aligned(16))) char lds_all[];
  auto ka = __builtin_amdgcn_kernarg_segment_ptr();
  unsigned b = blockIdx.x;
  unsigned w = __builtin_amdgcn_readfirstlane(threadIdx.x >> 6);
  asm volatile(R"ASM(
s_load_dwordx16 s[64:79], %0, 0
s_mov_b32 s48, %1
s_mov_b32 s49, %2
v_mbcnt_lo_u32_b32 v0, -1, 0
v_mbcnt_hi_u32_b32 v0, -1, v0
s_getpc_b64 s[58:59]
L_cpf:
s_lshr_b32 s56, s48, 3
s_mul_i32 s56, s56, 3072
s_mul_i32 s57, s49, 768
s_add_u32 s56, s56, s57
v_min_u32 v46, 5, v0
v_lshlrev_b32 v46, 7, v46
v_add_u32 v46, s56, v46
v_min_u32 v46, L_main_end-L_cpf-4, v46
global_load_dword a255, v46, s[58:59]
s_mov_b32 s10, 0x200000
s_mov_b32 s11, 0x20000
s_mov_b32 s14, 0x80000
s_mov_b32 s15, 0x20000
s_mov_b32 s26, 0x80000
s_mov_b32 s27, 0x20000
s_mov_b32 s30, 0x1c0000
s_mov_b32 s31, 0x20000
s_mov_b32 s34, 0x1c0000
s_mov_b32 s35, 0x20000
s_mov_b32 s38, 0x1c0000
s_mov_b32 s39, 0x20000
s_mov_b32 s42, 0x200000
s_mov_b32 s43, 0x20000
s_mov_b32 s46, 0x2000000
s_mov_b32 s47, 0x20000
s_mov_b32 s62, 0x4000
s_mov_b32 s63, 0x20000
s_mov_b32 s82, 0xffff0000
s_and_b32 s54, s48, 7
s_lshl_b32 s54, s54, 3
s_bfe_u32 s55, s48, 0x30003
s_add_u32 s50, s54, s55
s_lshr_b32 s51, s48, 6
v_lshlrev_b32 v1, 4, v0
v_lshrrev_b32 v34, 3, v0
v_lshlrev_b32 v34, 10, v34
v_and_b32 v35, 7, v0
v_lshl_or_b32 v34, v35, 4, v34
s_lshl_b32 s54, s50, 15
s_lshl_b32 s55, s49, 13
s_add_u32 s54, s54, s55
v_lshrrev_b32 v40, 4, v0
v_and_b32 v41, 15, v0
v_lshlrev_b32 v42, 2, v40
v_xor_b32 v41, v41, v42
v_lshlrev_b32 v41, 4, v41
v_lshl_or_b32 v36, v40, 10, v41
v_add_u32 v37, 3072, v36
v_add_u32 v38, 0x1800, v36
v_add_u32 v39, 0x2400, v36
s_waitcnt lgkmcnt(0)
s_mov_b64 s[8:9], s[64:65]
buffer_load_dwordx4 v[2:5], v34, s[8:11], s54 offen offset:0
s_mov_b64 s[44:45], s[72:73]
buffer_load_dwordx4 v[6:9], v34, s[8:11], s54 offen offset:128
s_mov_b64 s[12:13], s[66:67]
buffer_load_dwordx4 v[10:13], v34, s[8:11], s54 offen offset:256
s_cmp_lt_u32 s51, 2
s_cselect_b64 s[60:61], s[68:69], s[70:71]
buffer_load_dwordx4 v[14:17], v34, s[8:11], s54 offen offset:384
s_mov_b64 s[24:25], s[74:75]
buffer_load_dwordx4 v[18:21], v34, s[8:11], s54 offen offset:512
s_mov_b64 s[28:29], s[76:77]
buffer_load_dwordx4 v[22:25], v34, s[8:11], s54 offen offset:640
s_mov_b64 s[32:33], s[78:79]
buffer_load_dwordx4 v[26:29], v34, s[8:11], s54 offen offset:768
s_load_dwordx2 s[36:37], %0, 64
s_load_dwordx2 s[40:41], %0, 72
buffer_load_dwordx4 v[30:33], v34, s[8:11], s54 offen offset:896
buffer_load_dword v35, off, s[44:47], 0 sc1
s_and_b32 s54, s48, 7
s_lshr_b32 s55, s48, 3
s_lshl_b32 s56, s54, 1
s_lshr_b32 s57, s55, 4
s_add_u32 s72, s56, s57
s_and_b32 s73, s55, 15
s_lshl_b32 s56, s54, 2
s_lshr_b32 s57, s55, 3
s_add_u32 s74, s56, s57
s_and_b32 s75, s55, 7
v_bfe_u32 v46, v0, 1, 4
v_lshlrev_b32 v46, 10, v46
v_and_b32 v47, 1, v0
v_lshl_or_b32 v46, v47, 7, v46
s_bfe_u32 s56, s48, 0x30003
s_lshl_b32 s56, s56, 16
s_lshl_b32 s57, s49, 14
s_add_u32 s56, s56, s57
s_lshl_b32 s57, s51, 8
s_add_u32 s56, s56, s57
buffer_load_dword a254, v46, s[12:15], s56 offen
v_lshlrev_b32 v47, 7, v47
s_bfe_u32 s56, s48, 0x40003
s_lshl_b32 s56, s56, 10
s_lshl_b32 s57, s49, 8
s_add_u32 s56, s56, s57
buffer_load_dword a253, v47, s[60:63], s56 offen
v_mov_b32 v82, 0x3f80
s_lshl_b32 s70, s49, 12
v_mov_b32 v83, 0x3f800000
v_lshrrev_b32 v40, 3, v0
s_lshl_b32 s54, s49, 3
v_add_u32 v40, s54, v40
v_and_b32 v41, 7, v0
v_lshlrev_b32 v42, 2, v41
v_lshl_add_u32 v42, v40, 4, v42
v_lshlrev_b32 v43, 2, v40
v_add_u32 v43, 512, v43
v_cmp_gt_u32 vcc, 4, v41
s_nop 1
v_cndmask_b32 v42, v43, v42, vcc
v_add_u32 v84, 0x18000, v42
v_mov_b32 v43, 0x3eb8aa3b
v_cndmask_b32 v85, 1.0, v43, vcc
s_and_b32 s55, s51, 1
s_lshl_b32 s56, s55, 8
s_add_u32 s56, s56, 0x18000
s_lshl_b32 s57, s55, 6
s_add_u32 s57, s57, 0x18200
s_cmp_lt_u32 s51, 2
s_cselect_b32 s56, s56, s57
s_cselect_b32 s57, 15, 48
s_cselect_b32 s55, 4, 0
v_and_b32 v42, s57, v0
v_lshlrev_b32 v42, s55, v42
v_add_u32 v86, s56, v42
v_and_b32 v42, 31, v0
v_lshlrev_b32 v42, 2, v42
v_add_u32 v87, 0x18200, v42
v_lshrrev_b32 v40, 3, v0
s_lshl_b32 s54, s49, 3
v_add_u32 v40, s54, v40
v_and_b32 v41, 7, v0
v_xor_b32 v40, v40, v41
v_lshlrev_b32 v40, 4, v40
v_lshl_add_u32 v40, v41, 9, v40
v_add_u32 v50, 0x20000, v40
v_lshrrev_b32 v40, 4, v0
v_bfe_u32 v41, v0, 2, 2
v_lshl_add_u32 v41, v40, 3, v41
v_lshlrev_b32 v41, 5, v41
v_and_b32 v42, 3, v0
v_lshl_add_u32 v41, v42, 3, v41
s_lshl_b32 s54, s49, 12
s_add_u32 s54, s54, 0x1c000
v_add_u32 v51, s54, v41
v_add_u32 v61, s54, v1
s_and_b32 s55, s51, 1
s_lshl_b32 s56, s55, 4
v_and_b32 v42, 15, v0
v_add_u32 v42, s56, v42
s_lshl_b32 s57, s49, 13
s_add_u32 s57, s57, 0x20000
v_add_u32 v44, 0, v40
v_xor_b32 v45, v42, v44
v_lshlrev_b32 v45, 4, v45
v_lshl_add_u32 v45, v44, 9, v45
v_add_u32 v52, s57, v45
v_add_u32 v44, 4, v40
v_xor_b32 v45, v42, v44
v_lshlrev_b32 v45, 4, v45
v_lshl_add_u32 v45, v44, 9, v45
v_add_u32 v53, s57, v45
s_lshl_b32 s56, s55, 8
s_add_u32 s56, s56, 0x10000
s_lshl_b32 s57, s55, 9
s_add_u32 s57, s57, 0x20000
s_cmp_lt_u32 s51, 2
s_cselect_b32 s83, s56, s57
s_cselect_b32 s57, 9, 8
s_lshl_b32 s54, s50, 10
s_add_u32 s83, s83, s54
v_and_b32 v44, 15, v0
v_lshlrev_b32 v44, 4, v44
v_lshrrev_b32 v45, 5, v0
v_lshl_add_u32 v44, v45, 3, v44
v_and_b32 v45, 1, v40
v_lshlrev_b32 v45, s57, v45
v_add_u32 v60, v44, v45
v_and_b32 v44, 31, v0
v_lshrrev_b32 v45, 5, v0
v_add_u32 v46, 0, v45
v_xor_b32 v47, v44, v46
v_lshlrev_b32 v47, 4, v47
v_lshl_add_u32 v47, v46, 9, v47
v_add_u32 v54, 0x20000, v47
v_add_u32 v46, 2, v45
v_xor_b32 v47, v44, v46
v_lshlrev_b32 v47, 4, v47
v_lshl_add_u32 v47, v46, 9, v47
v_add_u32 v55, 0x20000, v47
v_add_u32 v46, 4, v45
v_xor_b32 v47, v44, v46
v_lshlrev_b32 v47, 4, v47
v_lshl_add_u32 v47, v46, 9, v47
v_add_u32 v56, 0x20000, v47
v_add_u32 v46, 6, v45
v_xor_b32 v47, v44, v46
v_lshlrev_b32 v47, 4, v47
v_lshl_add_u32 v47, v46, 9, v47
v_add_u32 v57, 0x20000, v47
v_bfe_u32 v46, v0, 2, 2
v_lshl_add_u32 v47, v45, 3, v46
v_lshlrev_b32 v47, 8, v47
v_xor_b32 v46, s49, v46
v_lshl_add_u32 v47, v46, 6, v47
v_bfe_u32 v46, v0, 4, 1
v_lshl_add_u32 v47, v46, 5, v47
v_and_b32 v46, 3, v0
v_lshl_add_u32 v58, v46, 3, v47
v_add_u32 v59, 0x10000, v58
v_and_b32 v46, 31, v0
v_lshrrev_b32 v47, 5, v0
v_lshlrev_b32 v47, 3, v47
v_sub_u32 v46, v46, v47
v_mov_b32 v47, 0x3f80
v_subrev_u32 v44, 0, v46
v_cmp_gt_u32 vcc, 2, v44
v_lshlrev_b32 v44, 4, v44
v_lshlrev_b32 v44, v44, v47
v_cndmask_b32 v44, 0, v44, vcc
v_accvgpr_write_b32 a216, v44
v_subrev_u32 v45, 2, v46
v_cmp_gt_u32 vcc, 2, v45
v_lshlrev_b32 v45, 4, v45
v_lshlrev_b32 v45, v45, v47
v_cndmask_b32 v45, 0, v45, vcc
v_accvgpr_write_b32 a217, v45
v_subrev_u32 v44, 4, v46
s_add_u32 s71, s70, 0x1c000
v_cmp_gt_u32 vcc, 2, v44
v_lshlrev_b32 v44, 4, v44
v_lshlrev_b32 v44, v44, v47
v_cndmask_b32 v44, 0, v44, vcc
v_accvgpr_write_b32 a218, v44
v_subrev_u32 v45, 6, v46
v_cmp_gt_u32 vcc, 2, v45
v_lshlrev_b32 v45, 4, v45
v_lshlrev_b32 v45, v45, v47
v_cndmask_b32 v45, 0, v45, vcc
v_accvgpr_write_b32 a219, v45
v_subrev_u32 v44, 16, v46
v_cmp_gt_u32 vcc, 2, v44
v_lshlrev_b32 v44, 4, v44
v_lshlrev_b32 v44, v44, v47
v_cndmask_b32 v44, 0, v44, vcc
v_accvgpr_write_b32 a220, v44
v_subrev_u32 v45, 18, v46
v_cmp_gt_u32 vcc, 2, v45
v_lshlrev_b32 v45, 4, v45
v_lshlrev_b32 v45, v45, v47
v_cndmask_b32 v45, 0, v45, vcc
v_accvgpr_write_b32 a221, v45
v_subrev_u32 v44, 20, v46
v_cmp_gt_u32 vcc, 2, v44
v_lshlrev_b32 v44, 4, v44
v_lshlrev_b32 v44, v44, v47
v_cndmask_b32 v44, 0, v44, vcc
v_accvgpr_write_b32 a222, v44
v_subrev_u32 v45, 22, v46
v_cmp_gt_u32 vcc, 2, v45
v_lshlrev_b32 v45, 4, v45
v_lshlrev_b32 v45, v45, v47
v_cndmask_b32 v45, 0, v45, vcc
v_accvgpr_write_b32 a223, v45
v_and_b32 v48, 27, v0
v_cmp_eq_u32 vcc, 16, v48
v_mov_b32 v49, 0x3f803f80
s_nop 0
v_cndmask_b32 v48, 0, v49, vcc
v_accvgpr_write_b32 a64, v48
v_accvgpr_write_b32 a65, v48
v_accvgpr_write_b32 a66, v48
v_accvgpr_write_b32 a67, v48
v_accvgpr_write_b32 a68, v48
v_accvgpr_write_b32 a69, v48
v_accvgpr_write_b32 a70, v48
v_accvgpr_write_b32 a71, v48
v_accvgpr_write_b32 a72, v48
v_accvgpr_write_b32 a73, v48
v_accvgpr_write_b32 a74, v48
v_accvgpr_write_b32 a75, v48
v_accvgpr_write_b32 a76, v48
v_accvgpr_write_b32 a77, v48
v_accvgpr_write_b32 a78, v48
v_accvgpr_write_b32 a79, v48
v_accvgpr_write_b32 a80, v48
v_accvgpr_write_b32 a81, v48
v_accvgpr_write_b32 a82, v48
v_accvgpr_write_b32 a83, v48
v_accvgpr_write_b32 a84, v48
v_accvgpr_write_b32 a85, v48
v_accvgpr_write_b32 a86, v48
v_accvgpr_write_b32 a87, v48
v_accvgpr_write_b32 a88, v48
v_accvgpr_write_b32 a89, v48
v_accvgpr_write_b32 a90, v48
v_accvgpr_write_b32 a91, v48
v_accvgpr_write_b32 a92, v48
v_accvgpr_write_b32 a93, v48
v_accvgpr_write_b32 a94, v48
v_accvgpr_write_b32 a95, v48
v_accvgpr_write_b32 a96, v48
v_accvgpr_write_b32 a97, v48
v_accvgpr_write_b32 a98, v48
v_accvgpr_write_b32 a99, v48
v_accvgpr_write_b32 a100, v48
v_accvgpr_write_b32 a101, v48
v_accvgpr_write_b32 a102, v48
v_accvgpr_write_b32 a103, v48
v_accvgpr_write_b32 a104, v48
v_accvgpr_write_b32 a105, v48
v_accvgpr_write_b32 a106, v48
v_accvgpr_write_b32 a107, v48
v_accvgpr_write_b32 a108, v48
v_accvgpr_write_b32 a109, v48
v_accvgpr_write_b32 a110, v48
v_accvgpr_write_b32 a111, v48
v_accvgpr_write_b32 a112, v48
v_accvgpr_write_b32 a113, v48
v_accvgpr_write_b32 a114, v48
v_accvgpr_write_b32 a115, v48
v_accvgpr_write_b32 a116, v48
v_accvgpr_write_b32 a117, v48
v_accvgpr_write_b32 a118, v48
v_accvgpr_write_b32 a119, v48
v_accvgpr_write_b32 a120, v48
v_accvgpr_write_b32 a121, v48
v_accvgpr_write_b32 a122, v48
v_accvgpr_write_b32 a123, v48
v_accvgpr_write_b32 a124, v48
v_accvgpr_write_b32 a125, v48
v_accvgpr_write_b32 a126, v48
v_accvgpr_write_b32 a127, v48
v_accvgpr_write_b32 a128, v48
v_accvgpr_write_b32 a129, v48
v_accvgpr_write_b32 a130, v48
v_accvgpr_write_b32 a131, v48
v_accvgpr_write_b32 a132, v48
v_accvgpr_write_b32 a133, v48
v_accvgpr_write_b32 a134, v48
v_accvgpr_write_b32 a135, v48
v_accvgpr_write_b32 a136, v48
v_accvgpr_write_b32 a137, v48
v_accvgpr_write_b32 a138, v48
v_accvgpr_write_b32 a139, v48
v_accvgpr_write_b32 a140, v48
v_accvgpr_write_b32 a141, v48
v_accvgpr_write_b32 a142, v48
v_accvgpr_write_b32 a143, v48
v_accvgpr_write_b32 a144, v48
v_accvgpr_write_b32 a145, v48
v_accvgpr_write_b32 a146, v48
v_accvgpr_write_b32 a147, v48
v_accvgpr_write_b32 a148, v48
v_accvgpr_write_b32 a149, v48
s_mov_b32 m0, s71
v_accvgpr_write_b32 a150, v48
v_accvgpr_write_b32 a151, v48
v_accvgpr_write_b32 a152, v48
v_accvgpr_write_b32 a153, v48
v_accvgpr_write_b32 a154, v48
v_accvgpr_write_b32 a155, v48
v_accvgpr_write_b32 a156, v48
v_accvgpr_write_b32 a157, v48
v_accvgpr_write_b32 a158, v48
v_accvgpr_write_b32 a159, v48
v_accvgpr_write_b32 a160, v48
v_accvgpr_write_b32 a161, v48
v_accvgpr_write_b32 a162, v48
v_accvgpr_write_b32 a163, v48
v_accvgpr_write_b32 a164, v48
v_accvgpr_write_b32 a165, v48
v_accvgpr_write_b32 a166, v48
v_accvgpr_write_b32 a167, v48
v_accvgpr_write_b32 a168, v48
v_accvgpr_write_b32 a169, v48
v_accvgpr_write_b32 a170, v48
v_accvgpr_write_b32 a171, v48
v_accvgpr_write_b32 a172, v48
v_accvgpr_write_b32 a173, v48
v_accvgpr_write_b32 a174, v48
v_accvgpr_write_b32 a175, v48
v_accvgpr_write_b32 a176, v48
v_accvgpr_write_b32 a177, v48
v_accvgpr_write_b32 a178, v48
v_accvgpr_write_b32 a179, v48
v_accvgpr_write_b32 a180, v48
v_accvgpr_write_b32 a181, v48
v_accvgpr_write_b32 a182, v48
v_accvgpr_write_b32 a183, v48
v_accvgpr_write_b32 a184, v48
v_accvgpr_write_b32 a185, v48
v_accvgpr_write_b32 a186, v48
v_accvgpr_write_b32 a187, v48
v_accvgpr_write_b32 a188, v48
v_accvgpr_write_b32 a189, v48
v_accvgpr_write_b32 a190, v48
v_accvgpr_write_b32 a191, v48
v_mov_b32 v41, 0x3f803f80
v_mov_b32 v42, 0
v_mov_b32 v43, 0
v_mov_b32 v44, 0
v_mov_b32 v45, 0
s_lshl_b32 s93, s51, 2
s_add_u32 s93, s93, s49
s_lshl_b32 s93, s93, 2
s_add_u32 s93, s93, 3
s_mul_i32 s94, s49, 224
s_lshl_b32 s54, s75, 1
s_add_u32 s94, s94, s54
v_and_b32 v40, 31, v0
v_lshrrev_b32 v46, 5, v0
v_lshlrev_b32 v47, 3, v46
v_lshl_add_u32 v47, v40, 10, v47
v_accvgpr_write_b32 a242, v47
v_lshlrev_b32 v46, 4, v46
v_lshl_add_u32 v47, v40, 10, v46
v_accvgpr_write_b32 a241, v47
v_mul_u32_u24 v47, 0xe00, v40
v_add_u32 v47, v47, v46
v_accvgpr_write_b32 a240, v47
v_lshrrev_b32 v40, 3, v0
v_mul_u32_u24 v40, 896, v40
v_and_b32 v46, 7, v0
v_lshl_add_u32 v40, v46, 4, v40
v_accvgpr_write_b32 a243, v40
v_min_u32 v40, 31, v0
v_lshlrev_b32 v40, 7, v40
v_accvgpr_write_b32 a245, v40
v_subrev_u32 v40, 16, v0
v_min_u32 v40, 27, v40
v_lshrrev_b32 v46, 1, v40
v_and_b32 v40, 1, v40
v_lshl_add_u32 v40, v46, 4, v40
v_add_u32 v40, s94, v40
v_lshl_add_u32 v46, v0, 6, s93
v_cmp_gt_u32 vcc, 16, v0
s_nop 1
v_cndmask_b32 v40, v40, v46, vcc
v_lshlrev_b32 v40, 3, v40
v_accvgpr_write_b32 a244, v40
s_lshr_b32 s54, s48, 4
s_and_b32 s55, s48, 15
s_mul_i32 s84, s55, 114688
s_mul_i32 s57, s54, 224
s_add_u32 s84, s84, s57
s_lshl_b32 s57, s49, 5
s_add_u32 s84, s84, s57
s_mul_i32 s76, s54, 229376
s_lshl_b32 s57, s55, 11
s_add_u32 s76, s76, s57
s_add_u32 s76, s76, 0x600000
s_lshl_b32 s57, s49, 15
s_add_u32 s76, s76, s57
s_mul_i32 s79, s48, 7
s_add_u32 s79, s79, s49
s_lshl_b32 s79, s79, 7
s_add_u32 s79, s79, 0x30000
s_lshl_b32 s56, s54, 15
s_lshl_b32 s57, s55, 6
s_add_u32 s56, s56, s57
s_lshl_b32 s77, s55, 15
s_lshl_b32 s57, s54, 11
s_add_u32 s77, s77, s57
s_add_u32 s77, s77, 0x100000
s_add_u32 s57, s84, 128
s_add_u32 s58, s76, 0x20000
s_cmp_eq_u32 s49, 3
s_cselect_b32 s81, s56, s57
s_cselect_b32 s77, s77, s58
s_lshl_b32 s54, s48, 2
s_add_u32 s54, s54, s49
s_lshr_b32 s80, s54, 4
s_and_b32 s54, s54, 15
s_lshl_b32 s85, s80, 15
s_lshl_b32 s57, s54, 6
s_add_u32 s85, s85, s57
s_lshr_b32 s57, s54, 1
s_mul_i32 s78, s57, 229376
s_lshl_b32 s57, s80, 12
s_add_u32 s78, s78, s57
s_and_b32 s57, s54, 1
s_lshl_b32 s57, s57, 10
s_waitcnt vmcnt(2)
s_waitcnt lgkmcnt(0)
v_dot2_f32_bf16 v42, v2, v41, v42
buffer_load_dwordx4 v1, s[60:63], s70 offen offset:0 lds
v_dot2_f32_bf16 v44, v2, v2, v44
v_dot2_f32_bf16 v43, v3, v41, v43
v_dot2_f32_bf16 v45, v3, v3, v45
v_dot2_f32_bf16 v42, v4, v41, v42
v_dot2_f32_bf16 v44, v4, v4, v44
v_dot2_f32_bf16 v43, v5, v41, v43
v_dot2_f32_bf16 v45, v5, v5, v45
v_dot2_f32_bf16 v42, v6, v41, v42
buffer_load_dwordx4 v1, s[60:63], s70 offen offset:1024 lds
v_dot2_f32_bf16 v44, v6, v6, v44
v_dot2_f32_bf16 v43, v7, v41, v43
v_dot2_f32_bf16 v45, v7, v7, v45
v_dot2_f32_bf16 v42, v8, v41, v42
v_dot2_f32_bf16 v44, v8, v8, v44
v_dot2_f32_bf16 v43, v9, v41, v43
v_dot2_f32_bf16 v45, v9, v9, v45
v_dot2_f32_bf16 v42, v10, v41, v42
buffer_load_dwordx4 v1, s[60:63], s70 offen offset:2048 lds
v_dot2_f32_bf16 v44, v10, v10, v44
v_dot2_f32_bf16 v43, v11, v41, v43
v_dot2_f32_bf16 v45, v11, v11, v45
v_dot2_f32_bf16 v42, v12, v41, v42
v_dot2_f32_bf16 v44, v12, v12, v44
v_dot2_f32_bf16 v43, v13, v41, v43
v_dot2_f32_bf16 v45, v13, v13, v45
v_dot2_f32_bf16 v42, v14, v41, v42
v_dot2_f32_bf16 v44, v14, v14, v44
buffer_load_dwordx4 v1, s[60:63], s70 offen offset:3072 lds
v_dot2_f32_bf16 v43, v15, v41, v43
v_dot2_f32_bf16 v45, v15, v15, v45
v_dot2_f32_bf16 v42, v16, v41, v42
v_dot2_f32_bf16 v44, v16, v16, v44
v_dot2_f32_bf16 v43, v17, v41, v43
v_dot2_f32_bf16 v45, v17, v17, v45
v_dot2_f32_bf16 v42, v18, v41, v42
v_dot2_f32_bf16 v44, v18, v18, v44
s_lshl_b32 s70, s49, 14
v_dot2_f32_bf16 v43, v19, v41, v43
v_dot2_f32_bf16 v45, v19, v19, v45
v_dot2_f32_bf16 v42, v20, v41, v42
v_dot2_f32_bf16 v44, v20, v20, v44
v_dot2_f32_bf16 v43, v21, v41, v43
v_dot2_f32_bf16 v45, v21, v21, v45
v_dot2_f32_bf16 v42, v22, v41, v42
v_dot2_f32_bf16 v44, v22, v22, v44
v_dot2_f32_bf16 v43, v23, v41, v43
s_lshl_b32 s71, s51, 8
v_dot2_f32_bf16 v45, v23, v23, v45
v_dot2_f32_bf16 v42, v24, v41, v42
v_dot2_f32_bf16 v44, v24, v24, v44
v_dot2_f32_bf16 v43, v25, v41, v43
v_dot2_f32_bf16 v45, v25, v25, v45
v_dot2_f32_bf16 v42, v26, v41, v42
v_dot2_f32_bf16 v44, v26, v26, v44
v_dot2_f32_bf16 v43, v27, v41, v43
s_add_u32 s70, s70, s71
v_dot2_f32_bf16 v45, v27, v27, v45
v_dot2_f32_bf16 v42, v28, v41, v42
v_dot2_f32_bf16 v44, v28, v28, v44
v_dot2_f32_bf16 v43, v29, v41, v43
v_dot2_f32_bf16 v45, v29, v29, v45
v_dot2_f32_bf16 v42, v30, v41, v42
v_dot2_f32_bf16 v44, v30, v30, v44
v_dot2_f32_bf16 v43, v31, v41, v43
v_dot2_f32_bf16 v45, v31, v31, v45
s_lshl_b32 s71, s49, 12
v_dot2_f32_bf16 v42, v32, v41, v42
v_dot2_f32_bf16 v44, v32, v32, v44
v_dot2_f32_bf16 v43, v33, v41, v43
v_dot2_f32_bf16 v45, v33, v33, v45
s_nop 1
v_add_f32 v42, v42, v43
v_add_f32 v44, v44, v45
s_nop 0
v_add_f32_dpp v42, v42, v42 quad_perm:[1,0,3,2] row_mask:0xf bank_mask:0xf
v_add_f32_dpp v44, v44, v44 quad_perm:[1,0,3,2] row_mask:0xf bank_mask:0xf
s_mov_b32 m0, s71
v_add_f32_dpp v42, v42, v42 quad_perm:[2,3,0,1] row_mask:0xf bank_mask:0xf
v_add_f32_dpp v44, v44, v44 quad_perm:[2,3,0,1] row_mask:0xf bank_mask:0xf
s_nop 0
v_add_f32_dpp v42, v42, v42 row_half_mirror row_mask:0xf bank_mask:0xf
v_add_f32_dpp v44, v44, v44 row_half_mirror row_mask:0xf bank_mask:0xf
v_mul_f32 v42, 0xbb000000, v42
v_mul_f32 v44, 0x3b000000, v44
v_cvt_pk_bf16_f32 v43, v42, v42
v_readfirstlane_b32 s52, v35
v_and_b32 v46, s82, v43
buffer_load_dwordx4 v36, s[12:15], s70 offen offset:0 lds
v_dot2_f32_bf16 v66, v2, v82, v46
v_dot2_f32_bf16 v67, v2, v83, v46
v_fma_f32 v44, -v42, v42, v44
v_dot2_f32_bf16 v68, v3, v82, v46
v_dot2_f32_bf16 v69, v3, v83, v46
v_max_f32 v44, 0, v44
v_dot2_f32_bf16 v70, v4, v82, v46
v_dot2_f32_bf16 v71, v4, v83, v46
buffer_load_dwordx4 v37, s[12:15], s70 offen offset:1024 lds
v_cvt_pk_bf16_f32 v44, v44, v44
v_dot2_f32_bf16 v72, v5, v82, v46
v_dot2_f32_bf16 v73, v5, v83, v46
v_and_b32 v44, s82, v44
v_cvt_pk_bf16_f32 v2, v66, v67
v_cvt_pk_bf16_f32 v3, v68, v69
v_add_f32 v44, 0x3727c5ac, v44
v_cvt_pk_bf16_f32 v4, v70, v71
v_cvt_pk_bf16_f32 v5, v72, v73
buffer_load_dwordx4 v38, s[12:15], s70 offen offset:2048 lds
v_cvt_pk_bf16_f32 v44, v44, v44
ds_write_b128 v50, v[2:5] offset:0
v_dot2_f32_bf16 v74, v6, v82, v46
v_and_b32 v44, s82, v44
v_dot2_f32_bf16 v75, v6, v83, v46
v_dot2_f32_bf16 v76, v7, v82, v46
v_sqrt_f32 v44, v44
v_dot2_f32_bf16 v77, v7, v83, v46
buffer_load_dwordx4 v39, s[12:15], s70 offen offset:3072 lds
v_dot2_f32_bf16 v78, v8, v82, v46
s_add_u32 s52, s52, 1
v_dot2_f32_bf16 v79, v8, v83, v46
v_dot2_f32_bf16 v80, v9, v82, v46
s_xor_b32 s53, s52, 0x5bd1e995
v_dot2_f32_bf16 v81, v9, v83, v46
v_cvt_pk_bf16_f32 v6, v74, v75
v_cvt_pk_bf16_f32 v44, v44, v44
v_cvt_pk_bf16_f32 v7, v76, v77
s_add_u32 s70, s70, 0x10000
s_add_u32 s78, s78, s57
v_cvt_pk_bf16_f32 v8, v78, v79
s_add_u32 s78, s78, 0xa00000
v_and_b32 v44, s82, v44
s_lshl_b32 s86, s51, 2
v_cvt_pk_bf16_f32 v9, v80, v81
s_add_u32 s86, s86, s49
ds_write_b128 v50, v[6:9] offset:4096
s_add_u32 s71, s71, 0x4000
s_lshl_b32 s86, s86, 15
v_rcp_f32 v48, v44
s_lshl_b32 s54, s51, 13
v_dot2_f32_bf16 v66, v10, v82, v46
s_add_u32 s86, s86, s54
v_dot2_f32_bf16 v67, v10, v83, v46
s_add_u32 s86, s86, 0x100000
v_mul_f32 v48, v85, v48
s_lshl_b32 s88, s49, 13
s_mov_b32 m0, s71
v_dot2_f32_bf16 v68, v11, v82, v46
s_mul_i32 s87, s73, 229376
v_dot2_f32_bf16 v69, v11, v83, v46
s_add_u32 s87, s87, s88
ds_write_b32 v84, v48
s_add_u32 s87, s87, 0x600000
v_dot2_f32_bf16 v70, v12, v82, v46
s_mul_i32 s89, s75, 229376
buffer_load_dwordx4 v36, s[12:15], s70 offen offset:0 lds
v_dot2_f32_bf16 v71, v12, v83, v46
s_mul_i32 s54, s49, 57344
v_dot2_f32_bf16 v72, v13, v82, v46
s_add_u32 s89, s89, s54
v_dot2_f32_bf16 v73, v13, v83, v46
s_add_u32 s89, s89, 0xa00000
v_cvt_pk_bf16_f32 v10, v66, v67
s_lshr_b32 s54, s49, 1
buffer_load_dwordx4 v37, s[12:15], s70 offen offset:1024 lds
v_cvt_pk_bf16_f32 v11, v68, v69
s_lshl_b32 s54, s54, 10
v_cvt_pk_bf16_f32 v12, v70, v71
s_xor_b32 s55, s54, 1024
v_cvt_pk_bf16_f32 v13, v72, v73
s_add_u32 s90, s89, s55
ds_write_b128 v50, v[10:13] offset:8192
s_add_u32 s89, s89, s54
v_dot2_f32_bf16 v74, v14, v82, v46
buffer_load_dwordx4 v38, s[12:15], s70 offen offset:2048 lds
s_lshl_b32 s91, s50, 15
v_dot2_f32_bf16 v75, v14, v83, v46
s_lshl_b32 s54, s51, 13
v_dot2_f32_bf16 v76, v15, v82, v46
s_add_u32 s91, s91, s54
v_dot2_f32_bf16 v77, v15, v83, v46
s_lshl_b32 s54, s49, 11
v_dot2_f32_bf16 v78, v16, v82, v46
buffer_load_dwordx4 v39, s[12:15], s70 offen offset:3072 lds
s_add_u32 s91, s91, s54
v_dot2_f32_bf16 v79, v16, v83, v46
s_lshl_b32 s92, s72, 2
v_dot2_f32_bf16 v80, v17, v82, v46
s_add_u32 s92, s92, s49
v_dot2_f32_bf16 v81, v17, v83, v46
s_mul_i32 s92, s92, 114688
v_cvt_pk_bf16_f32 v14, v74, v75
s_mul_i32 s54, s73, 7168
s_add_u32 s70, s70, 0x10000
v_cvt_pk_bf16_f32 v15, v76, v77
s_add_u32 s92, s92, s54
v_cvt_pk_bf16_f32 v16, v78, v79
s_add_u32 s92, s92, 0xc00000
v_cvt_pk_bf16_f32 v17, v80, v81
s_mul_i32 s54, s73, 14336
ds_write_b128 v50, v[14:17] offset:12288
s_add_u32 s54, s54, 0x30000
s_add_u32 s71, s71, 0x4000
v_dot2_f32_bf16 v66, v18, v82, v46
s_lshl_b32 s55, s49, 7
v_dot2_f32_bf16 v67, v18, v83, v46
s_add_u32 s95, s55, s54
v_dot2_f32_bf16 v68, v19, v82, v46
s_add_u32 s55, s49, 4
v_dot2_f32_bf16 v69, v19, v83, v46
s_min_u32 s55, s55, 6
v_dot2_f32_bf16 v70, v20, v82, v46
s_mov_b32 m0, s71
s_lshl_b32 s55, s55, 7
v_dot2_f32_bf16 v71, v20, v83, v46
s_add_u32 s96, s55, s54
v_dot2_f32_bf16 v72, v21, v82, v46
s_lshl_b32 s97, s50, 15
v_dot2_f32_bf16 v73, v21, v83, v46
s_lshl_b32 s54, s51, 8
v_cvt_pk_bf16_f32 v18, v66, v67
buffer_load_dwordx4 v36, s[12:15], s70 offen offset:0 lds
s_add_u32 s97, s97, s54
v_cvt_pk_bf16_f32 v19, v68, v69
s_lshl_b32 s54, s49, 6
v_cvt_pk_bf16_f32 v20, v70, v71
s_add_u32 s97, s97, s54
v_cvt_pk_bf16_f32 v21, v72, v73
s_lshl_b32 s98, s48, 2
ds_write_b128 v50, v[18:21] offset:16384
s_add_u32 s98, s98, s49
buffer_load_dwordx4 v37, s[12:15], s70 offen offset:1024 lds
v_dot2_f32_bf16 v74, v22, v82, v46
s_lshl_b32 s98, s98, 3
v_dot2_f32_bf16 v75, v22, v83, v46
s_add_u32 s98, s98, 0x2000
v_dot2_f32_bf16 v76, v23, v82, v46
s_mul_i32 s99, s73, 229376
v_dot2_f32_bf16 v77, v23, v83, v46
s_and_b32 s54, s72, 1
buffer_load_dwordx4 v38, s[12:15], s70 offen offset:2048 lds
v_dot2_f32_bf16 v78, v24, v82, v46
s_lshl_b32 s54, s54, 14
v_dot2_f32_bf16 v79, v24, v83, v46
s_add_u32 s99, s99, s54
v_dot2_f32_bf16 v80, v25, v82, v46
s_lshl_b32 s54, s49, 12
v_dot2_f32_bf16 v81, v25, v83, v46
s_add_u32 s99, s99, s54
v_cvt_pk_bf16_f32 v22, v74, v75
buffer_load_dwordx4 v39, s[12:15], s70 offen offset:3072 lds
s_add_u32 s99, s99, 0x618000
v_cvt_pk_bf16_f32 v23, v76, v77
s_lshl_b32 s100, s49, 11
v_cvt_pk_bf16_f32 v24, v78, v79
s_add_u32 s100, s100, 0x2000
v_cvt_pk_bf16_f32 v25, v80, v81
ds_write_b128 v50, v[22:25] offset:20480
v_dot2_f32_bf16 v66, v26, v82, v46
s_add_u32 s70, s70, 0x10000
v_dot2_f32_bf16 v67, v26, v83, v46
v_dot2_f32_bf16 v68, v27, v82, v46
v_dot2_f32_bf16 v69, v27, v83, v46
v_dot2_f32_bf16 v70, v28, v82, v46
v_dot2_f32_bf16 v71, v28, v83, v46
v_dot2_f32_bf16 v72, v29, v82, v46
v_dot2_f32_bf16 v73, v29, v83, v46
v_cvt_pk_bf16_f32 v26, v66, v67
v_cvt_pk_bf16_f32 v27, v68, v69
s_add_u32 s71, s71, 0x4000
v_cvt_pk_bf16_f32 v28, v70, v71
v_cvt_pk_bf16_f32 v29, v72, v73
ds_write_b128 v50, v[26:29] offset:24576
v_dot2_f32_bf16 v74, v30, v82, v46
v_dot2_f32_bf16 v75, v30, v83, v46
v_dot2_f32_bf16 v76, v31, v82, v46
v_dot2_f32_bf16 v77, v31, v83, v46
v_dot2_f32_bf16 v78, v32, v82, v46
s_mov_b32 m0, s71
v_dot2_f32_bf16 v79, v32, v83, v46
v_dot2_f32_bf16 v80, v33, v82, v46
v_dot2_f32_bf16 v81, v33, v83, v46
v_cvt_pk_bf16_f32 v30, v74, v75
v_cvt_pk_bf16_f32 v31, v76, v77
v_cvt_pk_bf16_f32 v32, v78, v79
v_cvt_pk_bf16_f32 v33, v80, v81
ds_write_b128 v50, v[30:33] offset:28672
s_waitcnt vmcnt(12)
s_waitcnt lgkmcnt(0)
s_barrier
ds_read_b128 v[88:91], v86
ds_read_b32 a246, v87
ds_read_b64_tr_b16 v[2:3], v51 offset:0
ds_read_b64_tr_b16 v[4:5], v51 offset:128
ds_read_b128 v[18:21], v52 offset:0
ds_read_b64_tr_b16 v[6:7], v51 offset:1024
ds_read_b64_tr_b16 v[8:9], v51 offset:1152
ds_read_b128 v[22:25], v53 offset:0
ds_read_b64_tr_b16 v[10:11], v51 offset:2048
ds_read_b64_tr_b16 v[12:13], v51 offset:2176
ds_read_b128 v[26:29], v52 offset:4096
ds_read_b64_tr_b16 v[14:15], v51 offset:3072
ds_read_b64_tr_b16 v[16:17], v51 offset:3200
ds_read_b128 v[30:33], v53 offset:4096
ds_read_b128 v[128:131], v54 offset:0
ds_read_b128 v[132:135], v55 offset:0
ds_read_b128 v[136:139], v56 offset:0
ds_read_b128 v[140:143], v57 offset:0
s_cmp_lt_u32 s51, 2
s_cbranch_scc0 vchain_1
s_waitcnt lgkmcnt(13)
v_mfma_f32_16x16x32_bf16 v[40:43], v[2:5], v[18:21], 0
s_waitcnt lgkmcnt(10)
v_mfma_f32_16x16x32_bf16 v[40:43], v[6:9], v[22:25], v[40:43]
s_waitcnt lgkmcnt(7)
v_mfma_f32_16x16x32_bf16 v[40:43], v[10:13], v[26:29], v[40:43]
s_waitcnt lgkmcnt(4)
v_mfma_f32_16x16x32_bf16 v[40:43], v[14:17], v[30:33], v[40:43]
s_branch kvjoin_2
vchain_1:
s_waitcnt lgkmcnt(13)
v_mfma_f32_16x16x32_bf16 v[40:43], v[18:21], v[2:5], 0
s_waitcnt lgkmcnt(10)
v_mfma_f32_16x16x32_bf16 v[40:43], v[22:25], v[6:9], v[40:43]
s_waitcnt lgkmcnt(7)
v_mfma_f32_16x16x32_bf16 v[40:43], v[26:29], v[10:13], v[40:43]
s_waitcnt lgkmcnt(4)
v_mfma_f32_16x16x32_bf16 v[40:43], v[30:33], v[14:17], v[40:43]
kvjoin_2:
ds_read_b128 v[144:147], v54 offset:4096
ds_read_b128 v[148:151], v55 offset:4096
ds_read_b128 v[152:155], v56 offset:4096
ds_read_b128 v[156:159], v57 offset:4096
s_nop 3
ds_write_b128 v61, v[40:43]
s_waitcnt lgkmcnt(0)
s_barrier
s_cmp_eq_u32 s49, 0
s_cbranch_scc0 nored_3
ds_read_b128 v[18:21], v61 offset:4096
ds_read_b128 v[22:25], v61 offset:8192
ds_read_b128 v[26:29], v61 offset:12288
s_waitcnt lgkmcnt(2)
v_pk_add_f32 v[40:41], v[40:41], v[18:19]
s_waitcnt lgkmcnt(0)
v_pk_add_f32 v[22:23], v[22:23], v[26:27]
v_pk_add_f32 v[42:43], v[42:43], v[20:21]
v_pk_add_f32 v[24:25], v[24:25], v[28:29]
v_pk_add_f32 v[40:41], v[40:41], v[22:23]
v_pk_add_f32 v[42:43], v[42:43], v[24:25]
v_mul_f32 v40, v88, v40
v_mul_f32 v41, v89, v41
v_mul_f32 v42, v90, v42
v_mul_f32 v43, v91, v43
v_cvt_pk_bf16_f32 v44, v40, v41
v_cvt_pk_bf16_f32 v45, v42, v43
buffer_store_dwordx2 v[44:45], v60, s[44:47], s83 offen sc1
v_mov_b32 v6, s52
v_mov_b32 v7, s53
s_xor_b32 s54, s52, 0x9e3779b9
v_mov_b32 v8, 0
v_mov_b32 v9, s54
s_cmp_lt_u32 s51, 2
s_cbranch_scc0 nored_3
v_mul_f32 v46, v40, v40
v_fmac_f32 v46, v41, v41
v_fmac_f32 v46, v42, v42
v_fmac_f32 v46, v43, v43
v_mov_b32 v47, v46
s_nop 1
v_permlane16_swap_b32 v46, v47
v_add_f32 v46, v46, v47
v_mov_b32 v47, v46
s_nop 1
v_permlane32_swap_b32 v46, v47
v_add_f32 v46, v46, v47
s_nop 1
v_max_f32_dpp v46, v46, v46 row_ror:8 row_mask:0xf bank_mask:0xf
s_nop 1
v_max_f32_dpp v46, v46, v46 row_ror:4 row_mask:0xf bank_mask:0xf
s_nop 1
v_max_f32_dpp v46, v46, v46 row_ror:2 row_mask:0xf bank_mask:0xf
s_nop 1
v_max_f32_dpp v8, v46, v46 row_ror:1 row_mask:0xf bank_mask:0xf
nored_3:
buffer_load_dwordx4 v36, s[12:15], s70 offen offset:0 lds
buffer_load_dwordx4 v37, s[12:15], s70 offen offset:1024 lds
buffer_load_dwordx4 v38, s[12:15], s70 offen offset:2048 lds
buffer_load_dwordx4 v39, s[12:15], s70 offen offset:3072 lds
s_add_u32 s70, s70, 0x10000
s_add_u32 s71, s71, 0x4000
s_mov_b32 m0, s71
s_nop 0
buffer_load_dwordx4 v36, s[12:15], s70 offen offset:0 lds
buffer_load_dwordx4 v37, s[12:15], s70 offen offset:1024 lds
buffer_load_dwordx4 v38, s[12:15], s70 offen offset:2048 lds
buffer_load_dwordx4 v39, s[12:15], s70 offen offset:3072 lds
s_add_u32 s70, s70, 0x10000
s_add_u32 s71, s71, 0x4000
s_mov_b32 m0, s71
s_nop 0
buffer_load_dwordx4 v36, s[12:15], s70 offen offset:0 lds
buffer_load_dwordx4 v37, s[12:15], s70 offen offset:1024 lds
buffer_load_dwordx4 v38, s[12:15], s70 offen offset:2048 lds
buffer_load_dwordx4 v39, s[12:15], s70 offen offset:3072 lds
s_add_u32 s70, s70, 0x10000
s_add_u32 s71, s71, 0x4000
s_mov_b32 m0, s71
s_cmp_eq_u32 s49, 0
s_cbranch_scc0 nopub_4
s_lshl_b32 s54, s50, 2
s_add_u32 s54, s54, s51
s_lshl_b32 s54, s54, 7
s_add_u32 s54, s54, 0xc0000
s_waitcnt vmcnt(12)
s_mov_b64 exec, 1
buffer_store_dwordx4 v[6:9], off, s[44:47], s54 sc1
s_mov_b64 exec, -1
nopub_4:
s_waitcnt vmcnt(16)
s_barrier
ds_read_b64_tr_b16 v[64:65], v58 offset:0
ds_read_b64_tr_b16 v[66:67], v58 offset:1024
ds_read_b128 v[160:163], v54 offset:8192
buffer_load_dwordx4 v36, s[12:15], s70 offen offset:0 lds
buffer_load_dwordx4 v37, s[12:15], s70 offen offset:1024 lds
buffer_load_dwordx4 v38, s[12:15], s70 offen offset:2048 lds
ds_read_b64_tr_b16 v[68:69], v58 offset:4096
ds_read_b64_tr_b16 v[70:71], v58 offset:5120
ds_read_b128 v[164:167], v55 offset:8192
buffer_load_dwordx4 v39, s[12:15], s70 offen offset:3072 lds
s_add_u32 s70, s70, 0x10000
ds_read_b64_tr_b16 v[72:73], v58 offset:8192
ds_read_b64_tr_b16 v[74:75], v58 offset:9216
ds_read_b128 v[168:171], v56 offset:8192
buffer_load_dwordx4 a[0:3], v36, s[12:15], s70 offen offset:0
buffer_load_dwordx4 a[4:7], v37, s[12:15], s70 offen offset:1024
ds_read_b64_tr_b16 v[76:77], v58 offset:12288
ds_read_b64_tr_b16 v[78:79], v58 offset:13312
ds_read_b128 v[172:175], v57 offset:8192
buffer_load_dwordx4 a[8:11], v38, s[12:15], s70 offen offset:2048
buffer_load_dwordx4 a[12:15], v39, s[12:15], s70 offen offset:3072
ds_read_b64_tr_b16 v[80:81], v58 offset:16384
ds_read_b64_tr_b16 v[82:83], v58 offset:17408
ds_read_b128 v[176:179], v54 offset:12288
s_waitcnt lgkmcnt(13)
v_mfma_f32_32x32x16_bf16 v[34:49], v[64:67], v[128:131], 0
ds_read_b64_tr_b16 v[84:85], v58 offset:20480
ds_read_b64_tr_b16 v[86:87], v58 offset:21504
ds_read_b128 v[180:183], v55 offset:12288
s_waitcnt lgkmcnt(13)
v_mfma_f32_32x32x16_bf16 v[34:49], v[68:71], v[132:135], v[34:49]
ds_read_b64_tr_b16 v[88:89], v58 offset:24576
ds_read_b64_tr_b16 v[90:91], v58 offset:25600
ds_read_b128 v[184:187], v56 offset:12288
s_waitcnt lgkmcnt(13)
v_mfma_f32_32x32x16_bf16 v[34:49], v[72:75], v[136:139], v[34:49]
ds_read_b64_tr_b16 v[92:93], v58 offset:28672
ds_read_b64_tr_b16 v[94:95], v58 offset:29696
ds_read_b128 v[188:191], v57 offset:12288
s_waitcnt lgkmcnt(13)
v_mfma_f32_32x32x16_bf16 v[34:49], v[76:79], v[140:143], v[34:49]
s_waitcnt vmcnt(16)
s_barrier
ds_read_b64_tr_b16 v[96:97], v58 offset:32768
ds_read_b64_tr_b16 v[98:99], v58 offset:33792
ds_read_b128 v[192:195], v54 offset:16384
s_waitcnt lgkmcnt(13)
v_mfma_f32_32x32x16_bf16 v[34:49], v[80:83], v[144:147], v[34:49]
s_lshl_b32 s60, s49, 14
s_add_u32 s64, s60, 0x20000
ds_read_b64_tr_b16 v[100:101], v58 offset:36864
ds_read_b64_tr_b16 v[102:103], v58 offset:37888
ds_read_b128 v[196:199], v55 offset:16384
s_waitcnt lgkmcnt(13)
v_mfma_f32_32x32x16_bf16 v[34:49], v[84:87], v[148:151], v[34:49]
s_add_u32 s60, s60, 0x10000
ds_read_b64_tr_b16 v[104:105], v58 offset:40960
ds_read_b64_tr_b16 v[106:107], v58 offset:41984
ds_read_b128 v[200:203], v56 offset:16384
s_waitcnt lgkmcnt(13)
v_mfma_f32_32x32x16_bf16 v[34:49], v[88:91], v[152:155], v[34:49]
s_add_u32 s61, s60, 0x1000
ds_read_b64_tr_b16 v[108:109], v58 offset:45056
ds_read_b64_tr_b16 v[110:111], v58 offset:46080
ds_read_b128 v[204:207], v57 offset:16384
s_waitcnt lgkmcnt(13)
v_mfma_f32_32x32x16_bf16 v[34:49], v[92:95], v[156:159], v[34:49]
s_add_u32 s65, s64, 0x1000
ds_read_b64_tr_b16 v[112:113], v58 offset:49152
ds_read_b64_tr_b16 v[114:115], v58 offset:50176
ds_read_b128 v[208:211], v54 offset:20480
s_waitcnt lgkmcnt(13)
v_mfma_f32_32x32x16_bf16 v[34:49], v[96:99], v[160:163], v[34:49]
s_add_u32 s62, s60, 0x2000
ds_read_b64_tr_b16 v[116:117], v58 offset:53248
ds_read_b64_tr_b16 v[118:119], v58 offset:54272
ds_read_b128 v[212:215], v55 offset:20480
s_waitcnt lgkmcnt(13)
v_mfma_f32_32x32x16_bf16 v[34:49], v[100:103], v[164:167], v[34:49]
s_add_u32 s66, s64, 0x2000
ds_read_b64_tr_b16 v[120:121], v58 offset:57344
ds_read_b64_tr_b16 v[122:123], v58 offset:58368
ds_read_b128 v[216:219], v56 offset:20480
s_waitcnt lgkmcnt(13)
v_mfma_f32_32x32x16_bf16 v[34:49], v[104:107], v[168:171], v[34:49]
s_add_u32 s63, s60, 0x3000
ds_read_b64_tr_b16 v[124:125], v58 offset:61440
ds_read_b64_tr_b16 v[126:127], v58 offset:62464
ds_read_b128 v[220:223], v57 offset:20480
s_waitcnt lgkmcnt(13)
v_mfma_f32_32x32x16_bf16 v[34:49], v[108:111], v[172:175], v[34:49]
s_add_u32 s67, s64, 0x3000
s_mov_b32 s70, 0xffff
s_waitcnt vmcnt(8)
s_barrier
ds_read_b64_tr_b16 v[64:65], v59 offset:0
ds_read_b64_tr_b16 v[66:67], v59 offset:1024
ds_read_b128 v[224:227], v54 offset:24576
s_waitcnt lgkmcnt(13)
v_mfma_f32_32x32x16_bf16 v[34:49], v[112:115], v[176:179], v[34:49]
s_mov_b32 s71, 0xffff
ds_read_b64_tr_b16 v[68:69], v59 offset:4096
ds_read_b64_tr_b16 v[70:71], v59 offset:5120
ds_read_b128 v[228:231], v55 offset:24576
s_waitcnt lgkmcnt(13)
v_mfma_f32_32x32x16_bf16 v[34:49], v[116:119], v[180:183], v[34:49]
v_lshrrev_b32 v31, 5, v0
ds_read_b64_tr_b16 v[72:73], v59 offset:8192
ds_read_b64_tr_b16 v[74:75], v59 offset:9216
ds_read_b128 v[232:235], v56 offset:24576
s_waitcnt lgkmcnt(13)
v_mfma_f32_32x32x16_bf16 v[34:49], v[120:123], v[184:187], v[34:49]
v_lshlrev_b32 v31, 4, v31
ds_read_b64_tr_b16 v[76:77], v59 offset:12288
ds_read_b64_tr_b16 v[78:79], v59 offset:13312
ds_read_b128 v[236:239], v57 offset:24576
s_waitcnt lgkmcnt(13)
v_mfma_f32_32x32x16_bf16 v[34:49], v[124:127], v[188:191], v[34:49]
v_and_or_b32 v31, v0, 15, v31
ds_read_b64_tr_b16 v[80:81], v59 offset:16384
ds_read_b64_tr_b16 v[82:83], v59 offset:17408
ds_read_b128 v[240:243], v54 offset:28672
s_waitcnt lgkmcnt(13)
v_mfma_f32_32x32x16_bf16 v[34:49], v[64:67], v[192:195], v[34:49]
v_lshlrev_b32 v31, 4, v31
ds_read_b64_tr_b16 v[84:85], v59 offset:20480
ds_read_b64_tr_b16 v[86:87], v59 offset:21504
ds_read_b128 v[244:247], v55 offset:28672
s_waitcnt lgkmcnt(13)
v_mfma_f32_32x32x16_bf16 v[34:49], v[68:71], v[196:199], v[34:49]
v_lshlrev_b32 v30, 7, v0
s_lshl_b32 s54, s49, 13
s_add_u32 s54, s54, 0xc0000
buffer_load_dwordx4 v[26:29], v30, s[44:47], s54 offen sc1
ds_read_b64_tr_b16 v[88:89], v59 offset:24576
ds_read_b64_tr_b16 v[90:91], v59 offset:25600
ds_read_b128 v[248:251], v56 offset:28672
s_waitcnt lgkmcnt(13)
v_mfma_f32_32x32x16_bf16 v[34:49], v[72:75], v[200:203], v[34:49]
ds_read_b64_tr_b16 v[92:93], v59 offset:28672
ds_read_b64_tr_b16 v[94:95], v59 offset:29696
ds_read_b128 v[252:255], v57 offset:28672
s_waitcnt lgkmcnt(13)
v_mfma_f32_32x32x16_bf16 v[34:49], v[76:79], v[204:207], v[34:49]
s_waitcnt vmcnt(4)
ds_write_b128 v61, a[0:3] offset:0
s_waitcnt vmcnt(3)
ds_write_b128 v61, a[4:7] offset:1024
s_waitcnt vmcnt(2)
ds_write_b128 v61, a[8:11] offset:2048
s_waitcnt vmcnt(1)
ds_write_b128 v61, a[12:15] offset:3072
s_waitcnt lgkmcnt(0)
s_barrier
ds_read_b64_tr_b16 v[96:97], v59 offset:32768
ds_read_b64_tr_b16 v[98:99], v59 offset:33792
v_mfma_f32_32x32x16_bf16 v[34:49], v[80:83], v[208:211], v[34:49]
ds_read_b64_tr_b16 v[100:101], v59 offset:36864
ds_read_b64_tr_b16 v[102:103], v59 offset:37888
v_mfma_f32_32x32x16_bf16 v[34:49], v[84:87], v[212:215], v[34:49]
ds_read_b64_tr_b16 v[104:105], v59 offset:40960
ds_read_b64_tr_b16 v[106:107], v59 offset:41984
v_mfma_f32_32x32x16_bf16 v[34:49], v[88:91], v[216:219], v[34:49]
ds_read_b64_tr_b16 v[108:109], v59 offset:45056
ds_read_b64_tr_b16 v[110:111], v59 offset:46080
v_mfma_f32_32x32x16_bf16 v[34:49], v[92:95], v[220:223], v[34:49]
ds_read_b64_tr_b16 v[112:113], v59 offset:49152
ds_read_b64_tr_b16 v[114:115], v59 offset:50176
s_waitcnt lgkmcnt(8)
v_mfma_f32_32x32x16_bf16 v[34:49], v[96:99], v[224:227], v[34:49]
ds_read_b64_tr_b16 v[116:117], v59 offset:53248
ds_read_b64_tr_b16 v[118:119], v59 offset:54272
s_waitcnt lgkmcnt(8)
v_mfma_f32_32x32x16_bf16 v[34:49], v[100:103], v[228:231], v[34:49]
ds_read_b64_tr_b16 v[120:121], v59 offset:57344
ds_read_b64_tr_b16 v[122:123], v59 offset:58368
s_waitcnt lgkmcnt(8)
v_mfma_f32_32x32x16_bf16 v[34:49], v[104:107], v[232:235], v[34:49]
ds_read_b64_tr_b16 v[124:125], v59 offset:61440
ds_read_b64_tr_b16 v[126:127], v59 offset:62464
s_waitcnt lgkmcnt(8)
v_mfma_f32_32x32x16_bf16 v[34:49], v[108:111], v[236:239], v[34:49]
s_waitcnt lgkmcnt(6)
v_mfma_f32_32x32x16_bf16 v[34:49], v[112:115], v[240:243], v[34:49]
s_waitcnt lgkmcnt(4)
v_mfma_f32_32x32x16_bf16 v[34:49], v[116:119], v[244:247], v[34:49]
s_waitcnt lgkmcnt(2)
v_mfma_f32_32x32x16_bf16 v[34:49], v[120:123], v[248:251], v[34:49]
s_waitcnt lgkmcnt(0)
v_mfma_f32_32x32x16_bf16 v[34:49], v[124:127], v[252:255], v[34:49]
s_xor_b32 s57, s52, 0x9e3779b9
s_waitcnt vmcnt(0)
v_cmp_eq_u32 vcc, s52, v26
v_cmp_eq_u32 s[58:59], s53, v27
s_and_b64 s[58:59], vcc, s[58:59]
v_cmp_eq_u32 vcc, s57, v29
s_and_b64 vcc, vcc, s[58:59]
s_cmp_eq_u64 vcc, -1
s_cbranch_scc0 kvlate_5
v_mov_b32 v62, 0
buffer_load_dwordx4 a[0:3], v1, s[44:47], s60 offen offset:0 sc1
v_mov_b32 v63, 0
v_mov_b32 v64, 0
v_mov_b32 v65, 0
v_mov_b32 v66, 0
v_mov_b32 v67, 0
buffer_load_dwordx4 a[4:7], v1, s[44:47], s60 offen offset:1024 sc1
v_mov_b32 v68, 0
v_mov_b32 v69, 0
v_mov_b32 v70, 0
v_mov_b32 v71, 0
v_mov_b32 v72, 0
buffer_load_dwordx4 a[8:11], v1, s[44:47], s60 offen offset:2048 sc1
v_mov_b32 v73, 0
v_mov_b32 v74, 0
v_mov_b32 v75, 0
v_mov_b32 v76, 0
v_mov_b32 v77, 0
buffer_load_dwordx4 a[12:15], v1, s[44:47], s60 offen offset:3072 sc1
v_lshrrev_b32 v53, 5, v0
s_lshl_b32 s54, s49, 3
s_add_u32 s54, s54, 0x24000
v_lshl_add_u32 v53, v53, 2, s54
s_lshl_b32 s54, s49, 11
s_mov_b64 exec, s[70:71]
buffer_load_dwordx4 a[64:67], v31, s[44:47], s64 offen offset:0 sc1
buffer_load_dwordx4 a[68:71], v31, s[44:47], s64 offen offset:512 sc1
s_mov_b64 exec, -1
s_add_u32 s54, s54, 0x22000
v_add_u32 v52, s54, v1
v_lshrrev_b32 v83, 5, v0
v_lshlrev_b32 v83, 4, v83
v_and_or_b32 v83, v0, 15, v83
s_mov_b64 exec, s[70:71]
buffer_load_dwordx4 a[72:75], v31, s[44:47], s64 offen offset:1024 sc1
buffer_load_dwordx4 a[76:79], v31, s[44:47], s64 offen offset:1536 sc1
s_mov_b64 exec, -1
v_lshlrev_b32 v83, 4, v83
v_add_u32 v78, 0x22000, v1
s_mul_i32 s54, s49, 2304
s_add_u32 s54, s54, 0x10000
v_add_u32 v79, s54, v1
s_mov_b64 exec, s[70:71]
buffer_load_dwordx4 a[80:83], v31, s[44:47], s64 offen offset:2048 sc1
buffer_load_dwordx4 a[84:87], v31, s[44:47], s64 offen offset:2560 sc1
s_mov_b64 exec, -1
v_lshlrev_b32 v80, 2, v0
s_add_u32 s54, s54, 2048
v_add_u32 v80, s54, v80
v_max_f32_dpp v28, v28, v28 row_ror:8 row_mask:0xf bank_mask:0xf
s_nop 1
v_max_f32_dpp v28, v28, v28 row_ror:4 row_mask:0xf bank_mask:0xf
s_mov_b64 exec, s[70:71]
buffer_load_dwordx4 a[88:91], v31, s[44:47], s64 offen offset:3072 sc1
buffer_load_dwordx4 a[92:95], v31, s[44:47], s64 offen offset:3584 sc1
s_mov_b64 exec, -1
v_max_f32_dpp v28, v28, v28 row_ror:2 row_mask:0xf bank_mask:0xf
s_nop 1
v_max_f32_dpp v28, v28, v28 row_ror:1 row_mask:0xf bank_mask:0xf
v_mov_b32 v82, v28
s_nop 1
v_permlane16_swap_b32 v28, v82
v_max_f32 v28, v28, v82
v_mov_b32 v82, v28
s_nop 1
v_permlane32_swap_b32 v28, v82
v_max_f32 v28, v28, v82
s_lshl_b32 s54, s49, 2
s_add_u32 s54, s54, 0x24020
v_mov_b32 v82, s54
ds_write_b32 v82, v28
v_accvgpr_read_b32 v50, a246
v_mul_f32 v34, v50, v34
v_mul_f32 v35, v50, v35
v_cvt_pk_bf16_f32 v18, v34, v35
v_mul_f32 v36, v50, v36
v_mul_f32 v37, v50, v37
v_cvt_pk_bf16_f32 v19, v36, v37
v_mul_f32 v38, v50, v38
v_mul_f32 v39, v50, v39
v_cvt_pk_bf16_f32 v20, v38, v39
v_mul_f32 v40, v50, v40
v_mul_f32 v41, v50, v41
v_cvt_pk_bf16_f32 v21, v40, v41
v_mul_f32 v42, v50, v42
v_mul_f32 v43, v50, v43
v_cvt_pk_bf16_f32 v22, v42, v43
v_mul_f32 v44, v50, v44
v_mul_f32 v45, v50, v45
v_cvt_pk_bf16_f32 v23, v44, v45
v_mul_f32 v46, v50, v46
v_mul_f32 v47, v50, v47
v_cvt_pk_bf16_f32 v24, v46, v47
v_mul_f32 v48, v50, v48
v_mul_f32 v49, v50, v49
v_cvt_pk_bf16_f32 v25, v48, v49
ds_write_b128 v52, v[18:21]
ds_write_b128 v52, v[22:25] offset:1024
v_mov_b32 v50, 0
v_mov_b32 v51, 0
v_dot2_f32_bf16 v50, v18, v18, v50
v_dot2_f32_bf16 v51, v22, v22, v51
v_dot2_f32_bf16 v50, v19, v19, v50
v_dot2_f32_bf16 v51, v23, v23, v51
v_dot2_f32_bf16 v50, v20, v20, v50
v_dot2_f32_bf16 v51, v24, v24, v51
v_dot2_f32_bf16 v50, v21, v21, v50
v_dot2_f32_bf16 v51, v25, v25, v51
s_nop 2
v_permlane32_swap_b32 v50, v51
v_add_f32 v50, v50, v51
s_nop 1
v_max_f32_dpp v50, v50, v50 row_ror:8 row_mask:0xf bank_mask:0xf
s_nop 1
v_max_f32_dpp v50, v50, v50 row_ror:4 row_mask:0xf bank_mask:0xf
s_nop 1
v_max_f32_dpp v50, v50, v50 row_ror:2 row_mask:0xf bank_mask:0xf
s_nop 1
v_max_f32_dpp v50, v50, v50 row_ror:1 row_mask:0xf bank_mask:0xf
v_mov_b32 v51, v50
s_nop 1
v_permlane16_swap_b32 v50, v51
v_max_f32 v50, v50, v51
ds_write_b32 v53, v50
kvjoined_6:
s_waitcnt lgkmcnt(0)
s_barrier
v_mov_b32 v82, 0x24000
ds_read_b128 v[84:87], v82
ds_read_b128 v[88:91], v82 offset:16
ds_read_b128 v[92:95], v82 offset:32
ds_read_b128 v[58:61], v78
s_waitcnt lgkmcnt(3)
v_max3_f32 v84, v84, v85, v86
s_waitcnt lgkmcnt(2)
v_max3_f32 v88, v88, v89, v90
s_waitcnt lgkmcnt(1)
v_max3_f32 v92, v92, v93, v94
v_max3_f32 v84, v84, v87, v88
v_max_f32 v92, v92, v95
v_max_f32 v84, v84, v91
v_mul_f32 v84, v84, v92
v_mov_b32 v85, 0x45610000
v_cmp_le_f32 vcc, v84, v85
s_cmp_eq_u64 vcc, -1
s_cselect_b32 s69, 0, 1
s_cmp_lg_u32 s69, 0
s_cbranch_scc1 slowpre_13
slowback_14:
s_waitcnt vmcnt(11) lgkmcnt(0)
v_mfma_f32_32x32x16_bf16 v[2:17], a[0:3], v[58:61], v[62:77]
s_nop 7
s_nop 3
v_exp_f32 v2, v2
buffer_load_dwordx4 a[16:19], v1, s[44:47], s61 offen offset:0 sc1
v_exp_f32 v3, v3
v_exp_f32 v4, v4
v_cvt_pk_bf16_f32 v34, v2, v3
v_exp_f32 v5, v5
v_exp_f32 v6, v6
v_cvt_pk_bf16_f32 v35, v4, v5
v_exp_f32 v7, v7
v_exp_f32 v8, v8
v_cvt_pk_bf16_f32 v36, v6, v7
s_waitcnt vmcnt(11)
v_mfma_f32_32x32x16_bf16 v[18:33], a[4:7], v[58:61], v[62:77]
v_exp_f32 v9, v9
v_exp_f32 v10, v10
v_cvt_pk_bf16_f32 v37, v8, v9
v_exp_f32 v11, v11
v_exp_f32 v12, v12
v_cvt_pk_bf16_f32 v38, v10, v11
buffer_load_dwordx4 a[20:23], v1, s[44:47], s61 offen offset:1024 sc1
s_waitcnt vmcnt(9)
v_mfma_f32_32x32x16_bf16 v[42:57], a[64:67], v[34:37], 0
v_exp_f32 v13, v13
v_exp_f32 v14, v14
v_cvt_pk_bf16_f32 v39, v12, v13
v_exp_f32 v15, v15
v_exp_f32 v16, v16
v_cvt_pk_bf16_f32 v40, v14, v15
v_exp_f32 v17, v17
v_exp_f32 v18, v18
v_cvt_pk_bf16_f32 v41, v16, v17
v_exp_f32 v19, v19
v_exp_f32 v20, v20
s_waitcnt vmcnt(8)
v_mfma_f32_32x32x16_bf16 v[42:57], a[68:71], v[38:41], v[42:57]
v_cvt_pk_bf16_f32 v34, v18, v19
v_exp_f32 v21, v21
v_exp_f32 v22, v22
v_cvt_pk_bf16_f32 v35, v20, v21
buffer_load_dwordx4 a[24:27], v1, s[44:47], s61 offen offset:2048 sc1
v_exp_f32 v23, v23
v_exp_f32 v24, v24
v_cvt_pk_bf16_f32 v36, v22, v23
v_mfma_f32_32x32x16_bf16 v[2:17], a[8:11], v[58:61], v[62:77]
v_exp_f32 v25, v25
v_exp_f32 v26, v26
v_cvt_pk_bf16_f32 v37, v24, v25
v_exp_f32 v27, v27
v_exp_f32 v28, v28
v_cvt_pk_bf16_f32 v38, v26, v27
s_waitcnt vmcnt(8)
v_mfma_f32_32x32x16_bf16 v[42:57], a[72:75], v[34:37], v[42:57]
v_exp_f32 v29, v29
v_exp_f32 v30, v30
v_cvt_pk_bf16_f32 v39, v28, v29
v_exp_f32 v31, v31
v_exp_f32 v32, v32
v_cvt_pk_bf16_f32 v40, v30, v31
buffer_load_dwordx4 a[28:31], v1, s[44:47], s61 offen offset:3072 sc1
v_exp_f32 v33, v33
v_exp_f32 v2, v2
v_cvt_pk_bf16_f32 v41, v32, v33
v_exp_f32 v3, v3
v_exp_f32 v4, v4
s_waitcnt vmcnt(8)
v_mfma_f32_32x32x16_bf16 v[42:57], a[76:79], v[38:41], v[42:57]
v_cvt_pk_bf16_f32 v34, v2, v3
v_exp_f32 v5, v5
v_exp_f32 v6, v6
v_cvt_pk_bf16_f32 v35, v4, v5
v_exp_f32 v7, v7
v_exp_f32 v8, v8
v_cvt_pk_bf16_f32 v36, v6, v7
v_mfma_f32_32x32x16_bf16 v[18:33], a[12:15], v[58:61], v[62:77]
v_exp_f32 v9, v9
v_exp_f32 v10, v10
v_cvt_pk_bf16_f32 v37, v8, v9
s_mov_b64 exec, s[70:71]
buffer_load_dwordx4 a[96:99], v83, s[44:47], s65 offen offset:0 sc1
buffer_load_dwordx4 a[100:103], v83, s[44:47], s65 offen offset:512 sc1
buffer_load_dwordx4 a[104:107], v83, s[44:47], s65 offen offset:1024 sc1
buffer_load_dwordx4 a[108:111], v83, s[44:47], s65 offen offset:1536 sc1
s_mov_b64 exec, -1
v_exp_f32 v11, v11
v_exp_f32 v12, v12
v_cvt_pk_bf16_f32 v38, v10, v11
s_waitcnt vmcnt(11)
v_mfma_f32_32x32x16_bf16 v[42:57], a[80:83], v[34:37], v[42:57]
v_exp_f32 v13, v13
v_exp_f32 v14, v14
v_cvt_pk_bf16_f32 v39, v12, v13
v_exp_f32 v15, v15
v_exp_f32 v16, v16
v_cvt_pk_bf16_f32 v40, v14, v15
v_exp_f32 v17, v17
v_exp_f32 v18, v18
v_cvt_pk_bf16_f32 v41, v16, v17
v_exp_f32 v19, v19
v_exp_f32 v20, v20
s_waitcnt vmcnt(10)
v_mfma_f32_32x32x16_bf16 v[42:57], a[84:87], v[38:41], v[42:57]
v_cvt_pk_bf16_f32 v34, v18, v19
s_mov_b64 exec, s[70:71]
buffer_load_dwordx4 a[112:115], v83, s[44:47], s65 offen offset:2048 sc1
buffer_load_dwordx4 a[116:119], v83, s[44:47], s65 offen offset:2560 sc1
buffer_load_dwordx4 a[120:123], v83, s[44:47], s65 offen offset:3072 sc1
buffer_load_dwordx4 a[124:127], v83, s[44:47], s65 offen offset:3584 sc1
s_mov_b64 exec, -1
v_exp_f32 v21, v21
v_exp_f32 v22, v22
v_cvt_pk_bf16_f32 v35, v20, v21
v_exp_f32 v23, v23
v_exp_f32 v24, v24
v_cvt_pk_bf16_f32 v36, v22, v23
s_waitcnt vmcnt(11)
v_mfma_f32_32x32x16_bf16 v[2:17], a[16:19], v[58:61], v[62:77]
v_exp_f32 v25, v25
v_exp_f32 v26, v26
v_cvt_pk_bf16_f32 v37, v24, v25
v_exp_f32 v27, v27
v_exp_f32 v28, v28
v_cvt_pk_bf16_f32 v38, v26, v27
v_mfma_f32_32x32x16_bf16 v[42:57], a[88:91], v[34:37], v[42:57]
v_exp_f32 v29, v29
v_exp_f32 v30, v30
v_cvt_pk_bf16_f32 v39, v28, v29
buffer_load_dwordx4 a[32:35], v1, s[44:47], s62 offen offset:0 sc1
v_exp_f32 v31, v31
v_exp_f32 v32, v32
v_cvt_pk_bf16_f32 v40, v30, v31
v_exp_f32 v33, v33
v_exp_f32 v2, v2
v_cvt_pk_bf16_f32 v41, v32, v33
v_exp_f32 v3, v3
v_exp_f32 v4, v4
v_mfma_f32_32x32x16_bf16 v[42:57], a[92:95], v[38:41], v[42:57]
v_cvt_pk_bf16_f32 v34, v2, v3
v_exp_f32 v5, v5
v_exp_f32 v6, v6
v_cvt_pk_bf16_f32 v35, v4, v5
v_exp_f32 v7, v7
v_exp_f32 v8, v8
v_cvt_pk_bf16_f32 v36, v6, v7
buffer_load_dwordx4 a[36:39], v1, s[44:47], s62 offen offset:1024 sc1
s_waitcnt vmcnt(12)
v_mfma_f32_32x32x16_bf16 v[18:33], a[20:23], v[58:61], v[62:77]
v_exp_f32 v9, v9
v_exp_f32 v10, v10
v_cvt_pk_bf16_f32 v37, v8, v9
v_exp_f32 v11, v11
v_exp_f32 v12, v12
v_cvt_pk_bf16_f32 v38, v10, v11
s_waitcnt vmcnt(9)
v_mfma_f32_32x32x16_bf16 v[42:57], a[96:99], v[34:37], v[42:57]
v_exp_f32 v13, v13
v_exp_f32 v14, v14
v_cvt_pk_bf16_f32 v39, v12, v13
v_exp_f32 v15, v15
v_exp_f32 v16, v16
v_cvt_pk_bf16_f32 v40, v14, v15
v_exp_f32 v17, v17
v_exp_f32 v18, v18
v_cvt_pk_bf16_f32 v41, v16, v17
buffer_load_dwordx4 a[40:43], v1, s[44:47], s62 offen offset:2048 sc1
v_exp_f32 v19, v19
v_exp_f32 v20, v20
s_waitcnt vmcnt(9)
v_mfma_f32_32x32x16_bf16 v[42:57], a[100:103], v[38:41], v[42:57]
v_cvt_pk_bf16_f32 v34, v18, v19
v_exp_f32 v21, v21
v_exp_f32 v22, v22
v_cvt_pk_bf16_f32 v35, v20, v21
v_exp_f32 v23, v23
v_exp_f32 v24, v24
v_cvt_pk_bf16_f32 v36, v22, v23
v_mfma_f32_32x32x16_bf16 v[2:17], a[24:27], v[58:61], v[62:77]
v_exp_f32 v25, v25
v_exp_f32 v26, v26
v_cvt_pk_bf16_f32 v37, v24, v25
v_exp_f32 v27, v27
v_exp_f32 v28, v28
v_cvt_pk_bf16_f32 v38, v26, v27
buffer_load_dwordx4 a[44:47], v1, s[44:47], s62 offen offset:3072 sc1
s_waitcnt vmcnt(9)
v_mfma_f32_32x32x16_bf16 v[42:57], a[104:107], v[34:37], v[42:57]
v_exp_f32 v29, v29
v_exp_f32 v30, v30
v_cvt_pk_bf16_f32 v39, v28, v29
v_exp_f32 v31, v31
v_exp_f32 v32, v32
v_cvt_pk_bf16_f32 v40, v30, v31
v_exp_f32 v33, v33
v_exp_f32 v2, v2
v_cvt_pk_bf16_f32 v41, v32, v33
v_exp_f32 v3, v3
v_exp_f32 v4, v4
s_waitcnt vmcnt(8)
v_mfma_f32_32x32x16_bf16 v[42:57], a[108:111], v[38:41], v[42:57]
v_cvt_pk_bf16_f32 v34, v2, v3
v_exp_f32 v5, v5
v_exp_f32 v6, v6
v_cvt_pk_bf16_f32 v35, v4, v5
s_mov_b64 exec, s[70:71]
buffer_load_dwordx4 a[128:131], v83, s[44:47], s66 offen offset:0 sc1
buffer_load_dwordx4 a[132:135], v83, s[44:47], s66 offen offset:512 sc1
buffer_load_dwordx4 a[136:139], v83, s[44:47], s66 offen offset:1024 sc1
buffer_load_dwordx4 a[140:143], v83, s[44:47], s66 offen offset:1536 sc1
s_mov_b64 exec, -1
v_exp_f32 v7, v7
v_exp_f32 v8, v8
v_cvt_pk_bf16_f32 v36, v6, v7
v_mfma_f32_32x32x16_bf16 v[18:33], a[28:31], v[58:61], v[62:77]
v_exp_f32 v9, v9
v_exp_f32 v10, v10
v_cvt_pk_bf16_f32 v37, v8, v9
v_exp_f32 v11, v11
v_exp_f32 v12, v12
v_cvt_pk_bf16_f32 v38, v10, v11
s_waitcnt vmcnt(11)
v_mfma_f32_32x32x16_bf16 v[42:57], a[112:115], v[34:37], v[42:57]
v_exp_f32 v13, v13
v_exp_f32 v14, v14
v_cvt_pk_bf16_f32 v39, v12, v13
v_exp_f32 v15, v15
v_exp_f32 v16, v16
v_cvt_pk_bf16_f32 v40, v14, v15
s_mov_b64 exec, s[70:71]
buffer_load_dwordx4 a[144:147], v83, s[44:47], s66 offen offset:2048 sc1
buffer_load_dwordx4 a[148:151], v83, s[44:47], s66 offen offset:2560 sc1
buffer_load_dwordx4 a[152:155], v83, s[44:47], s66 offen offset:3072 sc1
buffer_load_dwordx4 a[156:159], v83, s[44:47], s66 offen offset:3584 sc1
s_mov_b64 exec, -1
v_exp_f32 v17, v17
v_exp_f32 v18, v18
v_cvt_pk_bf16_f32 v41, v16, v17
v_exp_f32 v19, v19
v_exp_f32 v20, v20
s_waitcnt vmcnt(14)
v_mfma_f32_32x32x16_bf16 v[42:57], a[116:119], v[38:41], v[42:57]
v_cvt_pk_bf16_f32 v34, v18, v19
v_exp_f32 v21, v21
v_exp_f32 v22, v22
v_cvt_pk_bf16_f32 v35, v20, v21
v_exp_f32 v23, v23
v_exp_f32 v24, v24
v_cvt_pk_bf16_f32 v36, v22, v23
s_waitcnt vmcnt(11)
v_mfma_f32_32x32x16_bf16 v[2:17], a[32:35], v[58:61], v[62:77]
v_exp_f32 v25, v25
v_exp_f32 v26, v26
v_cvt_pk_bf16_f32 v37, v24, v25
buffer_load_dwordx4 a[48:51], v1, s[44:47], s63 offen offset:0 sc1
v_exp_f32 v27, v27
v_exp_f32 v28, v28
v_cvt_pk_bf16_f32 v38, v26, v27
v_mfma_f32_32x32x16_bf16 v[42:57], a[120:123], v[34:37], v[42:57]
v_exp_f32 v29, v29
v_exp_f32 v30, v30
v_cvt_pk_bf16_f32 v39, v28, v29
v_exp_f32 v31, v31
v_exp_f32 v32, v32
v_cvt_pk_bf16_f32 v40, v30, v31
v_exp_f32 v33, v33
v_exp_f32 v2, v2
v_cvt_pk_bf16_f32 v41, v32, v33
v_exp_f32 v3, v3
v_exp_f32 v4, v4
v_mfma_f32_32x32x16_bf16 v[42:57], a[124:127], v[38:41], v[42:57]
v_cvt_pk_bf16_f32 v34, v2, v3
buffer_load_dwordx4 a[52:55], v1, s[44:47], s63 offen offset:1024 sc1
v_exp_f32 v5, v5
v_exp_f32 v6, v6
v_cvt_pk_bf16_f32 v35, v4, v5
v_exp_f32 v7, v7
v_exp_f32 v8, v8
v_cvt_pk_bf16_f32 v36, v6, v7
s_waitcnt vmcnt(12)
v_mfma_f32_32x32x16_bf16 v[18:33], a[36:39], v[58:61], v[62:77]
v_exp_f32 v9, v9
v_exp_f32 v10, v10
v_cvt_pk_bf16_f32 v37, v8, v9
v_exp_f32 v11, v11
v_exp_f32 v12, v12
v_cvt_pk_bf16_f32 v38, v10, v11
s_waitcnt vmcnt(9)
v_mfma_f32_32x32x16_bf16 v[42:57], a[128:131], v[34:37], v[42:57]
v_exp_f32 v13, v13
v_exp_f32 v14, v14
v_cvt_pk_bf16_f32 v39, v12, v13
buffer_load_dwordx4 a[56:59], v1, s[44:47], s63 offen offset:2048 sc1
v_exp_f32 v15, v15
v_exp_f32 v16, v16
v_cvt_pk_bf16_f32 v40, v14, v15
v_exp_f32 v17, v17
v_exp_f32 v18, v18
v_cvt_pk_bf16_f32 v41, v16, v17
v_exp_f32 v19, v19
v_exp_f32 v20, v20
s_waitcnt vmcnt(9)
v_mfma_f32_32x32x16_bf16 v[42:57], a[132:135], v[38:41], v[42:57]
v_cvt_pk_bf16_f32 v34, v18, v19
v_exp_f32 v21, v21
v_exp_f32 v22, v22
v_cvt_pk_bf16_f32 v35, v20, v21
v_exp_f32 v23, v23
v_exp_f32 v24, v24
v_cvt_pk_bf16_f32 v36, v22, v23
buffer_load_dwordx4 a[60:63], v1, s[44:47], s63 offen offset:3072 sc1
v_mfma_f32_32x32x16_bf16 v[2:17], a[40:43], v[58:61], v[62:77]
v_exp_f32 v25, v25
v_exp_f32 v26, v26
v_cvt_pk_bf16_f32 v37, v24, v25
v_exp_f32 v27, v27
v_exp_f32 v28, v28
v_cvt_pk_bf16_f32 v38, v26, v27
s_waitcnt vmcnt(9)
v_mfma_f32_32x32x16_bf16 v[42:57], a[136:139], v[34:37], v[42:57]
v_exp_f32 v29, v29
v_exp_f32 v30, v30
v_cvt_pk_bf16_f32 v39, v28, v29
v_exp_f32 v31, v31
v_exp_f32 v32, v32
v_cvt_pk_bf16_f32 v40, v30, v31
v_exp_f32 v33, v33
v_exp_f32 v2, v2
v_cvt_pk_bf16_f32 v41, v32, v33
s_mov_b64 exec, s[70:71]
buffer_load_dwordx4 a[160:163], v83, s[44:47], s67 offen offset:0 sc1
buffer_load_dwordx4 a[164:167], v83, s[44:47], s67 offen offset:512 sc1
buffer_load_dwordx4 a[168:171], v83, s[44:47], s67 offen offset:1024 sc1
buffer_load_dwordx4 a[172:175], v83, s[44:47], s67 offen offset:1536 sc1
s_mov_b64 exec, -1
v_exp_f32 v3, v3
v_exp_f32 v4, v4
s_waitcnt vmcnt(12)
v_mfma_f32_32x32x16_bf16 v[42:57], a[140:143], v[38:41], v[42:57]
v_cvt_pk_bf16_f32 v34, v2, v3
v_exp_f32 v5, v5
v_exp_f32 v6, v6
v_cvt_pk_bf16_f32 v35, v4, v5
v_exp_f32 v7, v7
v_exp_f32 v8, v8
v_cvt_pk_bf16_f32 v36, v6, v7
v_mfma_f32_32x32x16_bf16 v[18:33], a[44:47], v[58:61], v[62:77]
v_exp_f32 v9, v9
v_exp_f32 v10, v10
v_cvt_pk_bf16_f32 v37, v8, v9
v_exp_f32 v11, v11
v_exp_f32 v12, v12
v_cvt_pk_bf16_f32 v38, v10, v11
s_mov_b64 exec, s[70:71]
buffer_load_dwordx4 a[176:179], v83, s[44:47], s67 offen offset:2048 sc1
buffer_load_dwordx4 a[180:183], v83, s[44:47], s67 offen offset:2560 sc1
buffer_load_dwordx4 a[184:187], v83, s[44:47], s67 offen offset:3072 sc1
buffer_load_dwordx4 a[188:191], v83, s[44:47], s67 offen offset:3584 sc1
s_mov_b64 exec, -1
s_waitcnt vmcnt(15)
v_mfma_f32_32x32x16_bf16 v[42:57], a[144:147], v[34:37], v[42:57]
v_exp_f32 v13, v13
v_exp_f32 v14, v14
v_cvt_pk_bf16_f32 v39, v12, v13
v_exp_f32 v15, v15
v_exp_f32 v16, v16
v_cvt_pk_bf16_f32 v40, v14, v15
v_exp_f32 v17, v17
v_exp_f32 v18, v18
v_cvt_pk_bf16_f32 v41, v16, v17
v_exp_f32 v19, v19
v_exp_f32 v20, v20
s_waitcnt vmcnt(14)
v_mfma_f32_32x32x16_bf16 v[42:57], a[148:151], v[38:41], v[42:57]
v_cvt_pk_bf16_f32 v34, v18, v19
v_exp_f32 v21, v21
v_exp_f32 v22, v22
v_cvt_pk_bf16_f32 v35, v20, v21
v_exp_f32 v23, v23
v_exp_f32 v24, v24
v_cvt_pk_bf16_f32 v36, v22, v23
s_waitcnt vmcnt(11)
v_mfma_f32_32x32x16_bf16 v[2:17], a[48:51], v[58:61], v[62:77]
v_exp_f32 v25, v25
v_exp_f32 v26, v26
v_cvt_pk_bf16_f32 v37, v24, v25
v_mov_b32 v88, -1
v_mov_b32 v89, -1
v_exp_f32 v27, v27
v_exp_f32 v28, v28
v_cvt_pk_bf16_f32 v38, v26, v27
v_mov_b32 v90, -1
v_mfma_f32_32x32x16_bf16 v[42:57], a[152:155], v[34:37], v[42:57]
v_exp_f32 v29, v29
v_exp_f32 v30, v30
v_cvt_pk_bf16_f32 v39, v28, v29
v_mov_b32 v91, -1
v_exp_f32 v31, v31
v_exp_f32 v32, v32
v_cvt_pk_bf16_f32 v40, v30, v31
v_lshlrev_b32 v92, 2, v0
v_exp_f32 v33, v33
v_exp_f32 v2, v2
v_cvt_pk_bf16_f32 v41, v32, v33
s_add_u32 s60, s91, 0x200000
s_add_u32 s61, s91, 0x400000
v_exp_f32 v3, v3
v_exp_f32 v4, v4
v_mfma_f32_32x32x16_bf16 v[42:57], a[156:159], v[38:41], v[42:57]
v_cvt_pk_bf16_f32 v34, v2, v3
s_lshr_b32 s62, s91, 3
v_exp_f32 v5, v5
v_exp_f32 v6, v6
v_cvt_pk_bf16_f32 v35, v4, v5
s_add_u32 s62, s62, 0x70000
v_exp_f32 v7, v7
v_exp_f32 v8, v8
v_cvt_pk_bf16_f32 v36, v6, v7
s_add_u32 s66, s92, 4096
s_waitcnt vmcnt(10)
v_mfma_f32_32x32x16_bf16 v[18:33], a[52:55], v[58:61], v[62:77]
v_exp_f32 v9, v9
v_exp_f32 v10, v10
v_cvt_pk_bf16_f32 v37, v8, v9
v_exp_f32 v11, v11
v_exp_f32 v12, v12
v_cvt_pk_bf16_f32 v38, v10, v11
s_waitcnt vmcnt(7)
v_mfma_f32_32x32x16_bf16 v[42:57], a[160:163], v[34:37], v[42:57]
v_exp_f32 v13, v13
v_exp_f32 v14, v14
v_cvt_pk_bf16_f32 v39, v12, v13
v_exp_f32 v15, v15
v_exp_f32 v16, v16
v_cvt_pk_bf16_f32 v40, v14, v15
v_exp_f32 v17, v17
v_exp_f32 v18, v18
v_cvt_pk_bf16_f32 v41, v16, v17
v_exp_f32 v19, v19
v_exp_f32 v20, v20
s_waitcnt vmcnt(6)
v_mfma_f32_32x32x16_bf16 v[42:57], a[164:167], v[38:41], v[42:57]
v_cvt_pk_bf16_f32 v34, v18, v19
v_exp_f32 v21, v21
v_exp_f32 v22, v22
v_cvt_pk_bf16_f32 v35, v20, v21
v_exp_f32 v23, v23
v_exp_f32 v24, v24
v_cvt_pk_bf16_f32 v36, v22, v23
v_mfma_f32_32x32x16_bf16 v[2:17], a[56:59], v[58:61], v[62:77]
v_exp_f32 v25, v25
v_exp_f32 v26, v26
v_cvt_pk_bf16_f32 v37, v24, v25
v_exp_f32 v27, v27
v_exp_f32 v28, v28
v_cvt_pk_bf16_f32 v38, v26, v27
s_waitcnt vmcnt(5)
v_mfma_f32_32x32x16_bf16 v[42:57], a[168:171], v[34:37], v[42:57]
v_exp_f32 v29, v29
v_exp_f32 v30, v30
v_cvt_pk_bf16_f32 v39, v28, v29
v_accvgpr_read_b32 v108, a240
v_exp_f32 v31, v31
v_exp_f32 v32, v32
v_cvt_pk_bf16_f32 v40, v30, v31
v_exp_f32 v33, v33
v_exp_f32 v2, v2
v_cvt_pk_bf16_f32 v41, v32, v33
v_exp_f32 v3, v3
v_exp_f32 v4, v4
s_waitcnt vmcnt(4)
v_mfma_f32_32x32x16_bf16 v[42:57], a[172:175], v[38:41], v[42:57]
v_cvt_pk_bf16_f32 v34, v2, v3
v_exp_f32 v5, v5
v_exp_f32 v6, v6
v_cvt_pk_bf16_f32 v35, v4, v5
v_exp_f32 v7, v7
v_exp_f32 v8, v8
v_cvt_pk_bf16_f32 v36, v6, v7
v_mfma_f32_32x32x16_bf16 v[18:33], a[60:63], v[58:61], v[62:77]
ds_read_b128 v[58:61], v78 offset:1024
s_cmp_lg_u32 s69, 0
s_cbranch_scc1 negslow_16
negback_17:
v_exp_f32 v9, v9
v_exp_f32 v10, v10
v_cvt_pk_bf16_f32 v37, v8, v9
v_exp_f32 v11, v11
v_exp_f32 v12, v12
v_cvt_pk_bf16_f32 v38, v10, v11
s_waitcnt vmcnt(3)
v_mfma_f32_32x32x16_bf16 v[42:57], a[176:179], v[34:37], v[42:57]
v_exp_f32 v13, v13
v_exp_f32 v14, v14
v_cvt_pk_bf16_f32 v39, v12, v13
v_accvgpr_read_b32 v109, a241
v_exp_f32 v15, v15
v_exp_f32 v16, v16
v_cvt_pk_bf16_f32 v40, v14, v15
v_exp_f32 v17, v17
v_exp_f32 v18, v18
v_cvt_pk_bf16_f32 v41, v16, v17
v_exp_f32 v19, v19
v_exp_f32 v20, v20
s_waitcnt vmcnt(2)
v_mfma_f32_32x32x16_bf16 v[42:57], a[180:183], v[38:41], v[42:57]
v_cvt_pk_bf16_f32 v34, v18, v19
v_exp_f32 v21, v21
v_exp_f32 v22, v22
v_cvt_pk_bf16_f32 v35, v20, v21
v_exp_f32 v23, v23
v_exp_f32 v24, v24
v_cvt_pk_bf16_f32 v36, v22, v23
s_waitcnt lgkmcnt(0)
v_mfma_f32_32x32x16_bf16 v[2:17], a[0:3], v[58:61], v[62:77]
v_exp_f32 v25, v25
v_exp_f32 v26, v26
v_cvt_pk_bf16_f32 v37, v24, v25
v_exp_f32 v27, v27
v_exp_f32 v28, v28
v_cvt_pk_bf16_f32 v38, v26, v27
s_waitcnt vmcnt(1)
v_mfma_f32_32x32x16_bf16 v[42:57], a[184:187], v[34:37], v[42:57]
v_exp_f32 v29, v29
v_exp_f32 v30, v30
v_cvt_pk_bf16_f32 v39, v28, v29
v_exp_f32 v31, v31
v_exp_f32 v32, v32
v_cvt_pk_bf16_f32 v40, v30, v31
buffer_load_dwordx4 a[192:195], v108, s[28:31], s84 offen
v_exp_f32 v33, v33
v_exp_f32 v2, v2
v_cvt_pk_bf16_f32 v41, v32, v33
v_exp_f32 v3, v3
v_exp_f32 v4, v4
s_waitcnt vmcnt(1)
v_mfma_f32_32x32x16_bf16 v[42:57], a[188:191], v[38:41], v[42:57]
v_cvt_pk_bf16_f32 v34, v2, v3
v_exp_f32 v5, v5
v_exp_f32 v6, v6
v_cvt_pk_bf16_f32 v35, v4, v5
v_exp_f32 v7, v7
v_exp_f32 v8, v8
v_cvt_pk_bf16_f32 v36, v6, v7
v_mfma_f32_32x32x16_bf16 v[18:33], a[4:7], v[58:61], v[62:77]
v_exp_f32 v9, v9
v_exp_f32 v10, v10
v_cvt_pk_bf16_f32 v37, v8, v9
v_exp_f32 v11, v11
v_exp_f32 v12, v12
v_cvt_pk_bf16_f32 v38, v10, v11
ds_write_b128 v79, v[42:45] offset:0
ds_write_b128 v79, v[46:49] offset:1024
ds_write_b32 v80, v50 offset:0
v_mfma_f32_32x32x16_bf16 v[42:57], a[64:67], v[34:37], 0
v_exp_f32 v13, v13
v_exp_f32 v14, v14
v_cvt_pk_bf16_f32 v39, v12, v13
v_exp_f32 v15, v15
v_exp_f32 v16, v16
v_cvt_pk_bf16_f32 v40, v14, v15
buffer_load_dwordx4 a[196:199], v108, s[32:35], s84 offen
v_exp_f32 v17, v17
v_exp_f32 v18, v18
v_cvt_pk_bf16_f32 v41, v16, v17
v_exp_f32 v19, v19
v_exp_f32 v20, v20
v_mfma_f32_32x32x16_bf16 v[42:57], a[68:71], v[38:41], v[42:57]
v_cvt_pk_bf16_f32 v34, v18, v19
v_exp_f32 v21, v21
v_exp_f32 v22, v22
v_cvt_pk_bf16_f32 v35, v20, v21
v_exp_f32 v23, v23
v_exp_f32 v24, v24
v_cvt_pk_bf16_f32 v36, v22, v23
v_mfma_f32_32x32x16_bf16 v[2:17], a[8:11], v[58:61], v[62:77]
v_exp_f32 v25, v25
v_exp_f32 v26, v26
v_cvt_pk_bf16_f32 v37, v24, v25
v_exp_f32 v27, v27
v_exp_f32 v28, v28
v_cvt_pk_bf16_f32 v38, v26, v27
v_mfma_f32_32x32x16_bf16 v[42:57], a[72:75], v[34:37], v[42:57]
v_exp_f32 v29, v29
v_exp_f32 v30, v30
v_cvt_pk_bf16_f32 v39, v28, v29
v_exp_f32 v31, v31
v_exp_f32 v32, v32
v_cvt_pk_bf16_f32 v40, v30, v31
v_exp_f32 v33, v33
v_exp_f32 v2, v2
v_cvt_pk_bf16_f32 v41, v32, v33
s_cmp_eq_u32 s49, 3
s_cbranch_scc1 ptwo_7
buffer_load_dwordx4 a[200:203], v108, s[28:31], s81 offen
buffer_load_dwordx4 a[204:207], v108, s[32:35], s81 offen
s_branch ptj_8
ptwo_7:
buffer_load_dwordx4 a[200:203], v109, s[24:27], s81 offen
buffer_load_dwordx4 a[204:207], v109, s[24:27], s81 offen offset:32
ptj_8:
v_exp_f32 v3, v3
v_exp_f32 v4, v4
v_mfma_f32_32x32x16_bf16 v[42:57], a[76:79], v[38:41], v[42:57]
v_cvt_pk_bf16_f32 v34, v2, v3
v_exp_f32 v5, v5
v_exp_f32 v6, v6
v_cvt_pk_bf16_f32 v35, v4, v5
v_exp_f32 v7, v7
v_exp_f32 v8, v8
v_cvt_pk_bf16_f32 v36, v6, v7
v_mfma_f32_32x32x16_bf16 v[18:33], a[12:15], v[58:61], v[62:77]
v_exp_f32 v9, v9
v_exp_f32 v10, v10
v_cvt_pk_bf16_f32 v37, v8, v9
v_exp_f32 v11, v11
v_exp_f32 v12, v12
v_cvt_pk_bf16_f32 v38, v10, v11
v_mfma_f32_32x32x16_bf16 v[42:57], a[80:83], v[34:37], v[42:57]
v_exp_f32 v13, v13
v_exp_f32 v14, v14
v_cvt_pk_bf16_f32 v39, v12, v13
v_exp_f32 v15, v15
v_exp_f32 v16, v16
v_cvt_pk_bf16_f32 v40, v14, v15
v_exp_f32 v17, v17
v_exp_f32 v18, v18
v_cvt_pk_bf16_f32 v41, v16, v17
buffer_load_dwordx4 a[208:211], v109, s[36:39], s85 offen
v_exp_f32 v19, v19
v_exp_f32 v20, v20
v_mfma_f32_32x32x16_bf16 v[42:57], a[84:87], v[38:41], v[42:57]
v_cvt_pk_bf16_f32 v34, v18, v19
v_exp_f32 v21, v21
v_exp_f32 v22, v22
v_cvt_pk_bf16_f32 v35, v20, v21
v_exp_f32 v23, v23
v_exp_f32 v24, v24
v_cvt_pk_bf16_f32 v36, v22, v23
v_mfma_f32_32x32x16_bf16 v[2:17], a[16:19], v[58:61], v[62:77]
v_exp_f32 v25, v25
v_exp_f32 v26, v26
v_cvt_pk_bf16_f32 v37, v24, v25
v_exp_f32 v27, v27
v_exp_f32 v28, v28
v_cvt_pk_bf16_f32 v38, v26, v27
v_mfma_f32_32x32x16_bf16 v[42:57], a[88:91], v[34:37], v[42:57]
v_exp_f32 v29, v29
v_exp_f32 v30, v30
v_cvt_pk_bf16_f32 v39, v28, v29
v_exp_f32 v31, v31
v_exp_f32 v32, v32
v_cvt_pk_bf16_f32 v40, v30, v31
v_exp_f32 v33, v33
v_exp_f32 v2, v2
v_cvt_pk_bf16_f32 v41, v32, v33
v_exp_f32 v3, v3
v_exp_f32 v4, v4
v_mfma_f32_32x32x16_bf16 v[42:57], a[92:95], v[38:41], v[42:57]
v_cvt_pk_bf16_f32 v34, v2, v3
buffer_load_dwordx4 a[212:215], v109, s[36:39], s85 offen offset:32
v_exp_f32 v5, v5
v_exp_f32 v6, v6
v_cvt_pk_bf16_f32 v35, v4, v5
v_exp_f32 v7, v7
v_exp_f32 v8, v8
v_cvt_pk_bf16_f32 v36, v6, v7
v_mfma_f32_32x32x16_bf16 v[18:33], a[20:23], v[58:61], v[62:77]
v_exp_f32 v9, v9
v_exp_f32 v10, v10
v_cvt_pk_bf16_f32 v37, v8, v9
v_exp_f32 v11, v11
v_exp_f32 v12, v12
v_cvt_pk_bf16_f32 v38, v10, v11
v_mfma_f32_32x32x16_bf16 v[42:57], a[96:99], v[34:37], v[42:57]
v_exp_f32 v13, v13
v_exp_f32 v14, v14
v_cvt_pk_bf16_f32 v39, v12, v13
v_exp_f32 v15, v15
v_exp_f32 v16, v16
v_cvt_pk_bf16_f32 v40, v14, v15
v_exp_f32 v17, v17
v_exp_f32 v18, v18
v_cvt_pk_bf16_f32 v41, v16, v17
v_exp_f32 v19, v19
v_exp_f32 v20, v20
v_mfma_f32_32x32x16_bf16 v[42:57], a[100:103], v[38:41], v[42:57]
v_cvt_pk_bf16_f32 v34, v18, v19
v_exp_f32 v21, v21
v_exp_f32 v22, v22
v_cvt_pk_bf16_f32 v35, v20, v21
v_exp_f32 v23, v23
v_exp_f32 v24, v24
v_cvt_pk_bf16_f32 v36, v22, v23
v_mfma_f32_32x32x16_bf16 v[2:17], a[24:27], v[58:61], v[62:77]
v_exp_f32 v25, v25
v_exp_f32 v26, v26
v_cvt_pk_bf16_f32 v37, v24, v25
v_exp_f32 v27, v27
v_exp_f32 v28, v28
v_cvt_pk_bf16_f32 v38, v26, v27
v_mfma_f32_32x32x16_bf16 v[42:57], a[104:107], v[34:37], v[42:57]
v_exp_f32 v29, v29
v_exp_f32 v30, v30
v_cvt_pk_bf16_f32 v39, v28, v29
v_exp_f32 v31, v31
v_exp_f32 v32, v32
v_cvt_pk_bf16_f32 v40, v30, v31
v_exp_f32 v33, v33
v_exp_f32 v2, v2
v_cvt_pk_bf16_f32 v41, v32, v33
v_exp_f32 v3, v3
v_exp_f32 v4, v4
v_mfma_f32_32x32x16_bf16 v[42:57], a[108:111], v[38:41], v[42:57]
v_cvt_pk_bf16_f32 v34, v2, v3
v_exp_f32 v5, v5
v_exp_f32 v6, v6
v_cvt_pk_bf16_f32 v35, v4, v5
v_exp_f32 v7, v7
v_exp_f32 v8, v8
v_cvt_pk_bf16_f32 v36, v6, v7
v_mfma_f32_32x32x16_bf16 v[18:33], a[28:31], v[58:61], v[62:77]
v_exp_f32 v9, v9
v_exp_f32 v10, v10
v_cvt_pk_bf16_f32 v37, v8, v9
v_exp_f32 v11, v11
v_exp_f32 v12, v12
v_cvt_pk_bf16_f32 v38, v10, v11
v_mfma_f32_32x32x16_bf16 v[42:57], a[112:115], v[34:37], v[42:57]
v_exp_f32 v13, v13
v_exp_f32 v14, v14
v_cvt_pk_bf16_f32 v39, v12, v13
v_exp_f32 v15, v15
v_exp_f32 v16, v16
v_cvt_pk_bf16_f32 v40, v14, v15
v_exp_f32 v17, v17
v_exp_f32 v18, v18
v_cvt_pk_bf16_f32 v41, v16, v17
v_exp_f32 v19, v19
v_exp_f32 v20, v20
v_mfma_f32_32x32x16_bf16 v[42:57], a[116:119], v[38:41], v[42:57]
v_cvt_pk_bf16_f32 v34, v18, v19
v_exp_f32 v21, v21
v_exp_f32 v22, v22
v_cvt_pk_bf16_f32 v35, v20, v21
v_exp_f32 v23, v23
v_exp_f32 v24, v24
v_cvt_pk_bf16_f32 v36, v22, v23
v_mfma_f32_32x32x16_bf16 v[2:17], a[32:35], v[58:61], v[62:77]
v_exp_f32 v25, v25
v_exp_f32 v26, v26
v_cvt_pk_bf16_f32 v37, v24, v25
v_exp_f32 v27, v27
v_exp_f32 v28, v28
v_cvt_pk_bf16_f32 v38, v26, v27
v_mfma_f32_32x32x16_bf16 v[42:57], a[120:123], v[34:37], v[42:57]
v_exp_f32 v29, v29
v_exp_f32 v30, v30
v_cvt_pk_bf16_f32 v39, v28, v29
v_exp_f32 v31, v31
v_exp_f32 v32, v32
v_cvt_pk_bf16_f32 v40, v30, v31
v_exp_f32 v33, v33
v_exp_f32 v2, v2
v_cvt_pk_bf16_f32 v41, v32, v33
v_exp_f32 v3, v3
v_exp_f32 v4, v4
v_mfma_f32_32x32x16_bf16 v[42:57], a[124:127], v[38:41], v[42:57]
v_cvt_pk_bf16_f32 v34, v2, v3
v_exp_f32 v5, v5
v_exp_f32 v6, v6
v_cvt_pk_bf16_f32 v35, v4, v5
v_exp_f32 v7, v7
v_exp_f32 v8, v8
v_cvt_pk_bf16_f32 v36, v6, v7
v_mfma_f32_32x32x16_bf16 v[18:33], a[36:39], v[58:61], v[62:77]
v_exp_f32 v9, v9
v_exp_f32 v10, v10
v_cvt_pk_bf16_f32 v37, v8, v9
v_exp_f32 v11, v11
v_exp_f32 v12, v12
v_cvt_pk_bf16_f32 v38, v10, v11
v_mfma_f32_32x32x16_bf16 v[42:57], a[128:131], v[34:37], v[42:57]
v_exp_f32 v13, v13
v_exp_f32 v14, v14
v_cvt_pk_bf16_f32 v39, v12, v13
v_exp_f32 v15, v15
v_exp_f32 v16, v16
v_cvt_pk_bf16_f32 v40, v14, v15
v_exp_f32 v17, v17
v_exp_f32 v18, v18
v_cvt_pk_bf16_f32 v41, v16, v17
v_exp_f32 v19, v19
v_exp_f32 v20, v20
v_mfma_f32_32x32x16_bf16 v[42:57], a[132:135], v[38:41], v[42:57]
v_cvt_pk_bf16_f32 v34, v18, v19
v_exp_f32 v21, v21
v_exp_f32 v22, v22
v_cvt_pk_bf16_f32 v35, v20, v21
v_exp_f32 v23, v23
v_exp_f32 v24, v24
v_cvt_pk_bf16_f32 v36, v22, v23
v_mfma_f32_32x32x16_bf16 v[2:17], a[40:43], v[58:61], v[62:77]
v_exp_f32 v25, v25
v_exp_f32 v26, v26
v_cvt_pk_bf16_f32 v37, v24, v25
v_exp_f32 v27, v27
v_exp_f32 v28, v28
v_cvt_pk_bf16_f32 v38, v26, v27
v_mfma_f32_32x32x16_bf16 v[42:57], a[136:139], v[34:37], v[42:57]
v_exp_f32 v29, v29
v_exp_f32 v30, v30
v_cvt_pk_bf16_f32 v39, v28, v29
v_exp_f32 v31, v31
v_exp_f32 v32, v32
v_cvt_pk_bf16_f32 v40, v30, v31
v_exp_f32 v33, v33
v_exp_f32 v2, v2
v_cvt_pk_bf16_f32 v41, v32, v33
v_exp_f32 v3, v3
v_exp_f32 v4, v4
v_mfma_f32_32x32x16_bf16 v[42:57], a[140:143], v[38:41], v[42:57]
v_cvt_pk_bf16_f32 v34, v2, v3
v_exp_f32 v5, v5
v_exp_f32 v6, v6
v_cvt_pk_bf16_f32 v35, v4, v5
v_exp_f32 v7, v7
v_exp_f32 v8, v8
v_cvt_pk_bf16_f32 v36, v6, v7
v_mfma_f32_32x32x16_bf16 v[18:33], a[44:47], v[58:61], v[62:77]
v_exp_f32 v9, v9
v_exp_f32 v10, v10
v_cvt_pk_bf16_f32 v37, v8, v9
v_exp_f32 v11, v11
v_exp_f32 v12, v12
v_cvt_pk_bf16_f32 v38, v10, v11
v_mfma_f32_32x32x16_bf16 v[42:57], a[144:147], v[34:37], v[42:57]
v_exp_f32 v13, v13
v_exp_f32 v14, v14
v_cvt_pk_bf16_f32 v39, v12, v13
v_exp_f32 v15, v15
v_exp_f32 v16, v16
v_cvt_pk_bf16_f32 v40, v14, v15
v_exp_f32 v17, v17
v_exp_f32 v18, v18
v_cvt_pk_bf16_f32 v41, v16, v17
v_exp_f32 v19, v19
v_exp_f32 v20, v20
v_mfma_f32_32x32x16_bf16 v[42:57], a[148:151], v[38:41], v[42:57]
v_cvt_pk_bf16_f32 v34, v18, v19
v_exp_f32 v21, v21
v_exp_f32 v22, v22
v_cvt_pk_bf16_f32 v35, v20, v21
v_exp_f32 v23, v23
v_exp_f32 v24, v24
v_cvt_pk_bf16_f32 v36, v22, v23
v_mfma_f32_32x32x16_bf16 v[2:17], a[48:51], v[58:61], v[62:77]
v_exp_f32 v25, v25
v_exp_f32 v26, v26
v_cvt_pk_bf16_f32 v37, v24, v25
v_exp_f32 v27, v27
v_exp_f32 v28, v28
v_cvt_pk_bf16_f32 v38, v26, v27
v_mfma_f32_32x32x16_bf16 v[42:57], a[152:155], v[34:37], v[42:57]
v_exp_f32 v29, v29
v_exp_f32 v30, v30
v_cvt_pk_bf16_f32 v39, v28, v29
v_exp_f32 v31, v31
v_exp_f32 v32, v32
v_cvt_pk_bf16_f32 v40, v30, v31
v_exp_f32 v33, v33
v_exp_f32 v2, v2
v_cvt_pk_bf16_f32 v41, v32, v33
v_exp_f32 v3, v3
v_exp_f32 v4, v4
v_mfma_f32_32x32x16_bf16 v[42:57], a[156:159], v[38:41], v[42:57]
v_cvt_pk_bf16_f32 v34, v2, v3
v_exp_f32 v5, v5
v_exp_f32 v6, v6
v_cvt_pk_bf16_f32 v35, v4, v5
v_exp_f32 v7, v7
v_exp_f32 v8, v8
v_cvt_pk_bf16_f32 v36, v6, v7
v_mfma_f32_32x32x16_bf16 v[18:33], a[52:55], v[58:61], v[62:77]
v_exp_f32 v9, v9
v_exp_f32 v10, v10
v_cvt_pk_bf16_f32 v37, v8, v9
v_exp_f32 v11, v11
v_exp_f32 v12, v12
v_cvt_pk_bf16_f32 v38, v10, v11
v_mfma_f32_32x32x16_bf16 v[42:57], a[160:163], v[34:37], v[42:57]
v_exp_f32 v13, v13
v_exp_f32 v14, v14
v_cvt_pk_bf16_f32 v39, v12, v13
v_exp_f32 v15, v15
v_exp_f32 v16, v16
v_cvt_pk_bf16_f32 v40, v14, v15
v_exp_f32 v17, v17
v_exp_f32 v18, v18
v_cvt_pk_bf16_f32 v41, v16, v17
v_exp_f32 v19, v19
v_exp_f32 v20, v20
v_mfma_f32_32x32x16_bf16 v[42:57], a[164:167], v[38:41], v[42:57]
v_cvt_pk_bf16_f32 v34, v18, v19
v_exp_f32 v21, v21
v_exp_f32 v22, v22
v_cvt_pk_bf16_f32 v35, v20, v21
v_exp_f32 v23, v23
v_exp_f32 v24, v24
v_cvt_pk_bf16_f32 v36, v22, v23
v_mfma_f32_32x32x16_bf16 v[2:17], a[56:59], v[58:61], v[62:77]
v_exp_f32 v25, v25
v_exp_f32 v26, v26
v_cvt_pk_bf16_f32 v37, v24, v25
v_exp_f32 v27, v27
v_exp_f32 v28, v28
v_cvt_pk_bf16_f32 v38, v26, v27
v_mfma_f32_32x32x16_bf16 v[42:57], a[168:171], v[34:37], v[42:57]
v_exp_f32 v29, v29
v_exp_f32 v30, v30
v_cvt_pk_bf16_f32 v39, v28, v29
v_exp_f32 v31, v31
v_exp_f32 v32, v32
v_cvt_pk_bf16_f32 v40, v30, v31
v_exp_f32 v33, v33
v_exp_f32 v2, v2
v_cvt_pk_bf16_f32 v41, v32, v33
v_exp_f32 v3, v3
v_exp_f32 v4, v4
v_mfma_f32_32x32x16_bf16 v[42:57], a[172:175], v[38:41], v[42:57]
v_cvt_pk_bf16_f32 v34, v2, v3
v_exp_f32 v5, v5
v_exp_f32 v6, v6
v_cvt_pk_bf16_f32 v35, v4, v5
v_exp_f32 v7, v7
v_exp_f32 v8, v8
v_cvt_pk_bf16_f32 v36, v6, v7
v_mfma_f32_32x32x16_bf16 v[18:33], a[60:63], v[58:61], v[62:77]
ds_read_b128 v[58:61], v78 offset:2048
s_cmp_lg_u32 s69, 0
s_cbranch_scc1 negslow_18
negback_19:
v_exp_f32 v9, v9
v_exp_f32 v10, v10
v_cvt_pk_bf16_f32 v37, v8, v9
v_exp_f32 v11, v11
v_exp_f32 v12, v12
v_cvt_pk_bf16_f32 v38, v10, v11
v_mfma_f32_32x32x16_bf16 v[42:57], a[176:179], v[34:37], v[42:57]
v_exp_f32 v13, v13
v_exp_f32 v14, v14
v_cvt_pk_bf16_f32 v39, v12, v13
v_exp_f32 v15, v15
v_exp_f32 v16, v16
v_cvt_pk_bf16_f32 v40, v14, v15
v_exp_f32 v17, v17
v_exp_f32 v18, v18
v_cvt_pk_bf16_f32 v41, v16, v17
v_exp_f32 v19, v19
v_exp_f32 v20, v20
v_mfma_f32_32x32x16_bf16 v[42:57], a[180:183], v[38:41], v[42:57]
v_cvt_pk_bf16_f32 v34, v18, v19
v_exp_f32 v21, v21
v_exp_f32 v22, v22
v_cvt_pk_bf16_f32 v35, v20, v21
v_exp_f32 v23, v23
v_exp_f32 v24, v24
v_cvt_pk_bf16_f32 v36, v22, v23
s_waitcnt lgkmcnt(0)
v_mfma_f32_32x32x16_bf16 v[2:17], a[0:3], v[58:61], v[62:77]
v_exp_f32 v25, v25
v_exp_f32 v26, v26
v_cvt_pk_bf16_f32 v37, v24, v25
v_exp_f32 v27, v27
v_exp_f32 v28, v28
v_cvt_pk_bf16_f32 v38, v26, v27
v_mfma_f32_32x32x16_bf16 v[42:57], a[184:187], v[34:37], v[42:57]
v_exp_f32 v29, v29
v_exp_f32 v30, v30
v_cvt_pk_bf16_f32 v39, v28, v29
v_exp_f32 v31, v31
v_exp_f32 v32, v32
v_cvt_pk_bf16_f32 v40, v30, v31
v_exp_f32 v33, v33
v_exp_f32 v2, v2
v_cvt_pk_bf16_f32 v41, v32, v33
v_exp_f32 v3, v3
v_exp_f32 v4, v4
v_mfma_f32_32x32x16_bf16 v[42:57], a[188:191], v[38:41], v[42:57]
v_cvt_pk_bf16_f32 v34, v2, v3
v_exp_f32 v5, v5
v_exp_f32 v6, v6
v_cvt_pk_bf16_f32 v35, v4, v5
v_exp_f32 v7, v7
v_exp_f32 v8, v8
v_cvt_pk_bf16_f32 v36, v6, v7
v_mfma_f32_32x32x16_bf16 v[18:33], a[4:7], v[58:61], v[62:77]
v_exp_f32 v9, v9
v_exp_f32 v10, v10
v_cvt_pk_bf16_f32 v37, v8, v9
v_exp_f32 v11, v11
v_exp_f32 v12, v12
v_cvt_pk_bf16_f32 v38, v10, v11
ds_write_b128 v79, v[42:45] offset:9216
ds_write_b128 v79, v[46:49] offset:10240
ds_write_b32 v80, v50 offset:9216
v_mfma_f32_32x32x16_bf16 v[42:57], a[64:67], v[34:37], 0
v_exp_f32 v13, v13
v_exp_f32 v14, v14
v_cvt_pk_bf16_f32 v39, v12, v13
v_exp_f32 v15, v15
v_exp_f32 v16, v16
v_cvt_pk_bf16_f32 v40, v14, v15
v_exp_f32 v17, v17
v_exp_f32 v18, v18
v_cvt_pk_bf16_f32 v41, v16, v17
v_exp_f32 v19, v19
v_exp_f32 v20, v20
v_mfma_f32_32x32x16_bf16 v[42:57], a[68:71], v[38:41], v[42:57]
v_cvt_pk_bf16_f32 v34, v18, v19
v_exp_f32 v21, v21
v_exp_f32 v22, v22
v_cvt_pk_bf16_f32 v35, v20, v21
v_exp_f32 v23, v23
v_exp_f32 v24, v24
v_cvt_pk_bf16_f32 v36, v22, v23
v_mfma_f32_32x32x16_bf16 v[2:17], a[8:11], v[58:61], v[62:77]
v_exp_f32 v25, v25
v_exp_f32 v26, v26
v_cvt_pk_bf16_f32 v37, v24, v25
v_exp_f32 v27, v27
v_exp_f32 v28, v28
v_cvt_pk_bf16_f32 v38, v26, v27
v_mfma_f32_32x32x16_bf16 v[42:57], a[72:75], v[34:37], v[42:57]
v_exp_f32 v29, v29
v_exp_f32 v30, v30
v_cvt_pk_bf16_f32 v39, v28, v29
v_exp_f32 v31, v31
v_exp_f32 v32, v32
v_cvt_pk_bf16_f32 v40, v30, v31
v_exp_f32 v33, v33
v_exp_f32 v2, v2
v_cvt_pk_bf16_f32 v41, v32, v33
v_exp_f32 v3, v3
v_exp_f32 v4, v4
v_mfma_f32_32x32x16_bf16 v[42:57], a[76:79], v[38:41], v[42:57]
v_cvt_pk_bf16_f32 v34, v2, v3
v_exp_f32 v5, v5
v_exp_f32 v6, v6
v_cvt_pk_bf16_f32 v35, v4, v5
v_exp_f32 v7, v7
v_exp_f32 v8, v8
v_cvt_pk_bf16_f32 v36, v6, v7
v_mfma_f32_32x32x16_bf16 v[18:33], a[12:15], v[58:61], v[62:77]
v_exp_f32 v9, v9
v_exp_f32 v10, v10
v_cvt_pk_bf16_f32 v37, v8, v9
v_exp_f32 v11, v11
v_exp_f32 v12, v12
v_cvt_pk_bf16_f32 v38, v10, v11
v_mfma_f32_32x32x16_bf16 v[42:57], a[80:83], v[34:37], v[42:57]
v_exp_f32 v13, v13
v_exp_f32 v14, v14
v_cvt_pk_bf16_f32 v39, v12, v13
v_exp_f32 v15, v15
v_exp_f32 v16, v16
v_cvt_pk_bf16_f32 v40, v14, v15
v_exp_f32 v17, v17
v_exp_f32 v18, v18
v_cvt_pk_bf16_f32 v41, v16, v17
v_exp_f32 v19, v19
v_exp_f32 v20, v20
v_mfma_f32_32x32x16_bf16 v[42:57], a[84:87], v[38:41], v[42:57]
v_cvt_pk_bf16_f32 v34, v18, v19
v_exp_f32 v21, v21
v_exp_f32 v22, v22
v_cvt_pk_bf16_f32 v35, v20, v21
v_exp_f32 v23, v23
v_exp_f32 v24, v24
v_cvt_pk_bf16_f32 v36, v22, v23
v_mfma_f32_32x32x16_bf16 v[2:17], a[16:19], v[58:61], v[62:77]
v_exp_f32 v25, v25
v_exp_f32 v26, v26
v_cvt_pk_bf16_f32 v37, v24, v25
v_exp_f32 v27, v27
v_exp_f32 v28, v28
v_cvt_pk_bf16_f32 v38, v26, v27
v_mfma_f32_32x32x16_bf16 v[42:57], a[88:91], v[34:37], v[42:57]
v_exp_f32 v29, v29
v_exp_f32 v30, v30
v_cvt_pk_bf16_f32 v39, v28, v29
v_exp_f32 v31, v31
v_exp_f32 v32, v32
v_cvt_pk_bf16_f32 v40, v30, v31
v_exp_f32 v33, v33
v_exp_f32 v2, v2
v_cvt_pk_bf16_f32 v41, v32, v33
v_exp_f32 v3, v3
v_exp_f32 v4, v4
v_mfma_f32_32x32x16_bf16 v[42:57], a[92:95], v[38:41], v[42:57]
v_cvt_pk_bf16_f32 v34, v2, v3
v_exp_f32 v5, v5
v_exp_f32 v6, v6
v_cvt_pk_bf16_f32 v35, v4, v5
buffer_store_dwordx4 v[88:91], v1, s[44:47], s60 offen offset:0 sc1
v_accvgpr_read_b32 v108, a242
v_exp_f32 v7, v7
v_exp_f32 v8, v8
v_cvt_pk_bf16_f32 v36, v6, v7
v_mfma_f32_32x32x16_bf16 v[18:33], a[20:23], v[58:61], v[62:77]
v_exp_f32 v9, v9
v_exp_f32 v10, v10
v_cvt_pk_bf16_f32 v37, v8, v9
v_exp_f32 v11, v11
v_exp_f32 v12, v12
v_cvt_pk_bf16_f32 v38, v10, v11
v_mfma_f32_32x32x16_bf16 v[42:57], a[96:99], v[34:37], v[42:57]
v_exp_f32 v13, v13
v_exp_f32 v14, v14
v_cvt_pk_bf16_f32 v39, v12, v13
buffer_load_dwordx2 v[100:101], v108, s[8:11], s97 offen offset:0
v_exp_f32 v15, v15
v_exp_f32 v16, v16
v_cvt_pk_bf16_f32 v40, v14, v15
v_exp_f32 v17, v17
v_exp_f32 v18, v18
v_cvt_pk_bf16_f32 v41, v16, v17
v_exp_f32 v19, v19
v_exp_f32 v20, v20
v_mfma_f32_32x32x16_bf16 v[42:57], a[100:103], v[38:41], v[42:57]
v_cvt_pk_bf16_f32 v34, v18, v19
v_exp_f32 v21, v21
v_exp_f32 v22, v22
v_cvt_pk_bf16_f32 v35, v20, v21
buffer_load_dwordx2 v[102:103], v108, s[8:11], s97 offen offset:16
v_exp_f32 v23, v23
v_exp_f32 v24, v24
v_cvt_pk_bf16_f32 v36, v22, v23
v_mfma_f32_32x32x16_bf16 v[2:17], a[24:27], v[58:61], v[62:77]
v_exp_f32 v25, v25
v_exp_f32 v26, v26
v_cvt_pk_bf16_f32 v37, v24, v25
buffer_store_dwordx4 v[88:91], v1, s[44:47], s60 offen offset:1024 sc1
v_exp_f32 v27, v27
v_exp_f32 v28, v28
v_cvt_pk_bf16_f32 v38, v26, v27
v_mfma_f32_32x32x16_bf16 v[42:57], a[104:107], v[34:37], v[42:57]
v_exp_f32 v29, v29
v_exp_f32 v30, v30
v_cvt_pk_bf16_f32 v39, v28, v29
buffer_load_dwordx2 v[104:105], v108, s[8:11], s97 offen offset:32
v_exp_f32 v31, v31
v_exp_f32 v32, v32
v_cvt_pk_bf16_f32 v40, v30, v31
v_exp_f32 v33, v33
v_exp_f32 v2, v2
v_cvt_pk_bf16_f32 v41, v32, v33
v_exp_f32 v3, v3
v_exp_f32 v4, v4
v_mfma_f32_32x32x16_bf16 v[42:57], a[108:111], v[38:41], v[42:57]
v_cvt_pk_bf16_f32 v34, v2, v3
v_exp_f32 v5, v5
v_exp_f32 v6, v6
v_cvt_pk_bf16_f32 v35, v4, v5
buffer_load_dwordx2 v[106:107], v108, s[8:11], s97 offen offset:48
v_exp_f32 v7, v7
v_exp_f32 v8, v8
v_cvt_pk_bf16_f32 v36, v6, v7
v_mfma_f32_32x32x16_bf16 v[18:33], a[28:31], v[58:61], v[62:77]
v_exp_f32 v9, v9
v_exp_f32 v10, v10
v_cvt_pk_bf16_f32 v37, v8, v9
v_exp_f32 v11, v11
v_exp_f32 v12, v12
v_cvt_pk_bf16_f32 v38, v10, v11
v_mfma_f32_32x32x16_bf16 v[42:57], a[112:115], v[34:37], v[42:57]
v_exp_f32 v13, v13
v_exp_f32 v14, v14
v_cvt_pk_bf16_f32 v39, v12, v13
buffer_store_dwordx4 v[88:91], v1, s[44:47], s61 offen offset:0 sc1
v_exp_f32 v15, v15
v_exp_f32 v16, v16
v_cvt_pk_bf16_f32 v40, v14, v15
v_exp_f32 v17, v17
v_exp_f32 v18, v18
v_cvt_pk_bf16_f32 v41, v16, v17
v_exp_f32 v19, v19
v_exp_f32 v20, v20
v_mfma_f32_32x32x16_bf16 v[42:57], a[116:119], v[38:41], v[42:57]
v_cvt_pk_bf16_f32 v34, v18, v19
v_exp_f32 v21, v21
v_exp_f32 v22, v22
v_cvt_pk_bf16_f32 v35, v20, v21
v_exp_f32 v23, v23
v_exp_f32 v24, v24
v_cvt_pk_bf16_f32 v36, v22, v23
v_mfma_f32_32x32x16_bf16 v[2:17], a[32:35], v[58:61], v[62:77]
v_exp_f32 v25, v25
v_exp_f32 v26, v26
v_cvt_pk_bf16_f32 v37, v24, v25
v_exp_f32 v27, v27
v_exp_f32 v28, v28
v_cvt_pk_bf16_f32 v38, v26, v27
v_mfma_f32_32x32x16_bf16 v[42:57], a[120:123], v[34:37], v[42:57]
v_exp_f32 v29, v29
v_exp_f32 v30, v30
v_cvt_pk_bf16_f32 v39, v28, v29
v_exp_f32 v31, v31
v_exp_f32 v32, v32
v_cvt_pk_bf16_f32 v40, v30, v31
v_exp_f32 v33, v33
v_exp_f32 v2, v2
v_cvt_pk_bf16_f32 v41, v32, v33
buffer_store_dwordx4 v[88:91], v1, s[44:47], s61 offen offset:1024 sc1
v_exp_f32 v3, v3
v_exp_f32 v4, v4
v_mfma_f32_32x32x16_bf16 v[42:57], a[124:127], v[38:41], v[42:57]
v_cvt_pk_bf16_f32 v34, v2, v3
v_exp_f32 v5, v5
v_exp_f32 v6, v6
v_cvt_pk_bf16_f32 v35, v4, v5
v_exp_f32 v7, v7
v_exp_f32 v8, v8
v_cvt_pk_bf16_f32 v36, v6, v7
v_mfma_f32_32x32x16_bf16 v[18:33], a[36:39], v[58:61], v[62:77]
v_exp_f32 v9, v9
v_exp_f32 v10, v10
v_cvt_pk_bf16_f32 v37, v8, v9
v_exp_f32 v11, v11
v_exp_f32 v12, v12
v_cvt_pk_bf16_f32 v38, v10, v11
v_mfma_f32_32x32x16_bf16 v[42:57], a[128:131], v[34:37], v[42:57]
v_exp_f32 v13, v13
v_exp_f32 v14, v14
v_cvt_pk_bf16_f32 v39, v12, v13
v_exp_f32 v15, v15
v_exp_f32 v16, v16
v_cvt_pk_bf16_f32 v40, v14, v15
v_exp_f32 v17, v17
v_exp_f32 v18, v18
v_cvt_pk_bf16_f32 v41, v16, v17
v_exp_f32 v19, v19
v_exp_f32 v20, v20
v_mfma_f32_32x32x16_bf16 v[42:57], a[132:135], v[38:41], v[42:57]
v_cvt_pk_bf16_f32 v34, v18, v19
v_exp_f32 v21, v21
v_exp_f32 v22, v22
v_cvt_pk_bf16_f32 v35, v20, v21
buffer_store_dword v88, v92, s[44:47], s62 offen sc1
v_exp_f32 v23, v23
v_exp_f32 v24, v24
v_cvt_pk_bf16_f32 v36, v22, v23
v_mfma_f32_32x32x16_bf16 v[2:17], a[40:43], v[58:61], v[62:77]
v_exp_f32 v25, v25
v_exp_f32 v26, v26
v_cvt_pk_bf16_f32 v37, v24, v25
v_exp_f32 v27, v27
v_exp_f32 v28, v28
v_cvt_pk_bf16_f32 v38, v26, v27
v_mfma_f32_32x32x16_bf16 v[42:57], a[136:139], v[34:37], v[42:57]
v_exp_f32 v29, v29
v_exp_f32 v30, v30
v_cvt_pk_bf16_f32 v39, v28, v29
v_exp_f32 v31, v31
v_exp_f32 v32, v32
v_cvt_pk_bf16_f32 v40, v30, v31
v_exp_f32 v33, v33
v_exp_f32 v2, v2
v_cvt_pk_bf16_f32 v41, v32, v33
v_exp_f32 v3, v3
v_exp_f32 v4, v4
v_mfma_f32_32x32x16_bf16 v[42:57], a[140:143], v[38:41], v[42:57]
v_cvt_pk_bf16_f32 v34, v2, v3
v_exp_f32 v5, v5
v_exp_f32 v6, v6
v_cvt_pk_bf16_f32 v35, v4, v5
v_exp_f32 v7, v7
v_exp_f32 v8, v8
v_cvt_pk_bf16_f32 v36, v6, v7
v_mfma_f32_32x32x16_bf16 v[18:33], a[44:47], v[58:61], v[62:77]
v_exp_f32 v9, v9
v_exp_f32 v10, v10
v_cvt_pk_bf16_f32 v37, v8, v9
buffer_store_dwordx4 v[88:91], v1, s[44:47], s92 offen offset:0 sc1
v_exp_f32 v11, v11
v_exp_f32 v12, v12
v_cvt_pk_bf16_f32 v38, v10, v11
v_mfma_f32_32x32x16_bf16 v[42:57], a[144:147], v[34:37], v[42:57]
v_exp_f32 v13, v13
v_exp_f32 v14, v14
v_cvt_pk_bf16_f32 v39, v12, v13
v_exp_f32 v15, v15
v_exp_f32 v16, v16
v_cvt_pk_bf16_f32 v40, v14, v15
v_exp_f32 v17, v17
v_exp_f32 v18, v18
v_cvt_pk_bf16_f32 v41, v16, v17
v_exp_f32 v19, v19
v_exp_f32 v20, v20
v_mfma_f32_32x32x16_bf16 v[42:57], a[148:151], v[38:41], v[42:57]
v_cvt_pk_bf16_f32 v34, v18, v19
v_exp_f32 v21, v21
v_exp_f32 v22, v22
v_cvt_pk_bf16_f32 v35, v20, v21
v_exp_f32 v23, v23
v_exp_f32 v24, v24
v_cvt_pk_bf16_f32 v36, v22, v23
v_mfma_f32_32x32x16_bf16 v[2:17], a[48:51], v[58:61], v[62:77]
v_exp_f32 v25, v25
v_exp_f32 v26, v26
v_cvt_pk_bf16_f32 v37, v24, v25
v_exp_f32 v27, v27
v_exp_f32 v28, v28
v_cvt_pk_bf16_f32 v38, v26, v27
v_mfma_f32_32x32x16_bf16 v[42:57], a[152:155], v[34:37], v[42:57]
v_exp_f32 v29, v29
v_exp_f32 v30, v30
v_cvt_pk_bf16_f32 v39, v28, v29
buffer_store_dwordx4 v[88:91], v1, s[44:47], s92 offen offset:1024 sc1
v_mov_b32 v232, 0x3f803f80
v_exp_f32 v31, v31
v_exp_f32 v32, v32
v_cvt_pk_bf16_f32 v40, v30, v31
v_mov_b32 v233, 0x3f803f80
v_exp_f32 v33, v33
v_exp_f32 v2, v2
v_cvt_pk_bf16_f32 v41, v32, v33
v_exp_f32 v3, v3
v_exp_f32 v4, v4
v_mfma_f32_32x32x16_bf16 v[42:57], a[156:159], v[38:41], v[42:57]
v_cvt_pk_bf16_f32 v34, v2, v3
v_mov_b32 v234, 0x3f803f80
v_exp_f32 v5, v5
v_exp_f32 v6, v6
v_cvt_pk_bf16_f32 v35, v4, v5
v_mov_b32 v235, 0x3f803f80
v_exp_f32 v7, v7
v_exp_f32 v8, v8
v_cvt_pk_bf16_f32 v36, v6, v7
v_mfma_f32_32x32x16_bf16 v[18:33], a[52:55], v[58:61], v[62:77]
v_exp_f32 v9, v9
v_exp_f32 v10, v10
v_cvt_pk_bf16_f32 v37, v8, v9
s_waitcnt vmcnt(16)
v_mfma_f32_32x32x16_bf16 v[128:143], a[192:195], a[216:219], 0
v_exp_f32 v11, v11
v_exp_f32 v12, v12
v_cvt_pk_bf16_f32 v38, v10, v11
s_waitcnt vmcnt(14)
v_mfma_f32_32x32x16_bf16 v[144:159], a[200:203], a[216:219], 0
v_mfma_f32_32x32x16_bf16 v[42:57], a[160:163], v[34:37], v[42:57]
v_exp_f32 v13, v13
v_exp_f32 v14, v14
v_cvt_pk_bf16_f32 v39, v12, v13
v_exp_f32 v15, v15
v_exp_f32 v16, v16
v_cvt_pk_bf16_f32 v40, v14, v15
s_waitcnt vmcnt(12)
v_mfma_f32_32x32x16_bf16 v[160:175], a[208:211], a[216:219], 0
v_exp_f32 v17, v17
v_exp_f32 v18, v18
v_cvt_pk_bf16_f32 v41, v16, v17
buffer_store_dwordx4 v[88:91], v1, s[44:47], s92 offen offset:2048 sc1
v_exp_f32 v19, v19
v_exp_f32 v20, v20
v_mfma_f32_32x32x16_bf16 v[42:57], a[164:167], v[38:41], v[42:57]
v_cvt_pk_bf16_f32 v34, v18, v19
v_mfma_f32_32x32x16_bf16 v[128:143], a[196:199], a[220:223], v[128:143]
v_exp_f32 v21, v21
v_exp_f32 v22, v22
v_cvt_pk_bf16_f32 v35, v20, v21
v_mfma_f32_32x32x16_bf16 v[144:159], a[204:207], a[220:223], v[144:159]
v_exp_f32 v23, v23
v_exp_f32 v24, v24
v_cvt_pk_bf16_f32 v36, v22, v23
v_mfma_f32_32x32x16_bf16 v[2:17], a[56:59], v[58:61], v[62:77]
v_exp_f32 v25, v25
v_exp_f32 v26, v26
v_cvt_pk_bf16_f32 v37, v24, v25
s_waitcnt vmcnt(12)
v_mfma_f32_32x32x16_bf16 v[160:175], a[212:215], a[220:223], v[160:175]
v_exp_f32 v27, v27
v_exp_f32 v28, v28
v_cvt_pk_bf16_f32 v38, v26, v27
v_cvt_pk_bf16_f32 v176, v128, v129
v_mfma_f32_32x32x16_bf16 v[42:57], a[168:171], v[34:37], v[42:57]
v_exp_f32 v29, v29
v_exp_f32 v30, v30
v_cvt_pk_bf16_f32 v39, v28, v29
v_exp_f32 v31, v31
v_exp_f32 v32, v32
v_cvt_pk_bf16_f32 v40, v30, v31
v_cvt_pk_bf16_f32 v177, v130, v131
v_exp_f32 v33, v33
v_exp_f32 v2, v2
v_cvt_pk_bf16_f32 v41, v32, v33
v_cvt_pk_bf16_f32 v178, v132, v133
v_exp_f32 v3, v3
v_exp_f32 v4, v4
v_mfma_f32_32x32x16_bf16 v[42:57], a[172:175], v[38:41], v[42:57]
v_cvt_pk_bf16_f32 v34, v2, v3
v_exp_f32 v5, v5
v_exp_f32 v6, v6
v_cvt_pk_bf16_f32 v35, v4, v5
buffer_store_dwordx4 v[88:91], v1, s[44:47], s92 offen offset:3072 sc1
v_cvt_pk_bf16_f32 v179, v134, v135
v_exp_f32 v7, v7
v_exp_f32 v8, v8
v_cvt_pk_bf16_f32 v36, v6, v7
v_mfma_f32_32x32x16_bf16 v[18:33], a[60:63], v[58:61], v[62:77]
ds_read_b128 v[58:61], v78 offset:3072
s_cmp_lg_u32 s69, 0
s_cbranch_scc1 negslow_20
negback_21:
v_exp_f32 v9, v9
v_exp_f32 v10, v10
v_cvt_pk_bf16_f32 v37, v8, v9
v_cvt_pk_bf16_f32 v180, v136, v137
v_exp_f32 v11, v11
v_exp_f32 v12, v12
v_cvt_pk_bf16_f32 v38, v10, v11
v_cvt_pk_bf16_f32 v181, v138, v139
v_mfma_f32_32x32x16_bf16 v[42:57], a[176:179], v[34:37], v[42:57]
v_exp_f32 v13, v13
v_exp_f32 v14, v14
v_cvt_pk_bf16_f32 v39, v12, v13
v_exp_f32 v15, v15
v_exp_f32 v16, v16
v_cvt_pk_bf16_f32 v40, v14, v15
v_cvt_pk_bf16_f32 v182, v140, v141
v_exp_f32 v17, v17
v_exp_f32 v18, v18
v_cvt_pk_bf16_f32 v41, v16, v17
v_cvt_pk_bf16_f32 v183, v142, v143
v_exp_f32 v19, v19
v_exp_f32 v20, v20
v_mfma_f32_32x32x16_bf16 v[42:57], a[180:183], v[38:41], v[42:57]
v_cvt_pk_bf16_f32 v34, v18, v19
v_exp_f32 v21, v21
v_exp_f32 v22, v22
v_cvt_pk_bf16_f32 v35, v20, v21
v_cvt_pk_bf16_f32 v184, v144, v145
v_exp_f32 v23, v23
v_exp_f32 v24, v24
v_cvt_pk_bf16_f32 v36, v22, v23
s_waitcnt lgkmcnt(0)
v_mfma_f32_32x32x16_bf16 v[2:17], a[0:3], v[58:61], v[62:77]
v_exp_f32 v25, v25
v_exp_f32 v26, v26
v_cvt_pk_bf16_f32 v37, v24, v25
buffer_store_dwordx4 v[88:91], v1, s[44:47], s66 offen offset:0 sc1
v_cvt_pk_bf16_f32 v185, v146, v147
v_exp_f32 v27, v27
v_exp_f32 v28, v28
v_cvt_pk_bf16_f32 v38, v26, v27
v_cvt_pk_bf16_f32 v186, v148, v149
v_mfma_f32_32x32x16_bf16 v[42:57], a[184:187], v[34:37], v[42:57]
v_exp_f32 v29, v29
v_exp_f32 v30, v30
v_cvt_pk_bf16_f32 v39, v28, v29
v_exp_f32 v31, v31
v_exp_f32 v32, v32
v_cvt_pk_bf16_f32 v40, v30, v31
v_cvt_pk_bf16_f32 v187, v150, v151
v_exp_f32 v33, v33
v_exp_f32 v2, v2
v_cvt_pk_bf16_f32 v41, v32, v33
v_cvt_pk_bf16_f32 v188, v152, v153
v_exp_f32 v3, v3
v_exp_f32 v4, v4
v_mfma_f32_32x32x16_bf16 v[42:57], a[188:191], v[38:41], v[42:57]
v_cvt_pk_bf16_f32 v34, v2, v3
v_exp_f32 v5, v5
v_exp_f32 v6, v6
v_cvt_pk_bf16_f32 v35, v4, v5
v_cvt_pk_bf16_f32 v189, v154, v155
v_exp_f32 v7, v7
v_exp_f32 v8, v8
v_cvt_pk_bf16_f32 v36, v6, v7
v_cvt_pk_bf16_f32 v190, v156, v157
v_mfma_f32_32x32x16_bf16 v[18:33], a[4:7], v[58:61], v[62:77]
v_exp_f32 v9, v9
v_exp_f32 v10, v10
v_cvt_pk_bf16_f32 v37, v8, v9
v_exp_f32 v11, v11
v_exp_f32 v12, v12
v_cvt_pk_bf16_f32 v38, v10, v11
v_cvt_pk_bf16_f32 v191, v158, v159
ds_write_b128 v79, v[42:45] offset:18432
ds_write_b128 v79, v[46:49] offset:19456
ds_write_b32 v80, v50 offset:18432
v_mfma_f32_32x32x16_bf16 v[42:57], a[64:67], v[34:37], 0
v_exp_f32 v13, v13
v_exp_f32 v14, v14
v_cvt_pk_bf16_f32 v39, v12, v13
buffer_store_dwordx4 v[88:91], v1, s[44:47], s66 offen offset:1024 sc1
v_exp_f32 v15, v15
v_exp_f32 v16, v16
v_cvt_pk_bf16_f32 v40, v14, v15
v_cvt_pk_bf16_f32 v192, v160, v161
v_exp_f32 v17, v17
v_exp_f32 v18, v18
v_cvt_pk_bf16_f32 v41, v16, v17
v_cvt_pk_bf16_f32 v193, v162, v163
v_exp_f32 v19, v19
v_exp_f32 v20, v20
v_mfma_f32_32x32x16_bf16 v[42:57], a[68:71], v[38:41], v[42:57]
v_cvt_pk_bf16_f32 v34, v18, v19
v_exp_f32 v21, v21
v_exp_f32 v22, v22
v_cvt_pk_bf16_f32 v35, v20, v21
v_cvt_pk_bf16_f32 v194, v164, v165
v_exp_f32 v23, v23
v_exp_f32 v24, v24
v_cvt_pk_bf16_f32 v36, v22, v23
v_cvt_pk_bf16_f32 v195, v166, v167
v_mfma_f32_32x32x16_bf16 v[2:17], a[8:11], v[58:61], v[62:77]
v_exp_f32 v25, v25
v_exp_f32 v26, v26
v_cvt_pk_bf16_f32 v37, v24, v25
v_exp_f32 v27, v27
v_exp_f32 v28, v28
v_cvt_pk_bf16_f32 v38, v26, v27
v_cvt_pk_bf16_f32 v196, v168, v169
v_mfma_f32_32x32x16_bf16 v[42:57], a[72:75], v[34:37], v[42:57]
v_exp_f32 v29, v29
v_exp_f32 v30, v30
v_cvt_pk_bf16_f32 v39, v28, v29
v_cvt_pk_bf16_f32 v197, v170, v171
v_exp_f32 v31, v31
v_exp_f32 v32, v32
v_cvt_pk_bf16_f32 v40, v30, v31
v_exp_f32 v33, v33
v_exp_f32 v2, v2
v_cvt_pk_bf16_f32 v41, v32, v33
buffer_store_dwordx4 v[88:91], v1, s[44:47], s66 offen offset:2048 sc1
v_cvt_pk_bf16_f32 v198, v172, v173
v_exp_f32 v3, v3
v_exp_f32 v4, v4
v_mfma_f32_32x32x16_bf16 v[42:57], a[76:79], v[38:41], v[42:57]
v_cvt_pk_bf16_f32 v34, v2, v3
v_exp_f32 v5, v5
v_exp_f32 v6, v6
v_cvt_pk_bf16_f32 v35, v4, v5
v_cvt_pk_bf16_f32 v199, v174, v175
v_exp_f32 v7, v7
v_exp_f32 v8, v8
v_cvt_pk_bf16_f32 v36, v6, v7
v_mfma_f32_32x32x16_bf16 v[200:215], v[232:235], v[176:179], 0
v_mfma_f32_32x32x16_bf16 v[18:33], a[12:15], v[58:61], v[62:77]
v_exp_f32 v9, v9
v_exp_f32 v10, v10
v_cvt_pk_bf16_f32 v37, v8, v9
v_exp_f32 v11, v11
v_exp_f32 v12, v12
v_cvt_pk_bf16_f32 v38, v10, v11
v_mfma_f32_32x32x16_bf16 v[216:231], v[232:235], v[184:187], 0
v_mfma_f32_32x32x16_bf16 v[42:57], a[80:83], v[34:37], v[42:57]
v_exp_f32 v13, v13
v_exp_f32 v14, v14
v_cvt_pk_bf16_f32 v39, v12, v13
v_mfma_f32_32x32x16_bf16 v[200:215], v[232:235], v[180:183], v[200:215]
v_exp_f32 v15, v15
v_exp_f32 v16, v16
v_cvt_pk_bf16_f32 v40, v14, v15
v_exp_f32 v17, v17
v_exp_f32 v18, v18
v_cvt_pk_bf16_f32 v41, v16, v17
v_mfma_f32_32x32x16_bf16 v[216:231], v[232:235], v[188:191], v[216:231]
v_exp_f32 v19, v19
v_exp_f32 v20, v20
v_mfma_f32_32x32x16_bf16 v[42:57], a[84:87], v[38:41], v[42:57]
v_cvt_pk_bf16_f32 v34, v18, v19
v_exp_f32 v21, v21
v_exp_f32 v22, v22
v_cvt_pk_bf16_f32 v35, v20, v21
buffer_store_dwordx4 v[176:179], v1, s[44:47], s76 offen sc1
v_exp_f32 v23, v23
v_exp_f32 v24, v24
v_cvt_pk_bf16_f32 v36, v22, v23
v_mfma_f32_32x32x16_bf16 v[2:17], a[16:19], v[58:61], v[62:77]
v_exp_f32 v25, v25
v_exp_f32 v26, v26
v_cvt_pk_bf16_f32 v37, v24, v25
v_exp_f32 v27, v27
v_exp_f32 v28, v28
v_cvt_pk_bf16_f32 v38, v26, v27
v_mfma_f32_32x32x16_bf16 v[42:57], a[88:91], v[34:37], v[42:57]
v_exp_f32 v29, v29
v_exp_f32 v30, v30
v_cvt_pk_bf16_f32 v39, v28, v29
v_exp_f32 v31, v31
v_exp_f32 v32, v32
v_cvt_pk_bf16_f32 v40, v30, v31
v_exp_f32 v33, v33
v_exp_f32 v2, v2
v_cvt_pk_bf16_f32 v41, v32, v33
v_exp_f32 v3, v3
v_exp_f32 v4, v4
v_mfma_f32_32x32x16_bf16 v[42:57], a[92:95], v[38:41], v[42:57]
v_cvt_pk_bf16_f32 v34, v2, v3
buffer_store_dwordx4 v[180:183], v1, s[44:47], s76 offen offset:1024 sc1
v_exp_f32 v5, v5
v_exp_f32 v6, v6
v_cvt_pk_bf16_f32 v35, v4, v5
v_exp_f32 v7, v7
v_exp_f32 v8, v8
v_cvt_pk_bf16_f32 v36, v6, v7
v_mfma_f32_32x32x16_bf16 v[18:33], a[20:23], v[58:61], v[62:77]
v_exp_f32 v9, v9
v_exp_f32 v10, v10
v_cvt_pk_bf16_f32 v37, v8, v9
v_exp_f32 v11, v11
v_exp_f32 v12, v12
v_cvt_pk_bf16_f32 v38, v10, v11
v_mfma_f32_32x32x16_bf16 v[42:57], a[96:99], v[34:37], v[42:57]
v_exp_f32 v13, v13
v_exp_f32 v14, v14
v_cvt_pk_bf16_f32 v39, v12, v13
v_exp_f32 v15, v15
v_exp_f32 v16, v16
v_cvt_pk_bf16_f32 v40, v14, v15
v_exp_f32 v17, v17
v_exp_f32 v18, v18
v_cvt_pk_bf16_f32 v41, v16, v17
v_exp_f32 v19, v19
v_exp_f32 v20, v20
v_mfma_f32_32x32x16_bf16 v[42:57], a[100:103], v[38:41], v[42:57]
v_cvt_pk_bf16_f32 v34, v18, v19
buffer_store_dwordx4 v[184:187], v1, s[44:47], s77 offen sc1
v_exp_f32 v21, v21
v_exp_f32 v22, v22
v_cvt_pk_bf16_f32 v35, v20, v21
v_exp_f32 v23, v23
v_exp_f32 v24, v24
v_cvt_pk_bf16_f32 v36, v22, v23
v_mfma_f32_32x32x16_bf16 v[2:17], a[24:27], v[58:61], v[62:77]
v_exp_f32 v25, v25
v_exp_f32 v26, v26
v_cvt_pk_bf16_f32 v37, v24, v25
v_exp_f32 v27, v27
v_exp_f32 v28, v28
v_cvt_pk_bf16_f32 v38, v26, v27
v_mfma_f32_32x32x16_bf16 v[42:57], a[104:107], v[34:37], v[42:57]
v_exp_f32 v29, v29
v_exp_f32 v30, v30
v_cvt_pk_bf16_f32 v39, v28, v29
v_exp_f32 v31, v31
v_exp_f32 v32, v32
v_cvt_pk_bf16_f32 v40, v30, v31
v_exp_f32 v33, v33
v_exp_f32 v2, v2
v_cvt_pk_bf16_f32 v41, v32, v33
buffer_store_dwordx4 v[188:191], v1, s[44:47], s77 offen offset:1024 sc1
v_exp_f32 v3, v3
v_exp_f32 v4, v4
v_mfma_f32_32x32x16_bf16 v[42:57], a[108:111], v[38:41], v[42:57]
v_cvt_pk_bf16_f32 v34, v2, v3
v_exp_f32 v5, v5
v_exp_f32 v6, v6
v_cvt_pk_bf16_f32 v35, v4, v5
v_exp_f32 v7, v7
v_exp_f32 v8, v8
v_cvt_pk_bf16_f32 v36, v6, v7
v_mfma_f32_32x32x16_bf16 v[18:33], a[28:31], v[58:61], v[62:77]
v_exp_f32 v9, v9
v_exp_f32 v10, v10
v_cvt_pk_bf16_f32 v37, v8, v9
v_exp_f32 v11, v11
v_exp_f32 v12, v12
v_cvt_pk_bf16_f32 v38, v10, v11
v_mfma_f32_32x32x16_bf16 v[42:57], a[112:115], v[34:37], v[42:57]
v_exp_f32 v13, v13
v_exp_f32 v14, v14
v_cvt_pk_bf16_f32 v39, v12, v13
v_exp_f32 v15, v15
v_exp_f32 v16, v16
v_cvt_pk_bf16_f32 v40, v14, v15
v_exp_f32 v17, v17
v_exp_f32 v18, v18
v_cvt_pk_bf16_f32 v41, v16, v17
s_cmp_lt_u32 s80, 56
s_cbranch_scc0 ptnoc_9
buffer_store_dwordx4 v[192:195], v1, s[44:47], s78 offen sc1
buffer_store_dwordx4 v[196:199], v1, s[44:47], s78 offen offset:2048 sc1
ptnoc_9:
v_exp_f32 v19, v19
v_exp_f32 v20, v20
v_mfma_f32_32x32x16_bf16 v[42:57], a[116:119], v[38:41], v[42:57]
v_cvt_pk_bf16_f32 v34, v18, v19
v_exp_f32 v21, v21
v_exp_f32 v22, v22
v_cvt_pk_bf16_f32 v35, v20, v21
v_exp_f32 v23, v23
v_exp_f32 v24, v24
v_cvt_pk_bf16_f32 v36, v22, v23
v_mfma_f32_32x32x16_bf16 v[2:17], a[32:35], v[58:61], v[62:77]
v_exp_f32 v25, v25
v_exp_f32 v26, v26
v_cvt_pk_bf16_f32 v37, v24, v25
v_exp_f32 v27, v27
v_exp_f32 v28, v28
v_cvt_pk_bf16_f32 v38, v26, v27
v_mfma_f32_32x32x16_bf16 v[42:57], a[120:123], v[34:37], v[42:57]
v_exp_f32 v29, v29
v_exp_f32 v30, v30
v_cvt_pk_bf16_f32 v39, v28, v29
v_exp_f32 v31, v31
v_exp_f32 v32, v32
v_cvt_pk_bf16_f32 v40, v30, v31
v_lshlrev_b32 v108, 2, v0
v_exp_f32 v33, v33
v_exp_f32 v2, v2
v_cvt_pk_bf16_f32 v41, v32, v33
v_exp_f32 v3, v3
v_exp_f32 v4, v4
v_mfma_f32_32x32x16_bf16 v[42:57], a[124:127], v[38:41], v[42:57]
v_cvt_pk_bf16_f32 v34, v2, v3
v_exp_f32 v5, v5
v_exp_f32 v6, v6
v_cvt_pk_bf16_f32 v35, v4, v5
v_exp_f32 v7, v7
v_exp_f32 v8, v8
v_cvt_pk_bf16_f32 v36, v6, v7
v_mfma_f32_32x32x16_bf16 v[18:33], a[36:39], v[58:61], v[62:77]
v_exp_f32 v9, v9
v_exp_f32 v10, v10
v_cvt_pk_bf16_f32 v37, v8, v9
v_exp_f32 v11, v11
v_exp_f32 v12, v12
v_cvt_pk_bf16_f32 v38, v10, v11
v_mfma_f32_32x32x16_bf16 v[42:57], a[128:131], v[34:37], v[42:57]
v_exp_f32 v13, v13
v_exp_f32 v14, v14
v_cvt_pk_bf16_f32 v39, v12, v13
v_exp_f32 v15, v15
v_exp_f32 v16, v16
v_cvt_pk_bf16_f32 v40, v14, v15
s_mov_b64 exec, 0xffffffff
buffer_store_dword v200, v108, s[44:47], s79 offen sc1
s_mov_b64 exec, -1
v_exp_f32 v17, v17
v_exp_f32 v18, v18
v_cvt_pk_bf16_f32 v41, v16, v17
v_exp_f32 v19, v19
v_exp_f32 v20, v20
v_mfma_f32_32x32x16_bf16 v[42:57], a[132:135], v[38:41], v[42:57]
v_cvt_pk_bf16_f32 v34, v18, v19
v_exp_f32 v21, v21
v_exp_f32 v22, v22
v_cvt_pk_bf16_f32 v35, v20, v21
v_exp_f32 v23, v23
v_exp_f32 v24, v24
v_cvt_pk_bf16_f32 v36, v22, v23
v_mfma_f32_32x32x16_bf16 v[2:17], a[40:43], v[58:61], v[62:77]
v_exp_f32 v25, v25
v_exp_f32 v26, v26
v_cvt_pk_bf16_f32 v37, v24, v25
v_exp_f32 v27, v27
v_exp_f32 v28, v28
v_cvt_pk_bf16_f32 v38, v26, v27
v_mfma_f32_32x32x16_bf16 v[42:57], a[136:139], v[34:37], v[42:57]
v_exp_f32 v29, v29
v_exp_f32 v30, v30
v_cvt_pk_bf16_f32 v39, v28, v29
s_cmp_eq_u32 s49, 3
s_cbranch_scc1 ptnocs_10
s_mov_b64 exec, 0xffffffff
buffer_store_dword v216, v108, s[44:47], s79 offen offset:512 sc1
s_mov_b64 exec, -1
ptnocs_10:
v_exp_f32 v31, v31
v_exp_f32 v32, v32
v_cvt_pk_bf16_f32 v40, v30, v31
v_exp_f32 v33, v33
v_exp_f32 v2, v2
v_cvt_pk_bf16_f32 v41, v32, v33
v_exp_f32 v3, v3
v_exp_f32 v4, v4
v_mfma_f32_32x32x16_bf16 v[42:57], a[140:143], v[38:41], v[42:57]
v_cvt_pk_bf16_f32 v34, v2, v3
v_exp_f32 v5, v5
v_exp_f32 v6, v6
v_cvt_pk_bf16_f32 v35, v4, v5
v_exp_f32 v7, v7
v_exp_f32 v8, v8
v_cvt_pk_bf16_f32 v36, v6, v7
v_mfma_f32_32x32x16_bf16 v[18:33], a[44:47], v[58:61], v[62:77]
v_exp_f32 v9, v9
v_exp_f32 v10, v10
v_cvt_pk_bf16_f32 v37, v8, v9
v_exp_f32 v11, v11
v_exp_f32 v12, v12
v_cvt_pk_bf16_f32 v38, v10, v11
v_mfma_f32_32x32x16_bf16 v[42:57], a[144:147], v[34:37], v[42:57]
v_exp_f32 v13, v13
v_exp_f32 v14, v14
v_cvt_pk_bf16_f32 v39, v12, v13
v_exp_f32 v15, v15
v_exp_f32 v16, v16
v_cvt_pk_bf16_f32 v40, v14, v15
v_exp_f32 v17, v17
v_exp_f32 v18, v18
v_cvt_pk_bf16_f32 v41, v16, v17
v_exp_f32 v19, v19
v_exp_f32 v20, v20
v_mfma_f32_32x32x16_bf16 v[42:57], a[148:151], v[38:41], v[42:57]
v_cvt_pk_bf16_f32 v34, v18, v19
v_exp_f32 v21, v21
v_exp_f32 v22, v22
v_cvt_pk_bf16_f32 v35, v20, v21
v_exp_f32 v23, v23
v_exp_f32 v24, v24
v_cvt_pk_bf16_f32 v36, v22, v23
v_mfma_f32_32x32x16_bf16 v[2:17], a[48:51], v[58:61], v[62:77]
v_exp_f32 v25, v25
v_exp_f32 v26, v26
v_cvt_pk_bf16_f32 v37, v24, v25
v_exp_f32 v27, v27
v_exp_f32 v28, v28
v_cvt_pk_bf16_f32 v38, v26, v27
v_mfma_f32_32x32x16_bf16 v[42:57], a[152:155], v[34:37], v[42:57]
v_exp_f32 v29, v29
v_exp_f32 v30, v30
v_cvt_pk_bf16_f32 v39, v28, v29
v_exp_f32 v31, v31
v_exp_f32 v32, v32
v_cvt_pk_bf16_f32 v40, v30, v31
v_exp_f32 v33, v33
v_exp_f32 v2, v2
v_cvt_pk_bf16_f32 v41, v32, v33
v_exp_f32 v3, v3
v_exp_f32 v4, v4
v_mfma_f32_32x32x16_bf16 v[42:57], a[156:159], v[38:41], v[42:57]
v_cvt_pk_bf16_f32 v34, v2, v3
v_exp_f32 v5, v5
v_exp_f32 v6, v6
v_cvt_pk_bf16_f32 v35, v4, v5
v_exp_f32 v7, v7
v_exp_f32 v8, v8
v_cvt_pk_bf16_f32 v36, v6, v7
v_mfma_f32_32x32x16_bf16 v[18:33], a[52:55], v[58:61], v[62:77]
v_exp_f32 v9, v9
v_exp_f32 v10, v10
v_cvt_pk_bf16_f32 v37, v8, v9
v_exp_f32 v11, v11
v_exp_f32 v12, v12
v_cvt_pk_bf16_f32 v38, v10, v11
v_mfma_f32_32x32x16_bf16 v[42:57], a[160:163], v[34:37], v[42:57]
v_exp_f32 v13, v13
v_exp_f32 v14, v14
v_cvt_pk_bf16_f32 v39, v12, v13
v_exp_f32 v15, v15
v_exp_f32 v16, v16
v_cvt_pk_bf16_f32 v40, v14, v15
v_exp_f32 v17, v17
v_exp_f32 v18, v18
v_cvt_pk_bf16_f32 v41, v16, v17
v_exp_f32 v19, v19
v_exp_f32 v20, v20
v_mfma_f32_32x32x16_bf16 v[42:57], a[164:167], v[38:41], v[42:57]
v_cvt_pk_bf16_f32 v34, v18, v19
v_exp_f32 v21, v21
v_exp_f32 v22, v22
v_cvt_pk_bf16_f32 v35, v20, v21
v_exp_f32 v23, v23
v_exp_f32 v24, v24
v_cvt_pk_bf16_f32 v36, v22, v23
v_mfma_f32_32x32x16_bf16 v[2:17], a[56:59], v[58:61], v[62:77]
v_exp_f32 v25, v25
v_exp_f32 v26, v26
v_cvt_pk_bf16_f32 v37, v24, v25
v_exp_f32 v27, v27
v_exp_f32 v28, v28
v_cvt_pk_bf16_f32 v38, v26, v27
v_mfma_f32_32x32x16_bf16 v[42:57], a[168:171], v[34:37], v[42:57]
v_exp_f32 v29, v29
v_exp_f32 v30, v30
v_cvt_pk_bf16_f32 v39, v28, v29
v_exp_f32 v31, v31
v_exp_f32 v32, v32
v_cvt_pk_bf16_f32 v40, v30, v31
v_exp_f32 v33, v33
v_exp_f32 v2, v2
v_cvt_pk_bf16_f32 v41, v32, v33
v_exp_f32 v3, v3
v_exp_f32 v4, v4
v_mfma_f32_32x32x16_bf16 v[42:57], a[172:175], v[38:41], v[42:57]
v_cvt_pk_bf16_f32 v34, v2, v3
v_exp_f32 v5, v5
v_exp_f32 v6, v6
v_cvt_pk_bf16_f32 v35, v4, v5
v_exp_f32 v7, v7
v_exp_f32 v8, v8
v_cvt_pk_bf16_f32 v36, v6, v7
v_mfma_f32_32x32x16_bf16 v[18:33], a[60:63], v[58:61], v[62:77]
ds_read_b128 v[58:61], v78 offset:4096
s_cmp_lg_u32 s69, 0
s_cbranch_scc1 negslow_22
negback_23:
v_exp_f32 v9, v9
v_exp_f32 v10, v10
v_cvt_pk_bf16_f32 v37, v8, v9
v_exp_f32 v11, v11
v_exp_f32 v12, v12
v_cvt_pk_bf16_f32 v38, v10, v11
v_mfma_f32_32x32x16_bf16 v[42:57], a[176:179], v[34:37], v[42:57]
v_exp_f32 v13, v13
v_exp_f32 v14, v14
v_cvt_pk_bf16_f32 v39, v12, v13
v_exp_f32 v15, v15
v_exp_f32 v16, v16
v_cvt_pk_bf16_f32 v40, v14, v15
v_exp_f32 v17, v17
v_exp_f32 v18, v18
v_cvt_pk_bf16_f32 v41, v16, v17
v_exp_f32 v19, v19
v_exp_f32 v20, v20
v_mfma_f32_32x32x16_bf16 v[42:57], a[180:183], v[38:41], v[42:57]
v_cvt_pk_bf16_f32 v34, v18, v19
v_exp_f32 v21, v21
v_exp_f32 v22, v22
v_cvt_pk_bf16_f32 v35, v20, v21
v_exp_f32 v23, v23
v_exp_f32 v24, v24
v_cvt_pk_bf16_f32 v36, v22, v23
s_waitcnt lgkmcnt(0)
v_mfma_f32_32x32x16_bf16 v[2:17], a[0:3], v[58:61], v[62:77]
v_exp_f32 v25, v25
v_exp_f32 v26, v26
v_cvt_pk_bf16_f32 v37, v24, v25
v_exp_f32 v27, v27
v_exp_f32 v28, v28
v_cvt_pk_bf16_f32 v38, v26, v27
v_mfma_f32_32x32x16_bf16 v[42:57], a[184:187], v[34:37], v[42:57]
v_exp_f32 v29, v29
v_exp_f32 v30, v30
v_cvt_pk_bf16_f32 v39, v28, v29
v_mov_b32 v108, s52
v_exp_f32 v31, v31
v_exp_f32 v32, v32
v_cvt_pk_bf16_f32 v40, v30, v31
v_exp_f32 v33, v33
v_exp_f32 v2, v2
v_cvt_pk_bf16_f32 v41, v32, v33
v_mov_b32 v109, s53
v_exp_f32 v3, v3
v_exp_f32 v4, v4
v_mfma_f32_32x32x16_bf16 v[42:57], a[188:191], v[38:41], v[42:57]
v_cvt_pk_bf16_f32 v34, v2, v3
v_exp_f32 v5, v5
v_exp_f32 v6, v6
v_cvt_pk_bf16_f32 v35, v4, v5
s_waitcnt vmcnt(0)
v_exp_f32 v7, v7
v_exp_f32 v8, v8
v_cvt_pk_bf16_f32 v36, v6, v7
v_mfma_f32_32x32x16_bf16 v[18:33], a[4:7], v[58:61], v[62:77]
v_exp_f32 v9, v9
v_exp_f32 v10, v10
v_cvt_pk_bf16_f32 v37, v8, v9
s_mov_b64 exec, 1
buffer_store_dwordx2 v[108:109], off, s[44:47], s98 sc1
s_mov_b64 exec, -1
v_exp_f32 v11, v11
v_exp_f32 v12, v12
v_cvt_pk_bf16_f32 v38, v10, v11
ds_write_b128 v79, v[42:45] offset:27648
ds_write_b128 v79, v[46:49] offset:28672
ds_write_b32 v80, v50 offset:27648
v_mfma_f32_32x32x16_bf16 v[42:57], a[64:67], v[34:37], 0
v_exp_f32 v13, v13
v_exp_f32 v14, v14
v_cvt_pk_bf16_f32 v39, v12, v13
v_exp_f32 v15, v15
v_exp_f32 v16, v16
v_cvt_pk_bf16_f32 v40, v14, v15
v_exp_f32 v17, v17
v_exp_f32 v18, v18
v_cvt_pk_bf16_f32 v41, v16, v17
v_exp_f32 v19, v19
v_exp_f32 v20, v20
v_mfma_f32_32x32x16_bf16 v[42:57], a[68:71], v[38:41], v[42:57]
v_cvt_pk_bf16_f32 v34, v18, v19
v_exp_f32 v21, v21
v_exp_f32 v22, v22
v_cvt_pk_bf16_f32 v35, v20, v21
v_exp_f32 v23, v23
v_exp_f32 v24, v24
v_cvt_pk_bf16_f32 v36, v22, v23
v_mfma_f32_32x32x16_bf16 v[2:17], a[8:11], v[58:61], v[62:77]
v_exp_f32 v25, v25
v_exp_f32 v26, v26
v_cvt_pk_bf16_f32 v37, v24, v25
v_exp_f32 v27, v27
v_exp_f32 v28, v28
v_cvt_pk_bf16_f32 v38, v26, v27
v_mfma_f32_32x32x16_bf16 v[42:57], a[72:75], v[34:37], v[42:57]
v_exp_f32 v29, v29
v_exp_f32 v30, v30
v_cvt_pk_bf16_f32 v39, v28, v29
v_exp_f32 v31, v31
v_exp_f32 v32, v32
v_cvt_pk_bf16_f32 v40, v30, v31
v_exp_f32 v33, v33
v_exp_f32 v2, v2
v_cvt_pk_bf16_f32 v41, v32, v33
v_exp_f32 v3, v3
v_exp_f32 v4, v4
v_mfma_f32_32x32x16_bf16 v[42:57], a[76:79], v[38:41], v[42:57]
v_cvt_pk_bf16_f32 v34, v2, v3
v_exp_f32 v5, v5
v_exp_f32 v6, v6
v_cvt_pk_bf16_f32 v35, v4, v5
v_exp_f32 v7, v7
v_exp_f32 v8, v8
v_cvt_pk_bf16_f32 v36, v6, v7
v_mfma_f32_32x32x16_bf16 v[18:33], a[12:15], v[58:61], v[62:77]
v_exp_f32 v9, v9
v_exp_f32 v10, v10
v_cvt_pk_bf16_f32 v37, v8, v9
v_exp_f32 v11, v11
v_exp_f32 v12, v12
v_cvt_pk_bf16_f32 v38, v10, v11
v_mfma_f32_32x32x16_bf16 v[42:57], a[80:83], v[34:37], v[42:57]
v_exp_f32 v13, v13
v_exp_f32 v14, v14
v_cvt_pk_bf16_f32 v39, v12, v13
v_exp_f32 v15, v15
v_exp_f32 v16, v16
v_cvt_pk_bf16_f32 v40, v14, v15
v_exp_f32 v17, v17
v_exp_f32 v18, v18
v_cvt_pk_bf16_f32 v41, v16, v17
v_exp_f32 v19, v19
v_exp_f32 v20, v20
v_mfma_f32_32x32x16_bf16 v[42:57], a[84:87], v[38:41], v[42:57]
v_cvt_pk_bf16_f32 v34, v18, v19
v_exp_f32 v21, v21
v_exp_f32 v22, v22
v_cvt_pk_bf16_f32 v35, v20, v21
v_exp_f32 v23, v23
v_exp_f32 v24, v24
v_cvt_pk_bf16_f32 v36, v22, v23
v_mfma_f32_32x32x16_bf16 v[2:17], a[16:19], v[58:61], v[62:77]
v_exp_f32 v25, v25
v_exp_f32 v26, v26
v_cvt_pk_bf16_f32 v37, v24, v25
v_exp_f32 v27, v27
v_exp_f32 v28, v28
v_cvt_pk_bf16_f32 v38, v26, v27
v_mfma_f32_32x32x16_bf16 v[42:57], a[88:91], v[34:37], v[42:57]
v_exp_f32 v29, v29
v_exp_f32 v30, v30
v_cvt_pk_bf16_f32 v39, v28, v29
v_exp_f32 v31, v31
v_exp_f32 v32, v32
v_cvt_pk_bf16_f32 v40, v30, v31
v_exp_f32 v33, v33
v_exp_f32 v2, v2
v_cvt_pk_bf16_f32 v41, v32, v33
v_exp_f32 v3, v3
v_exp_f32 v4, v4
v_mfma_f32_32x32x16_bf16 v[42:57], a[92:95], v[38:41], v[42:57]
v_cvt_pk_bf16_f32 v34, v2, v3
v_exp_f32 v5, v5
v_exp_f32 v6, v6
v_cvt_pk_bf16_f32 v35, v4, v5
v_exp_f32 v7, v7
v_exp_f32 v8, v8
v_cvt_pk_bf16_f32 v36, v6, v7
v_mfma_f32_32x32x16_bf16 v[18:33], a[20:23], v[58:61], v[62:77]
v_exp_f32 v9, v9
v_exp_f32 v10, v10
v_cvt_pk_bf16_f32 v37, v8, v9
v_exp_f32 v11, v11
v_exp_f32 v12, v12
v_cvt_pk_bf16_f32 v38, v10, v11
v_mfma_f32_32x32x16_bf16 v[42:57], a[96:99], v[34:37], v[42:57]
v_exp_f32 v13, v13
v_exp_f32 v14, v14
v_cvt_pk_bf16_f32 v39, v12, v13
v_exp_f32 v15, v15
v_exp_f32 v16, v16
v_cvt_pk_bf16_f32 v40, v14, v15
v_exp_f32 v17, v17
v_exp_f32 v18, v18
v_cvt_pk_bf16_f32 v41, v16, v17
v_exp_f32 v19, v19
v_exp_f32 v20, v20
v_mfma_f32_32x32x16_bf16 v[42:57], a[100:103], v[38:41], v[42:57]
v_cvt_pk_bf16_f32 v34, v18, v19
v_exp_f32 v21, v21
v_exp_f32 v22, v22
v_cvt_pk_bf16_f32 v35, v20, v21
v_exp_f32 v23, v23
v_exp_f32 v24, v24
v_cvt_pk_bf16_f32 v36, v22, v23
v_mfma_f32_32x32x16_bf16 v[2:17], a[24:27], v[58:61], v[62:77]
v_exp_f32 v25, v25
v_exp_f32 v26, v26
v_cvt_pk_bf16_f32 v37, v24, v25
v_exp_f32 v27, v27
v_exp_f32 v28, v28
v_cvt_pk_bf16_f32 v38, v26, v27
v_mfma_f32_32x32x16_bf16 v[42:57], a[104:107], v[34:37], v[42:57]
v_exp_f32 v29, v29
v_exp_f32 v30, v30
v_cvt_pk_bf16_f32 v39, v28, v29
v_exp_f32 v31, v31
v_exp_f32 v32, v32
v_cvt_pk_bf16_f32 v40, v30, v31
v_exp_f32 v33, v33
v_exp_f32 v2, v2
v_cvt_pk_bf16_f32 v41, v32, v33
v_exp_f32 v3, v3
v_exp_f32 v4, v4
v_mfma_f32_32x32x16_bf16 v[42:57], a[108:111], v[38:41], v[42:57]
v_cvt_pk_bf16_f32 v34, v2, v3
v_exp_f32 v5, v5
v_exp_f32 v6, v6
v_cvt_pk_bf16_f32 v35, v4, v5
v_exp_f32 v7, v7
v_exp_f32 v8, v8
v_cvt_pk_bf16_f32 v36, v6, v7
v_mfma_f32_32x32x16_bf16 v[18:33], a[28:31], v[58:61], v[62:77]
v_exp_f32 v9, v9
v_exp_f32 v10, v10
v_cvt_pk_bf16_f32 v37, v8, v9
v_exp_f32 v11, v11
v_exp_f32 v12, v12
v_cvt_pk_bf16_f32 v38, v10, v11
v_mfma_f32_32x32x16_bf16 v[42:57], a[112:115], v[34:37], v[42:57]
v_exp_f32 v13, v13
v_exp_f32 v14, v14
v_cvt_pk_bf16_f32 v39, v12, v13
v_exp_f32 v15, v15
v_exp_f32 v16, v16
v_cvt_pk_bf16_f32 v40, v14, v15
v_exp_f32 v17, v17
v_exp_f32 v18, v18
v_cvt_pk_bf16_f32 v41, v16, v17
v_exp_f32 v19, v19
v_exp_f32 v20, v20
v_mfma_f32_32x32x16_bf16 v[42:57], a[116:119], v[38:41], v[42:57]
v_cvt_pk_bf16_f32 v34, v18, v19
v_exp_f32 v21, v21
v_exp_f32 v22, v22
v_cvt_pk_bf16_f32 v35, v20, v21
v_exp_f32 v23, v23
v_exp_f32 v24, v24
v_cvt_pk_bf16_f32 v36, v22, v23
v_mfma_f32_32x32x16_bf16 v[2:17], a[32:35], v[58:61], v[62:77]
v_exp_f32 v25, v25
v_exp_f32 v26, v26
v_cvt_pk_bf16_f32 v37, v24, v25
v_exp_f32 v27, v27
v_exp_f32 v28, v28
v_cvt_pk_bf16_f32 v38, v26, v27
v_mfma_f32_32x32x16_bf16 v[42:57], a[120:123], v[34:37], v[42:57]
v_exp_f32 v29, v29
v_exp_f32 v30, v30
v_cvt_pk_bf16_f32 v39, v28, v29
v_exp_f32 v31, v31
v_exp_f32 v32, v32
v_cvt_pk_bf16_f32 v40, v30, v31
v_exp_f32 v33, v33
v_exp_f32 v2, v2
v_cvt_pk_bf16_f32 v41, v32, v33
v_exp_f32 v3, v3
v_exp_f32 v4, v4
v_mfma_f32_32x32x16_bf16 v[42:57], a[124:127], v[38:41], v[42:57]
v_cvt_pk_bf16_f32 v34, v2, v3
v_exp_f32 v5, v5
v_exp_f32 v6, v6
v_cvt_pk_bf16_f32 v35, v4, v5
v_exp_f32 v7, v7
v_exp_f32 v8, v8
v_cvt_pk_bf16_f32 v36, v6, v7
v_mfma_f32_32x32x16_bf16 v[18:33], a[36:39], v[58:61], v[62:77]
v_exp_f32 v9, v9
v_exp_f32 v10, v10
v_cvt_pk_bf16_f32 v37, v8, v9
v_exp_f32 v11, v11
v_exp_f32 v12, v12
v_cvt_pk_bf16_f32 v38, v10, v11
v_mfma_f32_32x32x16_bf16 v[42:57], a[128:131], v[34:37], v[42:57]
v_exp_f32 v13, v13
v_exp_f32 v14, v14
v_cvt_pk_bf16_f32 v39, v12, v13
v_exp_f32 v15, v15
v_exp_f32 v16, v16
v_cvt_pk_bf16_f32 v40, v14, v15
v_exp_f32 v17, v17
v_exp_f32 v18, v18
v_cvt_pk_bf16_f32 v41, v16, v17
v_exp_f32 v19, v19
v_exp_f32 v20, v20
v_mfma_f32_32x32x16_bf16 v[42:57], a[132:135], v[38:41], v[42:57]
v_cvt_pk_bf16_f32 v34, v18, v19
v_exp_f32 v21, v21
v_exp_f32 v22, v22
v_cvt_pk_bf16_f32 v35, v20, v21
v_exp_f32 v23, v23
v_exp_f32 v24, v24
v_cvt_pk_bf16_f32 v36, v22, v23
v_mfma_f32_32x32x16_bf16 v[2:17], a[40:43], v[58:61], v[62:77]
v_exp_f32 v25, v25
v_exp_f32 v26, v26
v_cvt_pk_bf16_f32 v37, v24, v25
v_exp_f32 v27, v27
v_exp_f32 v28, v28
v_cvt_pk_bf16_f32 v38, v26, v27
v_mfma_f32_32x32x16_bf16 v[42:57], a[136:139], v[34:37], v[42:57]
v_exp_f32 v29, v29
v_exp_f32 v30, v30
v_cvt_pk_bf16_f32 v39, v28, v29
v_exp_f32 v31, v31
v_exp_f32 v32, v32
v_cvt_pk_bf16_f32 v40, v30, v31
v_exp_f32 v33, v33
v_exp_f32 v2, v2
v_cvt_pk_bf16_f32 v41, v32, v33
v_exp_f32 v3, v3
v_exp_f32 v4, v4
v_mfma_f32_32x32x16_bf16 v[42:57], a[140:143], v[38:41], v[42:57]
v_cvt_pk_bf16_f32 v34, v2, v3
v_exp_f32 v5, v5
v_exp_f32 v6, v6
v_cvt_pk_bf16_f32 v35, v4, v5
v_exp_f32 v7, v7
v_exp_f32 v8, v8
v_cvt_pk_bf16_f32 v36, v6, v7
v_mfma_f32_32x32x16_bf16 v[18:33], a[44:47], v[58:61], v[62:77]
v_exp_f32 v9, v9
v_exp_f32 v10, v10
v_cvt_pk_bf16_f32 v37, v8, v9
v_exp_f32 v11, v11
v_exp_f32 v12, v12
v_cvt_pk_bf16_f32 v38, v10, v11
v_mfma_f32_32x32x16_bf16 v[42:57], a[144:147], v[34:37], v[42:57]
v_exp_f32 v13, v13
v_exp_f32 v14, v14
v_cvt_pk_bf16_f32 v39, v12, v13
v_exp_f32 v15, v15
v_exp_f32 v16, v16
v_cvt_pk_bf16_f32 v40, v14, v15
v_exp_f32 v17, v17
v_exp_f32 v18, v18
v_cvt_pk_bf16_f32 v41, v16, v17
v_exp_f32 v19, v19
v_exp_f32 v20, v20
v_mfma_f32_32x32x16_bf16 v[42:57], a[148:151], v[38:41], v[42:57]
v_cvt_pk_bf16_f32 v34, v18, v19
v_exp_f32 v21, v21
v_exp_f32 v22, v22
v_cvt_pk_bf16_f32 v35, v20, v21
v_exp_f32 v23, v23
v_exp_f32 v24, v24
v_cvt_pk_bf16_f32 v36, v22, v23
v_mfma_f32_32x32x16_bf16 v[2:17], a[48:51], v[58:61], v[62:77]
v_exp_f32 v25, v25
v_exp_f32 v26, v26
v_cvt_pk_bf16_f32 v37, v24, v25
v_exp_f32 v27, v27
v_exp_f32 v28, v28
v_cvt_pk_bf16_f32 v38, v26, v27
v_mfma_f32_32x32x16_bf16 v[42:57], a[152:155], v[34:37], v[42:57]
v_exp_f32 v29, v29
v_exp_f32 v30, v30
v_cvt_pk_bf16_f32 v39, v28, v29
v_exp_f32 v31, v31
v_exp_f32 v32, v32
v_cvt_pk_bf16_f32 v40, v30, v31
v_exp_f32 v33, v33
v_exp_f32 v2, v2
v_cvt_pk_bf16_f32 v41, v32, v33
v_exp_f32 v3, v3
v_exp_f32 v4, v4
v_mfma_f32_32x32x16_bf16 v[42:57], a[156:159], v[38:41], v[42:57]
v_cvt_pk_bf16_f32 v34, v2, v3
v_exp_f32 v5, v5
v_exp_f32 v6, v6
v_cvt_pk_bf16_f32 v35, v4, v5
v_lshlrev_b32 v120, 3, v0
v_exp_f32 v7, v7
v_exp_f32 v8, v8
v_cvt_pk_bf16_f32 v36, v6, v7
v_mfma_f32_32x32x16_bf16 v[18:33], a[52:55], v[58:61], v[62:77]
v_exp_f32 v9, v9
v_exp_f32 v10, v10
v_cvt_pk_bf16_f32 v37, v8, v9
s_lshl_b32 s54, s73, 9
v_exp_f32 v11, v11
v_exp_f32 v12, v12
v_cvt_pk_bf16_f32 v38, v10, v11
v_mfma_f32_32x32x16_bf16 v[42:57], a[160:163], v[34:37], v[42:57]
v_exp_f32 v13, v13
v_exp_f32 v14, v14
v_cvt_pk_bf16_f32 v39, v12, v13
v_exp_f32 v15, v15
v_exp_f32 v16, v16
v_cvt_pk_bf16_f32 v40, v14, v15
s_add_u32 s54, s54, 0x2000
v_exp_f32 v17, v17
v_exp_f32 v18, v18
v_cvt_pk_bf16_f32 v41, v16, v17
v_exp_f32 v19, v19
v_exp_f32 v20, v20
v_mfma_f32_32x32x16_bf16 v[42:57], a[164:167], v[38:41], v[42:57]
v_cvt_pk_bf16_f32 v34, v18, v19
v_exp_f32 v21, v21
v_exp_f32 v22, v22
v_cvt_pk_bf16_f32 v35, v20, v21
buffer_load_dwordx2 v[108:109], v120, s[44:47], s54 offen sc1
v_exp_f32 v23, v23
v_exp_f32 v24, v24
v_cvt_pk_bf16_f32 v36, v22, v23
v_mfma_f32_32x32x16_bf16 v[2:17], a[56:59], v[58:61], v[62:77]
v_exp_f32 v25, v25
v_exp_f32 v26, v26
v_cvt_pk_bf16_f32 v37, v24, v25
buffer_load_dwordx2 v[112:113], v120, s[44:47], s100 offen offset:0 sc1
v_exp_f32 v27, v27
v_exp_f32 v28, v28
v_cvt_pk_bf16_f32 v38, v26, v27
v_mfma_f32_32x32x16_bf16 v[42:57], a[168:171], v[34:37], v[42:57]
v_exp_f32 v29, v29
v_exp_f32 v30, v30
v_cvt_pk_bf16_f32 v39, v28, v29
v_exp_f32 v31, v31
v_exp_f32 v32, v32
v_cvt_pk_bf16_f32 v40, v30, v31
buffer_load_dwordx2 v[114:115], v120, s[44:47], s100 offen offset:512 sc1
v_exp_f32 v33, v33
v_exp_f32 v2, v2
v_cvt_pk_bf16_f32 v41, v32, v33
v_exp_f32 v3, v3
v_exp_f32 v4, v4
v_mfma_f32_32x32x16_bf16 v[42:57], a[172:175], v[38:41], v[42:57]
v_cvt_pk_bf16_f32 v34, v2, v3
v_exp_f32 v5, v5
v_exp_f32 v6, v6
v_cvt_pk_bf16_f32 v35, v4, v5
buffer_load_dwordx2 v[116:117], v120, s[44:47], s100 offen offset:1024 sc1
v_exp_f32 v7, v7
v_exp_f32 v8, v8
v_cvt_pk_bf16_f32 v36, v6, v7
v_mfma_f32_32x32x16_bf16 v[18:33], a[60:63], v[58:61], v[62:77]
ds_read_b128 v[58:61], v78 offset:5120
s_cmp_lg_u32 s69, 0
s_cbranch_scc1 negslow_24
negback_25:
v_exp_f32 v9, v9
v_exp_f32 v10, v10
v_cvt_pk_bf16_f32 v37, v8, v9
v_exp_f32 v11, v11
v_exp_f32 v12, v12
v_cvt_pk_bf16_f32 v38, v10, v11
buffer_load_dwordx2 v[118:119], v120, s[44:47], s100 offen offset:1536 sc1
v_mfma_f32_32x32x16_bf16 v[42:57], a[176:179], v[34:37], v[42:57]
v_exp_f32 v13, v13
v_exp_f32 v14, v14
v_cvt_pk_bf16_f32 v39, v12, v13
v_exp_f32 v15, v15
v_exp_f32 v16, v16
v_cvt_pk_bf16_f32 v40, v14, v15
v_accvgpr_read_b32 v121, a244
v_exp_f32 v17, v17
v_exp_f32 v18, v18
v_cvt_pk_bf16_f32 v41, v16, v17
v_exp_f32 v19, v19
v_exp_f32 v20, v20
v_mfma_f32_32x32x16_bf16 v[42:57], a[180:183], v[38:41], v[42:57]
v_cvt_pk_bf16_f32 v34, v18, v19
v_exp_f32 v21, v21
v_exp_f32 v22, v22
v_cvt_pk_bf16_f32 v35, v20, v21
s_mov_b32 s54, 0x2000
v_exp_f32 v23, v23
v_exp_f32 v24, v24
v_cvt_pk_bf16_f32 v36, v22, v23
s_waitcnt lgkmcnt(0)
v_mfma_f32_32x32x16_bf16 v[2:17], a[0:3], v[58:61], v[62:77]
v_exp_f32 v25, v25
v_exp_f32 v26, v26
v_cvt_pk_bf16_f32 v37, v24, v25
v_exp_f32 v27, v27
v_exp_f32 v28, v28
v_cvt_pk_bf16_f32 v38, v26, v27
buffer_load_dwordx2 v[110:111], v121, s[44:47], s54 offen sc1
v_mfma_f32_32x32x16_bf16 v[42:57], a[184:187], v[34:37], v[42:57]
v_exp_f32 v29, v29
v_exp_f32 v30, v30
v_cvt_pk_bf16_f32 v39, v28, v29
v_exp_f32 v31, v31
v_exp_f32 v32, v32
v_cvt_pk_bf16_f32 v40, v30, v31
v_exp_f32 v33, v33
v_exp_f32 v2, v2
v_cvt_pk_bf16_f32 v41, v32, v33
v_exp_f32 v3, v3
v_exp_f32 v4, v4
v_mfma_f32_32x32x16_bf16 v[42:57], a[188:191], v[38:41], v[42:57]
v_cvt_pk_bf16_f32 v34, v2, v3
v_exp_f32 v5, v5
v_exp_f32 v6, v6
v_cvt_pk_bf16_f32 v35, v4, v5
v_exp_f32 v7, v7
v_exp_f32 v8, v8
v_cvt_pk_bf16_f32 v36, v6, v7
v_mfma_f32_32x32x16_bf16 v[18:33], a[4:7], v[58:61], v[62:77]
v_exp_f32 v9, v9
v_exp_f32 v10, v10
v_cvt_pk_bf16_f32 v37, v8, v9
v_exp_f32 v11, v11
v_exp_f32 v12, v12
v_cvt_pk_bf16_f32 v38, v10, v11
ds_write_b128 v79, v[42:45] offset:36864
ds_write_b128 v79, v[46:49] offset:37888
ds_write_b32 v80, v50 offset:36864
v_mfma_f32_32x32x16_bf16 v[42:57], a[64:67], v[34:37], 0
v_exp_f32 v13, v13
v_exp_f32 v14, v14
v_cvt_pk_bf16_f32 v39, v12, v13
v_exp_f32 v15, v15
v_exp_f32 v16, v16
v_cvt_pk_bf16_f32 v40, v14, v15
v_exp_f32 v17, v17
v_exp_f32 v18, v18
v_cvt_pk_bf16_f32 v41, v16, v17
v_exp_f32 v19, v19
v_exp_f32 v20, v20
v_mfma_f32_32x32x16_bf16 v[42:57], a[68:71], v[38:41], v[42:57]
v_cvt_pk_bf16_f32 v34, v18, v19
v_exp_f32 v21, v21
v_exp_f32 v22, v22
v_cvt_pk_bf16_f32 v35, v20, v21
v_exp_f32 v23, v23
v_exp_f32 v24, v24
v_cvt_pk_bf16_f32 v36, v22, v23
v_mfma_f32_32x32x16_bf16 v[2:17], a[8:11], v[58:61], v[62:77]
v_exp_f32 v25, v25
v_exp_f32 v26, v26
v_cvt_pk_bf16_f32 v37, v24, v25
v_exp_f32 v27, v27
v_exp_f32 v28, v28
v_cvt_pk_bf16_f32 v38, v26, v27
v_mfma_f32_32x32x16_bf16 v[42:57], a[72:75], v[34:37], v[42:57]
v_exp_f32 v29, v29
v_exp_f32 v30, v30
v_cvt_pk_bf16_f32 v39, v28, v29
v_exp_f32 v31, v31
v_exp_f32 v32, v32
v_cvt_pk_bf16_f32 v40, v30, v31
v_exp_f32 v33, v33
v_exp_f32 v2, v2
v_cvt_pk_bf16_f32 v41, v32, v33
v_exp_f32 v3, v3
v_exp_f32 v4, v4
v_mfma_f32_32x32x16_bf16 v[42:57], a[76:79], v[38:41], v[42:57]
v_cvt_pk_bf16_f32 v34, v2, v3
v_exp_f32 v5, v5
v_exp_f32 v6, v6
v_cvt_pk_bf16_f32 v35, v4, v5
v_exp_f32 v7, v7
v_exp_f32 v8, v8
v_cvt_pk_bf16_f32 v36, v6, v7
v_mfma_f32_32x32x16_bf16 v[18:33], a[12:15], v[58:61], v[62:77]
v_exp_f32 v9, v9
v_exp_f32 v10, v10
v_cvt_pk_bf16_f32 v37, v8, v9
v_exp_f32 v11, v11
v_exp_f32 v12, v12
v_cvt_pk_bf16_f32 v38, v10, v11
v_mfma_f32_32x32x16_bf16 v[42:57], a[80:83], v[34:37], v[42:57]
v_exp_f32 v13, v13
v_exp_f32 v14, v14
v_cvt_pk_bf16_f32 v39, v12, v13
v_exp_f32 v15, v15
v_exp_f32 v16, v16
v_cvt_pk_bf16_f32 v40, v14, v15
v_exp_f32 v17, v17
v_exp_f32 v18, v18
v_cvt_pk_bf16_f32 v41, v16, v17
v_exp_f32 v19, v19
v_exp_f32 v20, v20
v_mfma_f32_32x32x16_bf16 v[42:57], a[84:87], v[38:41], v[42:57]
v_cvt_pk_bf16_f32 v34, v18, v19
v_exp_f32 v21, v21
v_exp_f32 v22, v22
v_cvt_pk_bf16_f32 v35, v20, v21
v_exp_f32 v23, v23
v_exp_f32 v24, v24
v_cvt_pk_bf16_f32 v36, v22, v23
v_mfma_f32_32x32x16_bf16 v[2:17], a[16:19], v[58:61], v[62:77]
v_exp_f32 v25, v25
v_exp_f32 v26, v26
v_cvt_pk_bf16_f32 v37, v24, v25
v_exp_f32 v27, v27
v_exp_f32 v28, v28
v_cvt_pk_bf16_f32 v38, v26, v27
v_mfma_f32_32x32x16_bf16 v[42:57], a[88:91], v[34:37], v[42:57]
v_exp_f32 v29, v29
v_exp_f32 v30, v30
v_cvt_pk_bf16_f32 v39, v28, v29
v_exp_f32 v31, v31
v_exp_f32 v32, v32
v_cvt_pk_bf16_f32 v40, v30, v31
v_exp_f32 v33, v33
v_exp_f32 v2, v2
v_cvt_pk_bf16_f32 v41, v32, v33
v_exp_f32 v3, v3
v_exp_f32 v4, v4
v_mfma_f32_32x32x16_bf16 v[42:57], a[92:95], v[38:41], v[42:57]
v_cvt_pk_bf16_f32 v34, v2, v3
v_exp_f32 v5, v5
v_exp_f32 v6, v6
v_cvt_pk_bf16_f32 v35, v4, v5
v_exp_f32 v7, v7
v_exp_f32 v8, v8
v_cvt_pk_bf16_f32 v36, v6, v7
v_mfma_f32_32x32x16_bf16 v[18:33], a[20:23], v[58:61], v[62:77]
v_exp_f32 v9, v9
v_exp_f32 v10, v10
v_cvt_pk_bf16_f32 v37, v8, v9
v_exp_f32 v11, v11
v_exp_f32 v12, v12
v_cvt_pk_bf16_f32 v38, v10, v11
v_mfma_f32_32x32x16_bf16 v[42:57], a[96:99], v[34:37], v[42:57]
v_exp_f32 v13, v13
v_exp_f32 v14, v14
v_cvt_pk_bf16_f32 v39, v12, v13
v_exp_f32 v15, v15
v_exp_f32 v16, v16
v_cvt_pk_bf16_f32 v40, v14, v15
v_exp_f32 v17, v17
v_exp_f32 v18, v18
v_cvt_pk_bf16_f32 v41, v16, v17
v_exp_f32 v19, v19
v_exp_f32 v20, v20
v_mfma_f32_32x32x16_bf16 v[42:57], a[100:103], v[38:41], v[42:57]
v_cvt_pk_bf16_f32 v34, v18, v19
v_exp_f32 v21, v21
v_exp_f32 v22, v22
v_cvt_pk_bf16_f32 v35, v20, v21
v_exp_f32 v23, v23
v_exp_f32 v24, v24
v_cvt_pk_bf16_f32 v36, v22, v23
v_mfma_f32_32x32x16_bf16 v[2:17], a[24:27], v[58:61], v[62:77]
v_exp_f32 v25, v25
v_exp_f32 v26, v26
v_cvt_pk_bf16_f32 v37, v24, v25
v_exp_f32 v27, v27
v_exp_f32 v28, v28
v_cvt_pk_bf16_f32 v38, v26, v27
s_waitcnt vmcnt(0)
v_mfma_f32_32x32x16_bf16 v[42:57], a[104:107], v[34:37], v[42:57]
v_exp_f32 v29, v29
v_exp_f32 v30, v30
v_cvt_pk_bf16_f32 v39, v28, v29
v_exp_f32 v31, v31
v_exp_f32 v32, v32
v_cvt_pk_bf16_f32 v40, v30, v31
v_exp_f32 v33, v33
v_exp_f32 v2, v2
v_cvt_pk_bf16_f32 v41, v32, v33
v_exp_f32 v3, v3
v_exp_f32 v4, v4
v_mfma_f32_32x32x16_bf16 v[42:57], a[108:111], v[38:41], v[42:57]
v_cvt_pk_bf16_f32 v34, v2, v3
v_exp_f32 v5, v5
v_exp_f32 v6, v6
v_cvt_pk_bf16_f32 v35, v4, v5
v_exp_f32 v7, v7
v_exp_f32 v8, v8
v_cvt_pk_bf16_f32 v36, v6, v7
v_mfma_f32_32x32x16_bf16 v[18:33], a[28:31], v[58:61], v[62:77]
v_exp_f32 v9, v9
v_exp_f32 v10, v10
v_cvt_pk_bf16_f32 v37, v8, v9
v_cmpx_eq_u64 vcc, s[52:53], v[108:109]
v_cmpx_eq_u64 vcc, s[52:53], v[110:111]
v_cmpx_eq_u64 vcc, s[52:53], v[112:113]
v_cmpx_eq_u64 vcc, s[52:53], v[114:115]
v_cmpx_eq_u64 vcc, s[52:53], v[116:117]
v_cmpx_eq_u64 vcc, s[52:53], v[118:119]
s_cmp_eq_u64 exec, -1
s_mov_b64 exec, -1
s_cbranch_scc0 ptslow_11
ptok_12:
v_exp_f32 v11, v11
v_exp_f32 v12, v12
v_cvt_pk_bf16_f32 v38, v10, v11
v_mfma_f32_32x32x16_bf16 v[42:57], a[112:115], v[34:37], v[42:57]
v_exp_f32 v13, v13
v_exp_f32 v14, v14
v_cvt_pk_bf16_f32 v39, v12, v13
v_exp_f32 v15, v15
v_exp_f32 v16, v16
v_cvt_pk_bf16_f32 v40, v14, v15
v_exp_f32 v17, v17
v_exp_f32 v18, v18
v_cvt_pk_bf16_f32 v41, v16, v17
v_exp_f32 v19, v19
v_exp_f32 v20, v20
v_mfma_f32_32x32x16_bf16 v[42:57], a[116:119], v[38:41], v[42:57]
v_cvt_pk_bf16_f32 v34, v18, v19
v_exp_f32 v21, v21
v_exp_f32 v22, v22
v_cvt_pk_bf16_f32 v35, v20, v21
v_exp_f32 v23, v23
v_exp_f32 v24, v24
v_cvt_pk_bf16_f32 v36, v22, v23
v_mfma_f32_32x32x16_bf16 v[2:17], a[32:35], v[58:61], v[62:77]
v_exp_f32 v25, v25
v_exp_f32 v26, v26
v_cvt_pk_bf16_f32 v37, v24, v25
buffer_load_dwordx4 v[128:131], v1, s[44:47], s86 offen offset:0 sc1
v_exp_f32 v27, v27
v_exp_f32 v28, v28
v_cvt_pk_bf16_f32 v38, v26, v27
v_mfma_f32_32x32x16_bf16 v[42:57], a[120:123], v[34:37], v[42:57]
v_exp_f32 v29, v29
v_exp_f32 v30, v30
v_cvt_pk_bf16_f32 v39, v28, v29
buffer_load_dwordx4 v[132:135], v1, s[44:47], s86 offen offset:1024 sc1
v_exp_f32 v31, v31
v_exp_f32 v32, v32
v_cvt_pk_bf16_f32 v40, v30, v31
v_exp_f32 v33, v33
v_exp_f32 v2, v2
v_cvt_pk_bf16_f32 v41, v32, v33
v_exp_f32 v3, v3
v_exp_f32 v4, v4
v_mfma_f32_32x32x16_bf16 v[42:57], a[124:127], v[38:41], v[42:57]
v_cvt_pk_bf16_f32 v34, v2, v3
buffer_load_dwordx4 v[136:139], v1, s[44:47], s86 offen offset:2048 sc1
v_exp_f32 v5, v5
v_exp_f32 v6, v6
v_cvt_pk_bf16_f32 v35, v4, v5
v_exp_f32 v7, v7
v_exp_f32 v8, v8
v_cvt_pk_bf16_f32 v36, v6, v7
buffer_load_dwordx4 v[140:143], v1, s[44:47], s86 offen offset:3072 sc1
v_mfma_f32_32x32x16_bf16 v[18:33], a[36:39], v[58:61], v[62:77]
v_exp_f32 v9, v9
v_exp_f32 v10, v10
v_cvt_pk_bf16_f32 v37, v8, v9
v_exp_f32 v11, v11
v_exp_f32 v12, v12
v_cvt_pk_bf16_f32 v38, v10, v11
v_mfma_f32_32x32x16_bf16 v[42:57], a[128:131], v[34:37], v[42:57]
v_exp_f32 v13, v13
v_exp_f32 v14, v14
v_cvt_pk_bf16_f32 v39, v12, v13
s_add_u32 s55, s86, 4096
v_exp_f32 v15, v15
v_exp_f32 v16, v16
v_cvt_pk_bf16_f32 v40, v14, v15
v_exp_f32 v17, v17
v_exp_f32 v18, v18
v_cvt_pk_bf16_f32 v41, v16, v17
buffer_load_dwordx4 v[144:147], v1, s[44:47], s55 offen offset:0 sc1
v_exp_f32 v19, v19
v_exp_f32 v20, v20
v_mfma_f32_32x32x16_bf16 v[42:57], a[132:135], v[38:41], v[42:57]
v_cvt_pk_bf16_f32 v34, v18, v19
v_exp_f32 v21, v21
v_exp_f32 v22, v22
v_cvt_pk_bf16_f32 v35, v20, v21
v_exp_f32 v23, v23
v_exp_f32 v24, v24
v_cvt_pk_bf16_f32 v36, v22, v23
buffer_load_dwordx4 v[148:151], v1, s[44:47], s55 offen offset:1024 sc1
v_mfma_f32_32x32x16_bf16 v[2:17], a[40:43], v[58:61], v[62:77]
v_exp_f32 v25, v25
v_exp_f32 v26, v26
v_cvt_pk_bf16_f32 v37, v24, v25
v_exp_f32 v27, v27
v_exp_f32 v28, v28
v_cvt_pk_bf16_f32 v38, v26, v27
buffer_load_dwordx4 v[152:155], v1, s[44:47], s55 offen offset:2048 sc1
v_mfma_f32_32x32x16_bf16 v[42:57], a[136:139], v[34:37], v[42:57]
v_exp_f32 v29, v29
v_exp_f32 v30, v30
v_cvt_pk_bf16_f32 v39, v28, v29
v_exp_f32 v31, v31
v_exp_f32 v32, v32
v_cvt_pk_bf16_f32 v40, v30, v31
v_exp_f32 v33, v33
v_exp_f32 v2, v2
v_cvt_pk_bf16_f32 v41, v32, v33
buffer_load_dwordx4 v[156:159], v1, s[44:47], s55 offen offset:3072 sc1
v_exp_f32 v3, v3
v_exp_f32 v4, v4
v_mfma_f32_32x32x16_bf16 v[42:57], a[140:143], v[38:41], v[42:57]
v_cvt_pk_bf16_f32 v34, v2, v3
v_exp_f32 v5, v5
v_exp_f32 v6, v6
v_cvt_pk_bf16_f32 v35, v4, v5
v_exp_f32 v7, v7
v_exp_f32 v8, v8
v_cvt_pk_bf16_f32 v36, v6, v7
s_xor_b32 s55, s86, 0x2000
v_mfma_f32_32x32x16_bf16 v[18:33], a[44:47], v[58:61], v[62:77]
v_exp_f32 v9, v9
v_exp_f32 v10, v10
v_cvt_pk_bf16_f32 v37, v8, v9
v_exp_f32 v11, v11
v_exp_f32 v12, v12
v_cvt_pk_bf16_f32 v38, v10, v11
buffer_load_dwordx4 v[160:163], v1, s[44:47], s55 offen offset:0 sc1
v_mfma_f32_32x32x16_bf16 v[42:57], a[144:147], v[34:37], v[42:57]
v_exp_f32 v13, v13
v_exp_f32 v14, v14
v_cvt_pk_bf16_f32 v39, v12, v13
v_exp_f32 v15, v15
v_exp_f32 v16, v16
v_cvt_pk_bf16_f32 v40, v14, v15
v_exp_f32 v17, v17
v_exp_f32 v18, v18
v_cvt_pk_bf16_f32 v41, v16, v17
buffer_load_dwordx4 v[164:167], v1, s[44:47], s55 offen offset:1024 sc1
v_exp_f32 v19, v19
v_exp_f32 v20, v20
v_mfma_f32_32x32x16_bf16 v[42:57], a[148:151], v[38:41], v[42:57]
v_cvt_pk_bf16_f32 v34, v18, v19
v_exp_f32 v21, v21
v_exp_f32 v22, v22
v_cvt_pk_bf16_f32 v35, v20, v21
buffer_load_dwordx4 v[168:171], v1, s[44:47], s55 offen offset:2048 sc1
v_exp_f32 v23, v23
v_exp_f32 v24, v24
v_cvt_pk_bf16_f32 v36, v22, v23
v_mfma_f32_32x32x16_bf16 v[2:17], a[48:51], v[58:61], v[62:77]
v_exp_f32 v25, v25
v_exp_f32 v26, v26
v_cvt_pk_bf16_f32 v37, v24, v25
v_exp_f32 v27, v27
v_exp_f32 v28, v28
v_cvt_pk_bf16_f32 v38, v26, v27
buffer_load_dwordx4 v[172:175], v1, s[44:47], s55 offen offset:3072 sc1
v_mfma_f32_32x32x16_bf16 v[42:57], a[152:155], v[34:37], v[42:57]
v_exp_f32 v29, v29
v_exp_f32 v30, v30
v_cvt_pk_bf16_f32 v39, v28, v29
v_exp_f32 v31, v31
v_exp_f32 v32, v32
v_cvt_pk_bf16_f32 v40, v30, v31
s_add_u32 s55, s55, 4096
v_exp_f32 v33, v33
v_exp_f32 v2, v2
v_cvt_pk_bf16_f32 v41, v32, v33
v_exp_f32 v3, v3
v_exp_f32 v4, v4
v_mfma_f32_32x32x16_bf16 v[42:57], a[156:159], v[38:41], v[42:57]
v_cvt_pk_bf16_f32 v34, v2, v3
v_exp_f32 v5, v5
v_exp_f32 v6, v6
v_cvt_pk_bf16_f32 v35, v4, v5
buffer_load_dwordx4 v[176:179], v1, s[44:47], s55 offen offset:0 sc1
v_exp_f32 v7, v7
v_exp_f32 v8, v8
v_cvt_pk_bf16_f32 v36, v6, v7
v_mfma_f32_32x32x16_bf16 v[18:33], a[52:55], v[58:61], v[62:77]
v_exp_f32 v9, v9
v_exp_f32 v10, v10
v_cvt_pk_bf16_f32 v37, v8, v9
v_exp_f32 v11, v11
v_exp_f32 v12, v12
v_cvt_pk_bf16_f32 v38, v10, v11
buffer_load_dwordx4 v[180:183], v1, s[44:47], s55 offen offset:1024 sc1
v_mfma_f32_32x32x16_bf16 v[42:57], a[160:163], v[34:37], v[42:57]
v_exp_f32 v13, v13
v_exp_f32 v14, v14
v_cvt_pk_bf16_f32 v39, v12, v13
v_exp_f32 v15, v15
v_exp_f32 v16, v16
v_cvt_pk_bf16_f32 v40, v14, v15
buffer_load_dwordx4 v[184:187], v1, s[44:47], s55 offen offset:2048 sc1
v_exp_f32 v17, v17
v_exp_f32 v18, v18
v_cvt_pk_bf16_f32 v41, v16, v17
v_exp_f32 v19, v19
v_exp_f32 v20, v20
v_mfma_f32_32x32x16_bf16 v[42:57], a[164:167], v[38:41], v[42:57]
v_cvt_pk_bf16_f32 v34, v18, v19
v_exp_f32 v21, v21
v_exp_f32 v22, v22
v_cvt_pk_bf16_f32 v35, v20, v21
buffer_load_dwordx4 v[188:191], v1, s[44:47], s55 offen offset:3072 sc1
v_exp_f32 v23, v23
v_exp_f32 v24, v24
v_cvt_pk_bf16_f32 v36, v22, v23
v_mfma_f32_32x32x16_bf16 v[2:17], a[56:59], v[58:61], v[62:77]
v_exp_f32 v25, v25
v_exp_f32 v26, v26
v_cvt_pk_bf16_f32 v37, v24, v25
s_xor_b32 s55, s86, 0x4000
v_exp_f32 v27, v27
v_exp_f32 v28, v28
v_cvt_pk_bf16_f32 v38, v26, v27
v_mfma_f32_32x32x16_bf16 v[42:57], a[168:171], v[34:37], v[42:57]
v_exp_f32 v29, v29
v_exp_f32 v30, v30
v_cvt_pk_bf16_f32 v39, v28, v29
v_exp_f32 v31, v31
v_exp_f32 v32, v32
v_cvt_pk_bf16_f32 v40, v30, v31
buffer_load_dwordx4 v[192:195], v1, s[44:47], s55 offen offset:0 sc1
v_exp_f32 v33, v33
v_exp_f32 v2, v2
v_cvt_pk_bf16_f32 v41, v32, v33
v_exp_f32 v3, v3
v_exp_f32 v4, v4
v_mfma_f32_32x32x16_bf16 v[42:57], a[172:175], v[38:41], v[42:57]
v_cvt_pk_bf16_f32 v34, v2, v3
buffer_load_dwordx4 v[196:199], v1, s[44:47], s55 offen offset:1024 sc1
v_exp_f32 v5, v5
v_exp_f32 v6, v6
v_cvt_pk_bf16_f32 v35, v4, v5
v_exp_f32 v7, v7
v_exp_f32 v8, v8
v_cvt_pk_bf16_f32 v36, v6, v7
v_mfma_f32_32x32x16_bf16 v[18:33], a[60:63], v[58:61], v[62:77]
ds_read_b128 v[58:61], v78 offset:6144
s_cmp_lg_u32 s69, 0
s_cbranch_scc1 negslow_26
negback_27:
v_exp_f32 v9, v9
v_exp_f32 v10, v10
v_cvt_pk_bf16_f32 v37, v8, v9
buffer_load_dwordx4 v[200:203], v1, s[44:47], s55 offen offset:2048 sc1
v_exp_f32 v11, v11
v_exp_f32 v12, v12
v_cvt_pk_bf16_f32 v38, v10, v11
v_mfma_f32_32x32x16_bf16 v[42:57], a[176:179], v[34:37], v[42:57]
v_exp_f32 v13, v13
v_exp_f32 v14, v14
v_cvt_pk_bf16_f32 v39, v12, v13
buffer_load_dwordx4 v[204:207], v1, s[44:47], s55 offen offset:3072 sc1
v_exp_f32 v15, v15
v_exp_f32 v16, v16
v_cvt_pk_bf16_f32 v40, v14, v15
v_exp_f32 v17, v17
v_exp_f32 v18, v18
v_cvt_pk_bf16_f32 v41, v16, v17
v_exp_f32 v19, v19
v_exp_f32 v20, v20
v_mfma_f32_32x32x16_bf16 v[42:57], a[180:183], v[38:41], v[42:57]
v_cvt_pk_bf16_f32 v34, v18, v19
s_add_u32 s55, s55, 4096
v_exp_f32 v21, v21
v_exp_f32 v22, v22
v_cvt_pk_bf16_f32 v35, v20, v21
v_exp_f32 v23, v23
v_exp_f32 v24, v24
v_cvt_pk_bf16_f32 v36, v22, v23
s_waitcnt lgkmcnt(0)
v_mfma_f32_32x32x16_bf16 v[2:17], a[0:3], v[58:61], v[62:77]
v_exp_f32 v25, v25
v_exp_f32 v26, v26
v_cvt_pk_bf16_f32 v37, v24, v25
buffer_load_dwordx4 v[208:211], v1, s[44:47], s55 offen offset:0 sc1
v_exp_f32 v27, v27
v_exp_f32 v28, v28
v_cvt_pk_bf16_f32 v38, v26, v27
v_mfma_f32_32x32x16_bf16 v[42:57], a[184:187], v[34:37], v[42:57]
v_exp_f32 v29, v29
v_exp_f32 v30, v30
v_cvt_pk_bf16_f32 v39, v28, v29
buffer_load_dwordx4 v[212:215], v1, s[44:47], s55 offen offset:1024 sc1
v_exp_f32 v31, v31
v_exp_f32 v32, v32
v_cvt_pk_bf16_f32 v40, v30, v31
v_exp_f32 v33, v33
v_exp_f32 v2, v2
v_cvt_pk_bf16_f32 v41, v32, v33
v_exp_f32 v3, v3
v_exp_f32 v4, v4
v_mfma_f32_32x32x16_bf16 v[42:57], a[188:191], v[38:41], v[42:57]
v_cvt_pk_bf16_f32 v34, v2, v3
buffer_load_dwordx4 v[216:219], v1, s[44:47], s55 offen offset:2048 sc1
v_exp_f32 v5, v5
v_exp_f32 v6, v6
v_cvt_pk_bf16_f32 v35, v4, v5
v_exp_f32 v7, v7
v_exp_f32 v8, v8
v_cvt_pk_bf16_f32 v36, v6, v7
buffer_load_dwordx4 v[220:223], v1, s[44:47], s55 offen offset:3072 sc1
v_mfma_f32_32x32x16_bf16 v[18:33], a[4:7], v[58:61], v[62:77]
v_exp_f32 v9, v9
v_exp_f32 v10, v10
v_cvt_pk_bf16_f32 v37, v8, v9
v_exp_f32 v11, v11
v_exp_f32 v12, v12
v_cvt_pk_bf16_f32 v38, v10, v11
ds_write_b128 v79, v[42:45] offset:46080
ds_write_b128 v79, v[46:49] offset:47104
ds_write_b32 v80, v50 offset:46080
v_mfma_f32_32x32x16_bf16 v[42:57], a[64:67], v[34:37], 0
v_exp_f32 v13, v13
v_exp_f32 v14, v14
v_cvt_pk_bf16_f32 v39, v12, v13
s_xor_b32 s55, s86, 0x6000
v_exp_f32 v15, v15
v_exp_f32 v16, v16
v_cvt_pk_bf16_f32 v40, v14, v15
v_exp_f32 v17, v17
v_exp_f32 v18, v18
v_cvt_pk_bf16_f32 v41, v16, v17
buffer_load_dwordx4 v[224:227], v1, s[44:47], s55 offen offset:0 sc1
v_exp_f32 v19, v19
v_exp_f32 v20, v20
v_mfma_f32_32x32x16_bf16 v[42:57], a[68:71], v[38:41], v[42:57]
v_cvt_pk_bf16_f32 v34, v18, v19
v_exp_f32 v21, v21
v_exp_f32 v22, v22
v_cvt_pk_bf16_f32 v35, v20, v21
v_exp_f32 v23, v23
v_exp_f32 v24, v24
v_cvt_pk_bf16_f32 v36, v22, v23
buffer_load_dwordx4 v[228:231], v1, s[44:47], s55 offen offset:1024 sc1
v_mfma_f32_32x32x16_bf16 v[2:17], a[8:11], v[58:61], v[62:77]
v_exp_f32 v25, v25
v_exp_f32 v26, v26
v_cvt_pk_bf16_f32 v37, v24, v25
v_exp_f32 v27, v27
v_exp_f32 v28, v28
v_cvt_pk_bf16_f32 v38, v26, v27
v_mfma_f32_32x32x16_bf16 v[42:57], a[72:75], v[34:37], v[42:57]
v_exp_f32 v29, v29
v_exp_f32 v30, v30
v_cvt_pk_bf16_f32 v39, v28, v29
buffer_load_dwordx4 v[232:235], v1, s[44:47], s55 offen offset:2048 sc1
v_exp_f32 v31, v31
v_exp_f32 v32, v32
v_cvt_pk_bf16_f32 v40, v30, v31
v_exp_f32 v33, v33
v_exp_f32 v2, v2
v_cvt_pk_bf16_f32 v41, v32, v33
buffer_load_dwordx4 v[236:239], v1, s[44:47], s55 offen offset:3072 sc1
v_exp_f32 v3, v3
v_exp_f32 v4, v4
v_mfma_f32_32x32x16_bf16 v[42:57], a[76:79], v[38:41], v[42:57]
v_cvt_pk_bf16_f32 v34, v2, v3
v_exp_f32 v5, v5
v_exp_f32 v6, v6
v_cvt_pk_bf16_f32 v35, v4, v5
v_exp_f32 v7, v7
v_exp_f32 v8, v8
v_cvt_pk_bf16_f32 v36, v6, v7
s_add_u32 s55, s55, 4096
v_mfma_f32_32x32x16_bf16 v[18:33], a[12:15], v[58:61], v[62:77]
v_exp_f32 v9, v9
v_exp_f32 v10, v10
v_cvt_pk_bf16_f32 v37, v8, v9
v_exp_f32 v11, v11
v_exp_f32 v12, v12
v_cvt_pk_bf16_f32 v38, v10, v11
buffer_load_dwordx4 v[240:243], v1, s[44:47], s55 offen offset:0 sc1
v_mfma_f32_32x32x16_bf16 v[42:57], a[80:83], v[34:37], v[42:57]
v_exp_f32 v13, v13
v_exp_f32 v14, v14
v_cvt_pk_bf16_f32 v39, v12, v13
v_exp_f32 v15, v15
v_exp_f32 v16, v16
v_cvt_pk_bf16_f32 v40, v14, v15
v_exp_f32 v17, v17
v_exp_f32 v18, v18
v_cvt_pk_bf16_f32 v41, v16, v17
buffer_load_dwordx4 v[244:247], v1, s[44:47], s55 offen offset:1024 sc1
v_exp_f32 v19, v19
v_exp_f32 v20, v20
v_mfma_f32_32x32x16_bf16 v[42:57], a[84:87], v[38:41], v[42:57]
v_cvt_pk_bf16_f32 v34, v18, v19
v_exp_f32 v21, v21
v_exp_f32 v22, v22
v_cvt_pk_bf16_f32 v35, v20, v21
buffer_load_dwordx4 v[248:251], v1, s[44:47], s55 offen offset:2048 sc1
v_exp_f32 v23, v23
v_exp_f32 v24, v24
v_cvt_pk_bf16_f32 v36, v22, v23
v_mfma_f32_32x32x16_bf16 v[2:17], a[16:19], v[58:61], v[62:77]
v_exp_f32 v25, v25
v_exp_f32 v26, v26
v_cvt_pk_bf16_f32 v37, v24, v25
v_exp_f32 v27, v27
v_exp_f32 v28, v28
v_cvt_pk_bf16_f32 v38, v26, v27
buffer_load_dwordx4 v[252:255], v1, s[44:47], s55 offen offset:3072 sc1
v_mfma_f32_32x32x16_bf16 v[42:57], a[88:91], v[34:37], v[42:57]
v_exp_f32 v29, v29
v_exp_f32 v30, v30
v_cvt_pk_bf16_f32 v39, v28, v29
v_exp_f32 v31, v31
v_exp_f32 v32, v32
v_cvt_pk_bf16_f32 v40, v30, v31
v_exp_f32 v33, v33
v_exp_f32 v2, v2
v_cvt_pk_bf16_f32 v41, v32, v33
v_accvgpr_read_b32 v108, a243
v_exp_f32 v3, v3
v_exp_f32 v4, v4
v_mfma_f32_32x32x16_bf16 v[42:57], a[92:95], v[38:41], v[42:57]
v_cvt_pk_bf16_f32 v34, v2, v3
v_exp_f32 v5, v5
v_exp_f32 v6, v6
v_cvt_pk_bf16_f32 v35, v4, v5
buffer_load_dwordx4 v[112:115], v108, s[44:47], s95 offen sc1
v_exp_f32 v7, v7
v_exp_f32 v8, v8
v_cvt_pk_bf16_f32 v36, v6, v7
v_mfma_f32_32x32x16_bf16 v[18:33], a[20:23], v[58:61], v[62:77]
v_exp_f32 v9, v9
v_exp_f32 v10, v10
v_cvt_pk_bf16_f32 v37, v8, v9
s_add_u32 s95, s95, 7168
v_exp_f32 v11, v11
v_exp_f32 v12, v12
v_cvt_pk_bf16_f32 v38, v10, v11
v_mfma_f32_32x32x16_bf16 v[42:57], a[96:99], v[34:37], v[42:57]
v_exp_f32 v13, v13
v_exp_f32 v14, v14
v_cvt_pk_bf16_f32 v39, v12, v13
buffer_load_dwordx4 v[116:119], v108, s[44:47], s95 offen sc1
v_exp_f32 v15, v15
v_exp_f32 v16, v16
v_cvt_pk_bf16_f32 v40, v14, v15
v_exp_f32 v17, v17
v_exp_f32 v18, v18
v_cvt_pk_bf16_f32 v41, v16, v17
buffer_load_dwordx4 v[120:123], v108, s[44:47], s96 offen sc1
v_exp_f32 v19, v19
v_exp_f32 v20, v20
v_mfma_f32_32x32x16_bf16 v[42:57], a[100:103], v[38:41], v[42:57]
v_cvt_pk_bf16_f32 v34, v18, v19
v_exp_f32 v21, v21
v_exp_f32 v22, v22
v_cvt_pk_bf16_f32 v35, v20, v21
s_add_u32 s96, s96, 7168
v_exp_f32 v23, v23
v_exp_f32 v24, v24
v_cvt_pk_bf16_f32 v36, v22, v23
v_mfma_f32_32x32x16_bf16 v[2:17], a[24:27], v[58:61], v[62:77]
v_exp_f32 v25, v25
v_exp_f32 v26, v26
v_cvt_pk_bf16_f32 v37, v24, v25
buffer_load_dwordx4 v[124:127], v108, s[44:47], s96 offen sc1
v_exp_f32 v27, v27
v_exp_f32 v28, v28
v_cvt_pk_bf16_f32 v38, v26, v27
v_mfma_f32_32x32x16_bf16 v[42:57], a[104:107], v[34:37], v[42:57]
v_exp_f32 v29, v29
v_exp_f32 v30, v30
v_cvt_pk_bf16_f32 v39, v28, v29
v_exp_f32 v31, v31
v_exp_f32 v32, v32
v_cvt_pk_bf16_f32 v40, v30, v31
s_add_u32 m0, s88, 0x0
v_exp_f32 v33, v33
v_exp_f32 v2, v2
v_cvt_pk_bf16_f32 v41, v32, v33
v_exp_f32 v3, v3
v_exp_f32 v4, v4
v_mfma_f32_32x32x16_bf16 v[42:57], a[108:111], v[38:41], v[42:57]
v_cvt_pk_bf16_f32 v34, v2, v3
s_add_u32 s54, s87, 0x0
v_exp_f32 v5, v5
v_exp_f32 v6, v6
v_cvt_pk_bf16_f32 v35, v4, v5
v_exp_f32 v7, v7
v_exp_f32 v8, v8
v_cvt_pk_bf16_f32 v36, v6, v7
v_mfma_f32_32x32x16_bf16 v[18:33], a[28:31], v[58:61], v[62:77]
v_exp_f32 v9, v9
v_exp_f32 v10, v10
v_cvt_pk_bf16_f32 v37, v8, v9
buffer_load_dwordx4 v1, s[44:47], s54 offen offset:0 sc1 lds
v_exp_f32 v11, v11
v_exp_f32 v12, v12
v_cvt_pk_bf16_f32 v38, v10, v11
v_mfma_f32_32x32x16_bf16 v[42:57], a[112:115], v[34:37], v[42:57]
v_exp_f32 v13, v13
v_exp_f32 v14, v14
v_cvt_pk_bf16_f32 v39, v12, v13
v_exp_f32 v15, v15
v_exp_f32 v16, v16
v_cvt_pk_bf16_f32 v40, v14, v15
buffer_load_dwordx4 v1, s[44:47], s54 offen offset:1024 sc1 lds
v_exp_f32 v17, v17
v_exp_f32 v18, v18
v_cvt_pk_bf16_f32 v41, v16, v17
v_exp_f32 v19, v19
v_exp_f32 v20, v20
v_mfma_f32_32x32x16_bf16 v[42:57], a[116:119], v[38:41], v[42:57]
v_cvt_pk_bf16_f32 v34, v18, v19
buffer_load_dwordx4 v1, s[44:47], s54 offen offset:2048 sc1 lds
v_exp_f32 v21, v21
v_exp_f32 v22, v22
v_cvt_pk_bf16_f32 v35, v20, v21
v_exp_f32 v23, v23
v_exp_f32 v24, v24
v_cvt_pk_bf16_f32 v36, v22, v23
v_mfma_f32_32x32x16_bf16 v[2:17], a[32:35], v[58:61], v[62:77]
v_exp_f32 v25, v25
v_exp_f32 v26, v26
v_cvt_pk_bf16_f32 v37, v24, v25
buffer_load_dwordx4 v1, s[44:47], s54 offen offset:3072 sc1 lds
v_exp_f32 v27, v27
v_exp_f32 v28, v28
v_cvt_pk_bf16_f32 v38, v26, v27
v_mfma_f32_32x32x16_bf16 v[42:57], a[120:123], v[34:37], v[42:57]
v_exp_f32 v29, v29
v_exp_f32 v30, v30
v_cvt_pk_bf16_f32 v39, v28, v29
v_exp_f32 v31, v31
v_exp_f32 v32, v32
v_cvt_pk_bf16_f32 v40, v30, v31
s_add_u32 m0, s88, 0x1000
v_exp_f32 v33, v33
v_exp_f32 v2, v2
v_cvt_pk_bf16_f32 v41, v32, v33
v_exp_f32 v3, v3
v_exp_f32 v4, v4
v_mfma_f32_32x32x16_bf16 v[42:57], a[124:127], v[38:41], v[42:57]
v_cvt_pk_bf16_f32 v34, v2, v3
s_add_u32 s54, s87, 0x1000
v_exp_f32 v5, v5
v_exp_f32 v6, v6
v_cvt_pk_bf16_f32 v35, v4, v5
v_exp_f32 v7, v7
v_exp_f32 v8, v8
v_cvt_pk_bf16_f32 v36, v6, v7
v_mfma_f32_32x32x16_bf16 v[18:33], a[36:39], v[58:61], v[62:77]
v_exp_f32 v9, v9
v_exp_f32 v10, v10
v_cvt_pk_bf16_f32 v37, v8, v9
buffer_load_dwordx4 v1, s[44:47], s54 offen offset:0 sc1 lds
v_exp_f32 v11, v11
v_exp_f32 v12, v12
v_cvt_pk_bf16_f32 v38, v10, v11
v_mfma_f32_32x32x16_bf16 v[42:57], a[128:131], v[34:37], v[42:57]
v_exp_f32 v13, v13
v_exp_f32 v14, v14
v_cvt_pk_bf16_f32 v39, v12, v13
v_exp_f32 v15, v15
v_exp_f32 v16, v16
v_cvt_pk_bf16_f32 v40, v14, v15
buffer_load_dwordx4 v1, s[44:47], s54 offen offset:1024 sc1 lds
v_exp_f32 v17, v17
v_exp_f32 v18, v18
v_cvt_pk_bf16_f32 v41, v16, v17
v_exp_f32 v19, v19
v_exp_f32 v20, v20
v_mfma_f32_32x32x16_bf16 v[42:57], a[132:135], v[38:41], v[42:57]
v_cvt_pk_bf16_f32 v34, v18, v19
v_exp_f32 v21, v21
v_exp_f32 v22, v22
v_cvt_pk_bf16_f32 v35, v20, v21
buffer_load_dwordx4 v1, s[44:47], s54 offen offset:2048 sc1 lds
v_exp_f32 v23, v23
v_exp_f32 v24, v24
v_cvt_pk_bf16_f32 v36, v22, v23
v_mfma_f32_32x32x16_bf16 v[2:17], a[40:43], v[58:61], v[62:77]
v_exp_f32 v25, v25
v_exp_f32 v26, v26
v_cvt_pk_bf16_f32 v37, v24, v25
buffer_load_dwordx4 v1, s[44:47], s54 offen offset:3072 sc1 lds
v_exp_f32 v27, v27
v_exp_f32 v28, v28
v_cvt_pk_bf16_f32 v38, v26, v27
v_mfma_f32_32x32x16_bf16 v[42:57], a[136:139], v[34:37], v[42:57]
v_exp_f32 v29, v29
v_exp_f32 v30, v30
v_cvt_pk_bf16_f32 v39, v28, v29
v_exp_f32 v31, v31
v_exp_f32 v32, v32
v_cvt_pk_bf16_f32 v40, v30, v31
s_add_u32 m0, s88, 0x8000
v_exp_f32 v33, v33
v_exp_f32 v2, v2
v_cvt_pk_bf16_f32 v41, v32, v33
v_exp_f32 v3, v3
v_exp_f32 v4, v4
v_mfma_f32_32x32x16_bf16 v[42:57], a[140:143], v[38:41], v[42:57]
v_cvt_pk_bf16_f32 v34, v2, v3
v_exp_f32 v5, v5
v_exp_f32 v6, v6
v_cvt_pk_bf16_f32 v35, v4, v5
s_add_u32 s54, s87, 0x8000
v_exp_f32 v7, v7
v_exp_f32 v8, v8
v_cvt_pk_bf16_f32 v36, v6, v7
v_mfma_f32_32x32x16_bf16 v[18:33], a[44:47], v[58:61], v[62:77]
v_exp_f32 v9, v9
v_exp_f32 v10, v10
v_cvt_pk_bf16_f32 v37, v8, v9
buffer_load_dwordx4 v1, s[44:47], s54 offen offset:0 sc1 lds
v_exp_f32 v11, v11
v_exp_f32 v12, v12
v_cvt_pk_bf16_f32 v38, v10, v11
v_mfma_f32_32x32x16_bf16 v[42:57], a[144:147], v[34:37], v[42:57]
v_exp_f32 v13, v13
v_exp_f32 v14, v14
v_cvt_pk_bf16_f32 v39, v12, v13
v_exp_f32 v15, v15
v_exp_f32 v16, v16
v_cvt_pk_bf16_f32 v40, v14, v15
buffer_load_dwordx4 v1, s[44:47], s54 offen offset:1024 sc1 lds
v_exp_f32 v17, v17
v_exp_f32 v18, v18
v_cvt_pk_bf16_f32 v41, v16, v17
v_exp_f32 v19, v19
v_exp_f32 v20, v20
v_mfma_f32_32x32x16_bf16 v[42:57], a[148:151], v[38:41], v[42:57]
v_cvt_pk_bf16_f32 v34, v18, v19
v_exp_f32 v21, v21
v_exp_f32 v22, v22
v_cvt_pk_bf16_f32 v35, v20, v21
buffer_load_dwordx4 v1, s[44:47], s54 offen offset:2048 sc1 lds
v_exp_f32 v23, v23
v_exp_f32 v24, v24
v_cvt_pk_bf16_f32 v36, v22, v23
v_mfma_f32_32x32x16_bf16 v[2:17], a[48:51], v[58:61], v[62:77]
v_exp_f32 v25, v25
v_exp_f32 v26, v26
v_cvt_pk_bf16_f32 v37, v24, v25
v_exp_f32 v27, v27
v_exp_f32 v28, v28
v_cvt_pk_bf16_f32 v38, v26, v27
buffer_load_dwordx4 v1, s[44:47], s54 offen offset:3072 sc1 lds
v_mfma_f32_32x32x16_bf16 v[42:57], a[152:155], v[34:37], v[42:57]
v_exp_f32 v29, v29
v_exp_f32 v30, v30
v_cvt_pk_bf16_f32 v39, v28, v29
v_exp_f32 v31, v31
v_exp_f32 v32, v32
v_cvt_pk_bf16_f32 v40, v30, v31
s_add_u32 m0, s88, 0x9000
v_exp_f32 v33, v33
v_exp_f32 v2, v2
v_cvt_pk_bf16_f32 v41, v32, v33
v_exp_f32 v3, v3
v_exp_f32 v4, v4
v_mfma_f32_32x32x16_bf16 v[42:57], a[156:159], v[38:41], v[42:57]
v_cvt_pk_bf16_f32 v34, v2, v3
v_exp_f32 v5, v5
v_exp_f32 v6, v6
v_cvt_pk_bf16_f32 v35, v4, v5
s_add_u32 s54, s87, 0x9000
v_exp_f32 v7, v7
v_exp_f32 v8, v8
v_cvt_pk_bf16_f32 v36, v6, v7
v_mfma_f32_32x32x16_bf16 v[18:33], a[52:55], v[58:61], v[62:77]
v_exp_f32 v9, v9
v_exp_f32 v10, v10
v_cvt_pk_bf16_f32 v37, v8, v9
v_exp_f32 v11, v11
v_exp_f32 v12, v12
v_cvt_pk_bf16_f32 v38, v10, v11
buffer_load_dwordx4 v1, s[44:47], s54 offen offset:0 sc1 lds
v_mfma_f32_32x32x16_bf16 v[42:57], a[160:163], v[34:37], v[42:57]
v_exp_f32 v13, v13
v_exp_f32 v14, v14
v_cvt_pk_bf16_f32 v39, v12, v13
v_exp_f32 v15, v15
v_exp_f32 v16, v16
v_cvt_pk_bf16_f32 v40, v14, v15
buffer_load_dwordx4 v1, s[44:47], s54 offen offset:1024 sc1 lds
v_exp_f32 v17, v17
v_exp_f32 v18, v18
v_cvt_pk_bf16_f32 v41, v16, v17
v_exp_f32 v19, v19
v_exp_f32 v20, v20
v_mfma_f32_32x32x16_bf16 v[42:57], a[164:167], v[38:41], v[42:57]
v_cvt_pk_bf16_f32 v34, v18, v19
v_exp_f32 v21, v21
v_exp_f32 v22, v22
v_cvt_pk_bf16_f32 v35, v20, v21
buffer_load_dwordx4 v1, s[44:47], s54 offen offset:2048 sc1 lds
v_exp_f32 v23, v23
v_exp_f32 v24, v24
v_cvt_pk_bf16_f32 v36, v22, v23
v_mfma_f32_32x32x16_bf16 v[2:17], a[56:59], v[58:61], v[62:77]
v_exp_f32 v25, v25
v_exp_f32 v26, v26
v_cvt_pk_bf16_f32 v37, v24, v25
v_exp_f32 v27, v27
v_exp_f32 v28, v28
v_cvt_pk_bf16_f32 v38, v26, v27
buffer_load_dwordx4 v1, s[44:47], s54 offen offset:3072 sc1 lds
v_mfma_f32_32x32x16_bf16 v[42:57], a[168:171], v[34:37], v[42:57]
v_exp_f32 v29, v29
v_exp_f32 v30, v30
v_cvt_pk_bf16_f32 v39, v28, v29
v_exp_f32 v31, v31
v_exp_f32 v32, v32
v_cvt_pk_bf16_f32 v40, v30, v31
v_exp_f32 v33, v33
v_exp_f32 v2, v2
v_cvt_pk_bf16_f32 v41, v32, v33
v_exp_f32 v3, v3
v_exp_f32 v4, v4
v_mfma_f32_32x32x16_bf16 v[42:57], a[172:175], v[38:41], v[42:57]
v_cvt_pk_bf16_f32 v34, v2, v3
v_exp_f32 v5, v5
v_exp_f32 v6, v6
v_cvt_pk_bf16_f32 v35, v4, v5
v_exp_f32 v7, v7
v_exp_f32 v8, v8
v_cvt_pk_bf16_f32 v36, v6, v7
v_mfma_f32_32x32x16_bf16 v[18:33], a[60:63], v[58:61], v[62:77]
ds_read_b128 v[58:61], v78 offset:7168
s_cmp_lg_u32 s69, 0
s_cbranch_scc1 negslow_28
negback_29:
v_exp_f32 v9, v9
v_exp_f32 v10, v10
v_cvt_pk_bf16_f32 v37, v8, v9
v_exp_f32 v11, v11
v_exp_f32 v12, v12
v_cvt_pk_bf16_f32 v38, v10, v11
v_mfma_f32_32x32x16_bf16 v[42:57], a[176:179], v[34:37], v[42:57]
v_exp_f32 v13, v13
v_exp_f32 v14, v14
v_cvt_pk_bf16_f32 v39, v12, v13
v_exp_f32 v15, v15
v_exp_f32 v16, v16
v_cvt_pk_bf16_f32 v40, v14, v15
v_exp_f32 v17, v17
v_exp_f32 v18, v18
v_cvt_pk_bf16_f32 v41, v16, v17
v_exp_f32 v19, v19
v_exp_f32 v20, v20
v_mfma_f32_32x32x16_bf16 v[42:57], a[180:183], v[38:41], v[42:57]
v_cvt_pk_bf16_f32 v34, v18, v19
v_exp_f32 v21, v21
v_exp_f32 v22, v22
v_cvt_pk_bf16_f32 v35, v20, v21
v_accvgpr_read_b32 v111, a245
v_exp_f32 v23, v23
v_exp_f32 v24, v24
v_cvt_pk_bf16_f32 v36, v22, v23
s_waitcnt lgkmcnt(0)
v_mfma_f32_32x32x16_bf16 v[2:17], a[0:3], v[58:61], v[62:77]
v_exp_f32 v25, v25
v_exp_f32 v26, v26
v_cvt_pk_bf16_f32 v37, v24, v25
v_exp_f32 v27, v27
v_exp_f32 v28, v28
v_cvt_pk_bf16_f32 v38, v26, v27
buffer_load_dword a224, v111, s[44:47], s99 offen sc1
v_mfma_f32_32x32x16_bf16 v[42:57], a[184:187], v[34:37], v[42:57]
v_exp_f32 v29, v29
v_exp_f32 v30, v30
v_cvt_pk_bf16_f32 v39, v28, v29
v_exp_f32 v31, v31
v_exp_f32 v32, v32
v_cvt_pk_bf16_f32 v40, v30, v31
v_exp_f32 v33, v33
v_exp_f32 v2, v2
v_cvt_pk_bf16_f32 v41, v32, v33
v_exp_f32 v3, v3
v_exp_f32 v4, v4
v_mfma_f32_32x32x16_bf16 v[42:57], a[188:191], v[38:41], v[42:57]
v_cvt_pk_bf16_f32 v34, v2, v3
v_exp_f32 v5, v5
v_exp_f32 v6, v6
v_cvt_pk_bf16_f32 v35, v4, v5
buffer_load_dwordx4 a[0:3], v1, s[44:47], s89 offen offset:0 sc1
v_exp_f32 v7, v7
v_exp_f32 v8, v8
v_cvt_pk_bf16_f32 v36, v6, v7
v_mfma_f32_32x32x16_bf16 v[18:33], a[4:7], v[58:61], v[62:77]
v_exp_f32 v9, v9
v_exp_f32 v10, v10
v_cvt_pk_bf16_f32 v37, v8, v9
v_exp_f32 v11, v11
v_exp_f32 v12, v12
v_cvt_pk_bf16_f32 v38, v10, v11
ds_write_b128 v79, v[42:45] offset:55296
ds_write_b128 v79, v[46:49] offset:56320
ds_write_b32 v80, v50 offset:55296
v_mfma_f32_32x32x16_bf16 v[42:57], a[64:67], v[34:37], 0
v_exp_f32 v13, v13
v_exp_f32 v14, v14
v_cvt_pk_bf16_f32 v39, v12, v13
v_exp_f32 v15, v15
v_exp_f32 v16, v16
v_cvt_pk_bf16_f32 v40, v14, v15
v_exp_f32 v17, v17
v_exp_f32 v18, v18
v_cvt_pk_bf16_f32 v41, v16, v17
v_exp_f32 v19, v19
v_exp_f32 v20, v20
v_mfma_f32_32x32x16_bf16 v[42:57], a[68:71], v[38:41], v[42:57]
v_cvt_pk_bf16_f32 v34, v18, v19
v_exp_f32 v21, v21
v_exp_f32 v22, v22
v_cvt_pk_bf16_f32 v35, v20, v21
buffer_load_dwordx4 a[4:7], v1, s[44:47], s90 offen offset:0 sc1
v_exp_f32 v23, v23
v_exp_f32 v24, v24
v_cvt_pk_bf16_f32 v36, v22, v23
v_mfma_f32_32x32x16_bf16 v[2:17], a[8:11], v[58:61], v[62:77]
v_exp_f32 v25, v25
v_exp_f32 v26, v26
v_cvt_pk_bf16_f32 v37, v24, v25
v_exp_f32 v27, v27
v_exp_f32 v28, v28
v_cvt_pk_bf16_f32 v38, v26, v27
v_mfma_f32_32x32x16_bf16 v[42:57], a[72:75], v[34:37], v[42:57]
v_exp_f32 v29, v29
v_exp_f32 v30, v30
v_cvt_pk_bf16_f32 v39, v28, v29
v_exp_f32 v31, v31
v_exp_f32 v32, v32
v_cvt_pk_bf16_f32 v40, v30, v31
v_exp_f32 v33, v33
v_exp_f32 v2, v2
v_cvt_pk_bf16_f32 v41, v32, v33
v_exp_f32 v3, v3
v_exp_f32 v4, v4
v_mfma_f32_32x32x16_bf16 v[42:57], a[76:79], v[38:41], v[42:57]
v_cvt_pk_bf16_f32 v34, v2, v3
v_exp_f32 v5, v5
v_exp_f32 v6, v6
v_cvt_pk_bf16_f32 v35, v4, v5
buffer_load_dwordx4 a[8:11], v1, s[44:47], s89 offen offset:2048 sc1
v_exp_f32 v7, v7
v_exp_f32 v8, v8
v_cvt_pk_bf16_f32 v36, v6, v7
v_mfma_f32_32x32x16_bf16 v[18:33], a[12:15], v[58:61], v[62:77]
v_exp_f32 v9, v9
v_exp_f32 v10, v10
v_cvt_pk_bf16_f32 v37, v8, v9
v_exp_f32 v11, v11
v_exp_f32 v12, v12
v_cvt_pk_bf16_f32 v38, v10, v11
v_mfma_f32_32x32x16_bf16 v[42:57], a[80:83], v[34:37], v[42:57]
v_exp_f32 v13, v13
v_exp_f32 v14, v14
v_cvt_pk_bf16_f32 v39, v12, v13
v_exp_f32 v15, v15
v_exp_f32 v16, v16
v_cvt_pk_bf16_f32 v40, v14, v15
v_exp_f32 v17, v17
v_exp_f32 v18, v18
v_cvt_pk_bf16_f32 v41, v16, v17
v_exp_f32 v19, v19
v_exp_f32 v20, v20
v_mfma_f32_32x32x16_bf16 v[42:57], a[84:87], v[38:41], v[42:57]
v_cvt_pk_bf16_f32 v34, v18, v19
v_exp_f32 v21, v21
v_exp_f32 v22, v22
v_cvt_pk_bf16_f32 v35, v20, v21
buffer_load_dwordx4 a[12:15], v1, s[44:47], s90 offen offset:2048 sc1
v_exp_f32 v23, v23
v_exp_f32 v24, v24
v_cvt_pk_bf16_f32 v36, v22, v23
v_mfma_f32_32x32x16_bf16 v[2:17], a[16:19], v[58:61], v[62:77]
v_exp_f32 v25, v25
v_exp_f32 v26, v26
v_cvt_pk_bf16_f32 v37, v24, v25
v_exp_f32 v27, v27
v_exp_f32 v28, v28
v_cvt_pk_bf16_f32 v38, v26, v27
v_mfma_f32_32x32x16_bf16 v[42:57], a[88:91], v[34:37], v[42:57]
v_exp_f32 v29, v29
v_exp_f32 v30, v30
v_cvt_pk_bf16_f32 v39, v28, v29
v_exp_f32 v31, v31
v_exp_f32 v32, v32
v_cvt_pk_bf16_f32 v40, v30, v31
v_exp_f32 v33, v33
v_exp_f32 v2, v2
v_cvt_pk_bf16_f32 v41, v32, v33
v_exp_f32 v3, v3
v_exp_f32 v4, v4
v_mfma_f32_32x32x16_bf16 v[42:57], a[92:95], v[38:41], v[42:57]
v_cvt_pk_bf16_f32 v34, v2, v3
v_exp_f32 v5, v5
v_exp_f32 v6, v6
v_cvt_pk_bf16_f32 v35, v4, v5
s_add_u32 s81, s89, 0x1000
buffer_load_dwordx4 a[16:19], v1, s[44:47], s81 offen offset:0 sc1
s_add_u32 s66, s89, 0xc000
buffer_load_dwordx4 a[192:195], v1, s[44:47], s66 offen offset:0 sc1
v_exp_f32 v7, v7
v_exp_f32 v8, v8
v_cvt_pk_bf16_f32 v36, v6, v7
v_mfma_f32_32x32x16_bf16 v[18:33], a[20:23], v[58:61], v[62:77]
v_exp_f32 v9, v9
v_exp_f32 v10, v10
v_cvt_pk_bf16_f32 v37, v8, v9
v_exp_f32 v11, v11
v_exp_f32 v12, v12
v_cvt_pk_bf16_f32 v38, v10, v11
s_add_u32 s67, s90, 0xc000
buffer_load_dwordx4 a[196:199], v1, s[44:47], s67 offen offset:0 sc1
v_mfma_f32_32x32x16_bf16 v[42:57], a[96:99], v[34:37], v[42:57]
v_exp_f32 v13, v13
v_exp_f32 v14, v14
v_cvt_pk_bf16_f32 v39, v12, v13
v_exp_f32 v15, v15
v_exp_f32 v16, v16
v_cvt_pk_bf16_f32 v40, v14, v15
v_exp_f32 v17, v17
v_exp_f32 v18, v18
v_cvt_pk_bf16_f32 v41, v16, v17
v_exp_f32 v19, v19
v_exp_f32 v20, v20
v_mfma_f32_32x32x16_bf16 v[42:57], a[100:103], v[38:41], v[42:57]
v_cvt_pk_bf16_f32 v34, v18, v19
buffer_load_dwordx4 a[200:203], v1, s[44:47], s66 offen offset:2048 sc1
v_exp_f32 v21, v21
v_exp_f32 v22, v22
v_cvt_pk_bf16_f32 v35, v20, v21
s_add_u32 s65, s90, 0x1000
buffer_load_dwordx4 a[20:23], v1, s[44:47], s65 offen offset:0 sc1
v_exp_f32 v23, v23
v_exp_f32 v24, v24
v_cvt_pk_bf16_f32 v36, v22, v23
v_mfma_f32_32x32x16_bf16 v[2:17], a[24:27], v[58:61], v[62:77]
v_exp_f32 v25, v25
v_exp_f32 v26, v26
v_cvt_pk_bf16_f32 v37, v24, v25
v_exp_f32 v27, v27
v_exp_f32 v28, v28
v_cvt_pk_bf16_f32 v38, v26, v27
buffer_load_dwordx4 a[204:207], v1, s[44:47], s67 offen offset:2048 sc1
v_mfma_f32_32x32x16_bf16 v[42:57], a[104:107], v[34:37], v[42:57]
v_exp_f32 v29, v29
v_exp_f32 v30, v30
v_cvt_pk_bf16_f32 v39, v28, v29
v_exp_f32 v31, v31
v_exp_f32 v32, v32
v_cvt_pk_bf16_f32 v40, v30, v31
v_exp_f32 v33, v33
v_exp_f32 v2, v2
v_cvt_pk_bf16_f32 v41, v32, v33
v_exp_f32 v3, v3
v_exp_f32 v4, v4
v_mfma_f32_32x32x16_bf16 v[42:57], a[108:111], v[38:41], v[42:57]
v_cvt_pk_bf16_f32 v34, v2, v3
s_add_u32 s66, s89, 0xd000
buffer_load_dwordx4 a[208:211], v1, s[44:47], s66 offen offset:0 sc1
v_exp_f32 v5, v5
v_exp_f32 v6, v6
v_cvt_pk_bf16_f32 v35, v4, v5
buffer_load_dwordx4 a[24:27], v1, s[44:47], s81 offen offset:2048 sc1
v_exp_f32 v7, v7
v_exp_f32 v8, v8
v_cvt_pk_bf16_f32 v36, v6, v7
v_mfma_f32_32x32x16_bf16 v[18:33], a[28:31], v[58:61], v[62:77]
v_exp_f32 v9, v9
v_exp_f32 v10, v10
v_cvt_pk_bf16_f32 v37, v8, v9
s_add_u32 s67, s90, 0xd000
buffer_load_dwordx4 a[212:215], v1, s[44:47], s67 offen offset:0 sc1
v_exp_f32 v11, v11
v_exp_f32 v12, v12
v_cvt_pk_bf16_f32 v38, v10, v11
v_mfma_f32_32x32x16_bf16 v[42:57], a[112:115], v[34:37], v[42:57]
v_exp_f32 v13, v13
v_exp_f32 v14, v14
v_cvt_pk_bf16_f32 v39, v12, v13
v_exp_f32 v15, v15
v_exp_f32 v16, v16
v_cvt_pk_bf16_f32 v40, v14, v15
v_exp_f32 v17, v17
v_exp_f32 v18, v18
v_cvt_pk_bf16_f32 v41, v16, v17
buffer_load_dwordx4 a[216:219], v1, s[44:47], s66 offen offset:2048 sc1
v_exp_f32 v19, v19
v_exp_f32 v20, v20
v_mfma_f32_32x32x16_bf16 v[42:57], a[116:119], v[38:41], v[42:57]
v_cvt_pk_bf16_f32 v34, v18, v19
v_exp_f32 v21, v21
v_exp_f32 v22, v22
v_cvt_pk_bf16_f32 v35, v20, v21
buffer_load_dwordx4 a[28:31], v1, s[44:47], s65 offen offset:2048 sc1
v_exp_f32 v23, v23
v_exp_f32 v24, v24
v_cvt_pk_bf16_f32 v36, v22, v23
v_mfma_f32_32x32x16_bf16 v[2:17], a[32:35], v[58:61], v[62:77]
v_exp_f32 v25, v25
v_exp_f32 v26, v26
v_cvt_pk_bf16_f32 v37, v24, v25
buffer_load_dwordx4 a[220:223], v1, s[44:47], s67 offen offset:2048 sc1
v_exp_f32 v27, v27
v_exp_f32 v28, v28
v_cvt_pk_bf16_f32 v38, v26, v27
v_mfma_f32_32x32x16_bf16 v[42:57], a[120:123], v[34:37], v[42:57]
v_exp_f32 v29, v29
v_exp_f32 v30, v30
v_cvt_pk_bf16_f32 v39, v28, v29
v_exp_f32 v31, v31
v_exp_f32 v32, v32
v_cvt_pk_bf16_f32 v40, v30, v31
v_exp_f32 v33, v33
v_exp_f32 v2, v2
v_cvt_pk_bf16_f32 v41, v32, v33
v_exp_f32 v3, v3
v_exp_f32 v4, v4
v_mfma_f32_32x32x16_bf16 v[42:57], a[124:127], v[38:41], v[42:57]
v_cvt_pk_bf16_f32 v34, v2, v3
v_exp_f32 v5, v5
v_exp_f32 v6, v6
v_cvt_pk_bf16_f32 v35, v4, v5
s_add_u32 s81, s89, 0x2000
buffer_load_dwordx4 a[32:35], v1, s[44:47], s81 offen offset:0 sc1
v_exp_f32 v7, v7
v_exp_f32 v8, v8
v_cvt_pk_bf16_f32 v36, v6, v7
v_mfma_f32_32x32x16_bf16 v[18:33], a[36:39], v[58:61], v[62:77]
v_exp_f32 v9, v9
v_exp_f32 v10, v10
v_cvt_pk_bf16_f32 v37, v8, v9
v_exp_f32 v11, v11
v_exp_f32 v12, v12
v_cvt_pk_bf16_f32 v38, v10, v11
v_mfma_f32_32x32x16_bf16 v[42:57], a[128:131], v[34:37], v[42:57]
v_exp_f32 v13, v13
v_exp_f32 v14, v14
v_cvt_pk_bf16_f32 v39, v12, v13
v_exp_f32 v15, v15
v_exp_f32 v16, v16
v_cvt_pk_bf16_f32 v40, v14, v15
v_exp_f32 v17, v17
v_exp_f32 v18, v18
v_cvt_pk_bf16_f32 v41, v16, v17
v_exp_f32 v19, v19
v_exp_f32 v20, v20
v_mfma_f32_32x32x16_bf16 v[42:57], a[132:135], v[38:41], v[42:57]
v_cvt_pk_bf16_f32 v34, v18, v19
v_exp_f32 v21, v21
v_exp_f32 v22, v22
v_cvt_pk_bf16_f32 v35, v20, v21
s_add_u32 s65, s90, 0x2000
buffer_load_dwordx4 a[36:39], v1, s[44:47], s65 offen offset:0 sc1
v_exp_f32 v23, v23
v_exp_f32 v24, v24
v_cvt_pk_bf16_f32 v36, v22, v23
v_mfma_f32_32x32x16_bf16 v[2:17], a[40:43], v[58:61], v[62:77]
v_exp_f32 v25, v25
v_exp_f32 v26, v26
v_cvt_pk_bf16_f32 v37, v24, v25
v_exp_f32 v27, v27
v_exp_f32 v28, v28
v_cvt_pk_bf16_f32 v38, v26, v27
v_mfma_f32_32x32x16_bf16 v[42:57], a[136:139], v[34:37], v[42:57]
v_exp_f32 v29, v29
v_exp_f32 v30, v30
v_cvt_pk_bf16_f32 v39, v28, v29
v_exp_f32 v31, v31
v_exp_f32 v32, v32
v_cvt_pk_bf16_f32 v40, v30, v31
v_exp_f32 v33, v33
v_exp_f32 v2, v2
v_cvt_pk_bf16_f32 v41, v32, v33
v_exp_f32 v3, v3
v_exp_f32 v4, v4
v_mfma_f32_32x32x16_bf16 v[42:57], a[140:143], v[38:41], v[42:57]
v_cvt_pk_bf16_f32 v34, v2, v3
v_exp_f32 v5, v5
v_exp_f32 v6, v6
v_cvt_pk_bf16_f32 v35, v4, v5
buffer_load_dwordx4 a[40:43], v1, s[44:47], s81 offen offset:2048 sc1
v_exp_f32 v7, v7
v_exp_f32 v8, v8
v_cvt_pk_bf16_f32 v36, v6, v7
v_mfma_f32_32x32x16_bf16 v[18:33], a[44:47], v[58:61], v[62:77]
v_exp_f32 v9, v9
v_exp_f32 v10, v10
v_cvt_pk_bf16_f32 v37, v8, v9
v_exp_f32 v11, v11
v_exp_f32 v12, v12
v_cvt_pk_bf16_f32 v38, v10, v11
v_mfma_f32_32x32x16_bf16 v[42:57], a[144:147], v[34:37], v[42:57]
v_exp_f32 v13, v13
v_exp_f32 v14, v14
v_cvt_pk_bf16_f32 v39, v12, v13
v_exp_f32 v15, v15
v_exp_f32 v16, v16
v_cvt_pk_bf16_f32 v40, v14, v15
v_exp_f32 v17, v17
v_exp_f32 v18, v18
v_cvt_pk_bf16_f32 v41, v16, v17
v_exp_f32 v19, v19
v_exp_f32 v20, v20
v_mfma_f32_32x32x16_bf16 v[42:57], a[148:151], v[38:41], v[42:57]
v_cvt_pk_bf16_f32 v34, v18, v19
v_exp_f32 v21, v21
v_exp_f32 v22, v22
v_cvt_pk_bf16_f32 v35, v20, v21
buffer_load_dwordx4 a[44:47], v1, s[44:47], s65 offen offset:2048 sc1
v_exp_f32 v23, v23
v_exp_f32 v24, v24
v_cvt_pk_bf16_f32 v36, v22, v23
v_mfma_f32_32x32x16_bf16 v[2:17], a[48:51], v[58:61], v[62:77]
v_exp_f32 v25, v25
v_exp_f32 v26, v26
v_cvt_pk_bf16_f32 v37, v24, v25
v_exp_f32 v27, v27
v_exp_f32 v28, v28
v_cvt_pk_bf16_f32 v38, v26, v27
v_mfma_f32_32x32x16_bf16 v[42:57], a[152:155], v[34:37], v[42:57]
v_exp_f32 v29, v29
v_exp_f32 v30, v30
v_cvt_pk_bf16_f32 v39, v28, v29
v_exp_f32 v31, v31
v_exp_f32 v32, v32
v_cvt_pk_bf16_f32 v40, v30, v31
v_exp_f32 v33, v33
v_exp_f32 v2, v2
v_cvt_pk_bf16_f32 v41, v32, v33
v_exp_f32 v3, v3
v_exp_f32 v4, v4
v_mfma_f32_32x32x16_bf16 v[42:57], a[156:159], v[38:41], v[42:57]
v_cvt_pk_bf16_f32 v34, v2, v3
v_exp_f32 v5, v5
v_exp_f32 v6, v6
v_cvt_pk_bf16_f32 v35, v4, v5
s_add_u32 s81, s89, 0x3000
buffer_load_dwordx4 a[48:51], v1, s[44:47], s81 offen offset:0 sc1
v_exp_f32 v7, v7
v_exp_f32 v8, v8
v_cvt_pk_bf16_f32 v36, v6, v7
v_mfma_f32_32x32x16_bf16 v[18:33], a[52:55], v[58:61], v[62:77]
v_exp_f32 v9, v9
v_exp_f32 v10, v10
v_cvt_pk_bf16_f32 v37, v8, v9
v_exp_f32 v11, v11
v_exp_f32 v12, v12
v_cvt_pk_bf16_f32 v38, v10, v11
v_mfma_f32_32x32x16_bf16 v[42:57], a[160:163], v[34:37], v[42:57]
v_exp_f32 v13, v13
v_exp_f32 v14, v14
v_cvt_pk_bf16_f32 v39, v12, v13
v_exp_f32 v15, v15
v_exp_f32 v16, v16
v_cvt_pk_bf16_f32 v40, v14, v15
v_exp_f32 v17, v17
v_exp_f32 v18, v18
v_cvt_pk_bf16_f32 v41, v16, v17
v_exp_f32 v19, v19
v_exp_f32 v20, v20
v_mfma_f32_32x32x16_bf16 v[42:57], a[164:167], v[38:41], v[42:57]
v_cvt_pk_bf16_f32 v34, v18, v19
v_exp_f32 v21, v21
v_exp_f32 v22, v22
v_cvt_pk_bf16_f32 v35, v20, v21
s_add_u32 s65, s90, 0x3000
buffer_load_dwordx4 a[52:55], v1, s[44:47], s65 offen offset:0 sc1
v_exp_f32 v23, v23
v_exp_f32 v24, v24
v_cvt_pk_bf16_f32 v36, v22, v23
v_mfma_f32_32x32x16_bf16 v[2:17], a[56:59], v[58:61], v[62:77]
v_exp_f32 v25, v25
v_exp_f32 v26, v26
v_cvt_pk_bf16_f32 v37, v24, v25
v_exp_f32 v27, v27
v_exp_f32 v28, v28
v_cvt_pk_bf16_f32 v38, v26, v27
v_mfma_f32_32x32x16_bf16 v[42:57], a[168:171], v[34:37], v[42:57]
v_exp_f32 v29, v29
v_exp_f32 v30, v30
v_cvt_pk_bf16_f32 v39, v28, v29
v_exp_f32 v31, v31
v_exp_f32 v32, v32
v_cvt_pk_bf16_f32 v40, v30, v31
v_exp_f32 v33, v33
v_exp_f32 v2, v2
v_cvt_pk_bf16_f32 v41, v32, v33
v_exp_f32 v3, v3
v_exp_f32 v4, v4
v_mfma_f32_32x32x16_bf16 v[42:57], a[172:175], v[38:41], v[42:57]
v_cvt_pk_bf16_f32 v34, v2, v3
v_exp_f32 v5, v5
v_exp_f32 v6, v6
v_cvt_pk_bf16_f32 v35, v4, v5
buffer_load_dwordx4 a[56:59], v1, s[44:47], s81 offen offset:2048 sc1
v_exp_f32 v7, v7
v_exp_f32 v8, v8
v_cvt_pk_bf16_f32 v36, v6, v7
v_mfma_f32_32x32x16_bf16 v[18:33], a[60:63], v[58:61], v[62:77]
v_exp_f32 v9, v9
v_exp_f32 v10, v10
v_cvt_pk_bf16_f32 v37, v8, v9
v_exp_f32 v11, v11
v_exp_f32 v12, v12
v_cvt_pk_bf16_f32 v38, v10, v11
v_mfma_f32_32x32x16_bf16 v[42:57], a[176:179], v[34:37], v[42:57]
v_exp_f32 v13, v13
v_exp_f32 v14, v14
v_cvt_pk_bf16_f32 v39, v12, v13
v_exp_f32 v15, v15
v_exp_f32 v16, v16
v_cvt_pk_bf16_f32 v40, v14, v15
v_exp_f32 v17, v17
v_exp_f32 v18, v18
v_cvt_pk_bf16_f32 v41, v16, v17
v_exp_f32 v19, v19
v_exp_f32 v20, v20
v_mfma_f32_32x32x16_bf16 v[42:57], a[180:183], v[38:41], v[42:57]
v_cvt_pk_bf16_f32 v34, v18, v19
v_exp_f32 v21, v21
v_exp_f32 v22, v22
v_cvt_pk_bf16_f32 v35, v20, v21
v_exp_f32 v23, v23
v_exp_f32 v24, v24
v_cvt_pk_bf16_f32 v36, v22, v23
v_exp_f32 v25, v25
v_exp_f32 v26, v26
v_cvt_pk_bf16_f32 v37, v24, v25
v_exp_f32 v27, v27
v_exp_f32 v28, v28
v_cvt_pk_bf16_f32 v38, v26, v27
v_mfma_f32_32x32x16_bf16 v[42:57], a[184:187], v[34:37], v[42:57]
v_exp_f32 v29, v29
v_exp_f32 v30, v30
v_cvt_pk_bf16_f32 v39, v28, v29
v_exp_f32 v31, v31
v_exp_f32 v32, v32
v_cvt_pk_bf16_f32 v40, v30, v31
v_exp_f32 v33, v33
s_nop 0
v_cvt_pk_bf16_f32 v41, v32, v33
s_nop 1
v_mfma_f32_32x32x16_bf16 v[42:57], a[188:191], v[38:41], v[42:57]
s_mul_i32 s54, s49, 18432
s_add_u32 s54, s54, 0x10000
v_add_u32 v82, s54, v1
v_lshlrev_b32 v83, 2, v0
s_add_u32 s54, s54, 2048
v_add_u32 v83, s54, v83
s_lshl_b32 s56, s50, 5
s_lshl_b32 s55, s51, 3
s_add_u32 s56, s56, s55
s_lshl_b32 s55, s49, 1
s_add_u32 s56, s56, s55
s_lshl_b32 s56, s56, 10
s_add_u32 s56, s56, 0x200000
ds_write_b128 v79, v[42:45] offset:64512
v_add_u32 v79, 1024, v79
ds_write_b128 v79, v[46:49] offset:64512
ds_write_b32 v80, v50 offset:64512
s_waitcnt lgkmcnt(0)
s_barrier
ds_read_b128 v[2:5], v82 offset:0
ds_read_b128 v[6:9], v82 offset:1024
ds_read_b32 v10, v83 offset:0
ds_read_b128 v[12:15], v82 offset:2304
ds_read_b128 v[16:19], v82 offset:3328
ds_read_b32 v20, v83 offset:2304
ds_read_b128 v[22:25], v82 offset:4608
ds_read_b128 v[26:29], v82 offset:5632
ds_read_b32 v30, v83 offset:4608
ds_read_b128 v[32:35], v82 offset:6912
ds_read_b128 v[36:39], v82 offset:7936
ds_read_b32 v40, v83 offset:6912
ds_read_b128 v[42:45], v82 offset:9216
ds_read_b128 v[46:49], v82 offset:10240
ds_read_b32 v50, v83 offset:9216
ds_read_b128 v[52:55], v82 offset:11520
ds_read_b128 v[56:59], v82 offset:12544
ds_read_b32 v60, v83 offset:11520
ds_read_b128 v[62:65], v82 offset:13824
ds_read_b128 v[66:69], v82 offset:14848
ds_read_b32 v70, v83 offset:13824
ds_read_b128 v[72:75], v82 offset:16128
ds_read_b128 v[76:79], v82 offset:17152
ds_read_b32 v80, v83 offset:16128
s_waitcnt lgkmcnt(15)
v_pk_add_f32 v[2:3], v[2:3], v[12:13]
v_pk_add_f32 v[4:5], v[4:5], v[14:15]
v_pk_add_f32 v[6:7], v[6:7], v[16:17]
v_pk_add_f32 v[8:9], v[8:9], v[18:19]
v_add_f32 v10, v10, v20
s_waitcnt lgkmcnt(14)
v_pk_add_f32 v[22:23], v[22:23], v[32:33]
v_pk_add_f32 v[24:25], v[24:25], v[34:35]
s_waitcnt lgkmcnt(13)
v_pk_add_f32 v[26:27], v[26:27], v[36:37]
v_pk_add_f32 v[28:29], v[28:29], v[38:39]
s_waitcnt lgkmcnt(12)
v_add_f32 v30, v30, v40
v_pk_add_f32 v[2:3], v[2:3], v[22:23]
v_pk_add_f32 v[4:5], v[4:5], v[24:25]
v_pk_add_f32 v[6:7], v[6:7], v[26:27]
v_pk_add_f32 v[8:9], v[8:9], v[28:29]
v_add_f32 v10, v10, v30
v_rcp_f32 v10, v10
s_waitcnt lgkmcnt(8)
v_pk_add_f32 v[42:43], v[42:43], v[52:53]
v_pk_add_f32 v[44:45], v[44:45], v[54:55]
s_waitcnt lgkmcnt(7)
v_pk_add_f32 v[46:47], v[46:47], v[56:57]
v_pk_add_f32 v[48:49], v[48:49], v[58:59]
s_waitcnt lgkmcnt(6)
v_add_f32 v50, v50, v60
s_waitcnt lgkmcnt(2)
v_pk_add_f32 v[62:63], v[62:63], v[72:73]
v_pk_add_f32 v[64:65], v[64:65], v[74:75]
s_waitcnt lgkmcnt(1)
v_pk_add_f32 v[66:67], v[66:67], v[76:77]
v_pk_add_f32 v[68:69], v[68:69], v[78:79]
s_waitcnt lgkmcnt(0)
v_add_f32 v70, v70, v80
v_pk_add_f32 v[42:43], v[42:43], v[62:63]
v_pk_add_f32 v[44:45], v[44:45], v[64:65]
v_pk_add_f32 v[46:47], v[46:47], v[66:67]
v_pk_add_f32 v[48:49], v[48:49], v[68:69]
v_add_f32 v50, v50, v70
v_rcp_f32 v50, v50
v_pk_mul_f32 v[2:3], v[2:3], v[10:11] op_sel_hi:[1,0]
v_pk_mul_f32 v[4:5], v[4:5], v[10:11] op_sel_hi:[1,0]
v_pk_mul_f32 v[6:7], v[6:7], v[10:11] op_sel_hi:[1,0]
v_pk_mul_f32 v[8:9], v[8:9], v[10:11] op_sel_hi:[1,0]
v_cvt_pk_bf16_f32 v92, v2, v3
v_cvt_pk_bf16_f32 v93, v4, v5
v_cvt_pk_bf16_f32 v94, v6, v7
v_cvt_pk_bf16_f32 v95, v8, v9
v_pk_mul_f32 v[42:43], v[42:43], v[50:51] op_sel_hi:[1,0]
v_pk_mul_f32 v[44:45], v[44:45], v[50:51] op_sel_hi:[1,0]
v_pk_mul_f32 v[46:47], v[46:47], v[50:51] op_sel_hi:[1,0]
v_pk_mul_f32 v[48:49], v[48:49], v[50:51] op_sel_hi:[1,0]
v_cvt_pk_bf16_f32 v96, v42, v43
v_cvt_pk_bf16_f32 v97, v44, v45
v_cvt_pk_bf16_f32 v98, v46, v47
v_cvt_pk_bf16_f32 v99, v48, v49
buffer_store_dwordx4 v[92:95], v1, s[44:47], s56 offen sc1
buffer_store_dwordx4 v[96:99], v1, s[44:47], s56 offen offset:1024 sc1
s_or_b32 s54, s48, s49
s_cmp_eq_u32 s54, 0
s_cbranch_scc0 noepoch_30
v_mov_b32 v108, s52
s_mov_b64 exec, 1
buffer_store_dword v108, off, s[44:47], 0 sc1
s_mov_b64 exec, -1
noepoch_30:
s_lshl_b32 s54, s49, 11
s_add_u32 s54, s54, 0x22000
v_add_u32 v82, s54, v1
ds_write_b128 v82, v[92:95]
ds_write_b128 v82, v[96:99] offset:1024
s_waitcnt lgkmcnt(0)
s_barrier
s_waitcnt vmcnt(44)
v_add_f32 v112, v112, v116
v_add_f32 v113, v113, v117
v_add_f32 v114, v114, v118
v_add_f32 v115, v115, v119
v_add_f32_dpp v112, v112, v112 row_ror:8 row_mask:0xf bank_mask:0xf
v_add_f32_dpp v113, v113, v113 row_ror:8 row_mask:0xf bank_mask:0xf
v_add_f32_dpp v114, v114, v114 row_ror:8 row_mask:0xf bank_mask:0xf
v_add_f32_dpp v115, v115, v115 row_ror:8 row_mask:0xf bank_mask:0xf
v_mov_b32 v116, v112
v_mov_b32 v117, v113
v_mov_b32 v118, v114
v_mov_b32 v119, v115
v_permlane16_swap_b32 v112, v116
v_permlane16_swap_b32 v113, v117
v_permlane16_swap_b32 v114, v118
v_permlane16_swap_b32 v115, v119
v_add_f32 v112, v112, v116
v_add_f32 v113, v113, v117
v_add_f32 v114, v114, v118
v_add_f32 v115, v115, v119
v_mov_b32 v116, v112
v_mov_b32 v117, v113
v_mov_b32 v118, v114
v_mov_b32 v119, v115
v_permlane32_swap_b32 v112, v116
v_permlane32_swap_b32 v113, v117
v_permlane32_swap_b32 v114, v118
v_permlane32_swap_b32 v115, v119
v_add_f32 v112, v112, v116
v_add_f32 v113, v113, v117
v_add_f32 v114, v114, v118
v_add_f32 v115, v115, v119
s_waitcnt vmcnt(42)
v_add_f32 v120, v120, v124
v_add_f32 v121, v121, v125
v_add_f32 v122, v122, v126
v_add_f32 v123, v123, v127
v_add_f32_dpp v120, v120, v120 row_ror:8 row_mask:0xf bank_mask:0xf
v_add_f32_dpp v121, v121, v121 row_ror:8 row_mask:0xf bank_mask:0xf
v_add_f32_dpp v122, v122, v122 row_ror:8 row_mask:0xf bank_mask:0xf
v_add_f32_dpp v123, v123, v123 row_ror:8 row_mask:0xf bank_mask:0xf
v_mov_b32 v124, v120
v_mov_b32 v125, v121
v_mov_b32 v126, v122
v_mov_b32 v127, v123
v_permlane16_swap_b32 v120, v124
v_permlane16_swap_b32 v121, v125
v_permlane16_swap_b32 v122, v126
v_permlane16_swap_b32 v123, v127
v_add_f32 v120, v120, v124
v_add_f32 v121, v121, v125
v_add_f32 v122, v122, v126
v_add_f32 v123, v123, v127
v_mov_b32 v124, v120
v_mov_b32 v125, v121
v_mov_b32 v126, v122
v_mov_b32 v127, v123
s_lshl_b32 s54, s50, 15
s_lshl_b32 s55, s49, 11
s_add_u32 s54, s54, s55
s_add_u32 s54, s54, 0x200000
s_xor_b32 s55, s51, 1
s_lshl_b32 s55, s55, 13
s_add_u32 s76, s55, s54
s_xor_b32 s55, s51, 2
s_lshl_b32 s55, s55, 13
s_add_u32 s77, s55, s54
s_xor_b32 s55, s51, 3
s_lshl_b32 s55, s55, 13
s_add_u32 s78, s55, s54
buffer_load_dwordx4 v[50:53], v1, s[44:47], s76 offen offset:0 sc1
buffer_load_dwordx4 v[54:57], v1, s[44:47], s76 offen offset:1024 sc1
buffer_load_dwordx4 v[58:61], v1, s[44:47], s77 offen offset:0 sc1
buffer_load_dwordx4 v[62:65], v1, s[44:47], s77 offen offset:1024 sc1
buffer_load_dwordx4 v[66:69], v1, s[44:47], s78 offen offset:0 sc1
buffer_load_dwordx4 v[70:73], v1, s[44:47], s78 offen offset:1024 sc1
v_add_u32 v83, 0x22000, v1
ds_read_b128 v[18:21], v83 offset:0
ds_read_b128 v[22:25], v83 offset:1024
ds_read_b128 v[26:29], v83 offset:2048
ds_read_b128 v[30:33], v83 offset:3072
ds_read_b128 v[34:37], v83 offset:4096
ds_read_b128 v[38:41], v83 offset:5120
ds_read_b128 v[42:45], v83 offset:6144
ds_read_b128 v[46:49], v83 offset:7168
s_waitcnt lgkmcnt(7)
v_mfma_f32_32x32x16_bf16 v[2:17], v[128:131], v[18:21], 0
v_permlane32_swap_b32 v120, v124
v_permlane32_swap_b32 v121, v125
v_permlane32_swap_b32 v122, v126
v_permlane32_swap_b32 v123, v127
v_add_f32 v120, v120, v124
v_add_f32 v121, v121, v125
s_waitcnt lgkmcnt(6)
v_mfma_f32_32x32x16_bf16 v[2:17], v[132:135], v[22:25], v[2:17]
v_add_f32 v122, v122, v126
v_add_f32 v123, v123, v127
s_mov_b32 s54, 0xff00ff00
s_mov_b32 s55, 0xff00ff00
s_mov_b32 s56, 0xffff0000
s_waitcnt lgkmcnt(5)
v_mfma_f32_32x32x16_bf16 v[2:17], v[136:139], v[26:29], v[2:17]
s_mov_b32 s57, 0xffff0000
s_mov_b32 s58, 0
s_mov_b32 s59, -1
v_cndmask_b32 v112, v112, v120, s[58:59]
v_cndmask_b32 v113, v113, v121, s[58:59]
v_cndmask_b32 v114, v114, v122, s[58:59]
s_waitcnt lgkmcnt(4)
v_mfma_f32_32x32x16_bf16 v[2:17], v[140:143], v[30:33], v[2:17]
v_cndmask_b32 v115, v115, v123, s[58:59]
v_cndmask_b32 v112, v112, v113, s[54:55]
v_cndmask_b32 v114, v114, v115, s[54:55]
v_cndmask_b32 v112, v112, v114, s[56:57]
v_cvt_pk_bf16_f32 v116, v112, v112
s_waitcnt lgkmcnt(3)
v_mfma_f32_32x32x16_bf16 v[2:17], v[144:147], v[34:37], v[2:17]
v_lshlrev_b32 v116, 16, v116
v_sub_f32 v113, v112, v116
v_cvt_pk_bf16_f32 v116, v112, v113
v_and_b32 v117, s82, v116
v_sub_f32 v113, v113, v117
v_mov_b32 v118, 0
s_waitcnt lgkmcnt(2)
v_mfma_f32_32x32x16_bf16 v[2:17], v[148:151], v[38:41], v[2:17]
v_mov_b32 v119, 0
v_cvt_pk_bf16_f32 v117, v113, v118
v_and_b32 v108, 7, v0
v_lshlrev_b32 v108, 6, v108
v_bfe_u32 v109, v0, 3, 2
s_waitcnt lgkmcnt(1)
v_mfma_f32_32x32x16_bf16 v[2:17], v[152:155], v[42:45], v[2:17]
v_lshl_add_u32 v108, v109, 4, v108
s_lshl_b32 s54, s49, 9
s_add_u32 s55, s49, 4
s_min_u32 s55, s55, 6
s_lshl_b32 s55, s55, 9
v_mov_b32 v109, s54
s_waitcnt lgkmcnt(0)
v_mfma_f32_32x32x16_bf16 v[2:17], v[156:159], v[46:49], v[2:17]
v_mov_b32 v110, s55
v_cndmask_b32 v109, v109, v110, s[58:59]
v_add_u32 v108, v108, v109
v_add_u32 v108, 0x25040, v108
ds_write_b128 v108, v[116:119]
s_lshl_b32 s54, s49, 11
s_add_u32 s54, s54, 0x18000
v_add_u32 v82, s54, v1
s_waitcnt vmcnt(5)
v_max3_u32 v84, v50, v51, v52
s_waitcnt vmcnt(4)
v_max3_u32 v84, v84, v53, v54
v_max3_u32 v84, v84, v55, v56
s_waitcnt vmcnt(3)
v_max3_u32 v84, v84, v57, v58
v_max3_u32 v84, v84, v59, v60
s_waitcnt vmcnt(2)
v_max3_u32 v84, v84, v61, v62
v_max3_u32 v84, v84, v63, v64
s_waitcnt vmcnt(1)
v_max3_u32 v84, v84, v65, v66
v_max3_u32 v84, v84, v67, v68
s_waitcnt vmcnt(0)
v_max3_u32 v84, v84, v69, v70
v_max3_u32 v84, v84, v71, v72
v_max_u32 v84, v84, v73
v_cmp_eq_u32 vcc, -1, v84
s_nop 4
s_cbranch_vccnz gretry_32
gok_31:
ds_write_b128 v82, v[50:53] offset:8192
ds_write_b128 v82, v[54:57] offset:9216
ds_write_b128 v82, v[58:61] offset:16384
ds_write_b128 v82, v[62:65] offset:17408
ds_write_b128 v82, v[66:69] offset:24576
ds_write_b128 v82, v[70:73] offset:25600
v_add_u32 v83, 0x18000, v1
s_waitcnt lgkmcnt(0)
s_barrier
ds_read_b128 v[18:21], v83 offset:8192
ds_read_b128 v[22:25], v83 offset:9216
ds_read_b128 v[26:29], v83 offset:10240
ds_read_b128 v[30:33], v83 offset:11264
ds_read_b128 v[34:37], v83 offset:12288
ds_read_b128 v[38:41], v83 offset:13312
ds_read_b128 v[42:45], v83 offset:14336
ds_read_b128 v[46:49], v83 offset:15360
ds_read_b128 v[50:53], v83 offset:16384
ds_read_b128 v[54:57], v83 offset:17408
ds_read_b128 v[58:61], v83 offset:18432
ds_read_b128 v[62:65], v83 offset:19456
ds_read_b128 v[66:69], v83 offset:20480
ds_read_b128 v[70:73], v83 offset:21504
ds_read_b128 v[74:77], v83 offset:22528
ds_read_b128 v[78:81], v83 offset:23552
s_waitcnt lgkmcnt(15)
v_mfma_f32_32x32x16_bf16 v[2:17], v[160:163], v[18:21], v[2:17]
ds_read_b128 v[18:21], v83 offset:24576
s_add_u32 m0, s88, 0x10000
s_add_u32 s54, s87, 0x10000
buffer_load_dwordx4 v1, s[44:47], s54 offen offset:0 sc1 lds
s_waitcnt lgkmcnt(15)
v_mfma_f32_32x32x16_bf16 v[2:17], v[164:167], v[22:25], v[2:17]
ds_read_b128 v[22:25], v83 offset:25600
buffer_load_dwordx4 v1, s[44:47], s54 offen offset:1024 sc1 lds
s_waitcnt lgkmcnt(15)
v_mfma_f32_32x32x16_bf16 v[2:17], v[168:171], v[26:29], v[2:17]
ds_read_b128 v[26:29], v83 offset:26624
buffer_load_dwordx4 v1, s[44:47], s54 offen offset:2048 sc1 lds
s_lshl_b32 s78, s51, 2
s_add_u32 s78, s78, s49
s_waitcnt lgkmcnt(15)
v_mfma_f32_32x32x16_bf16 v[2:17], v[172:175], v[30:33], v[2:17]
ds_read_b128 v[30:33], v83 offset:27648
buffer_load_dwordx4 v1, s[44:47], s54 offen offset:3072 sc1 lds
s_lshl_b32 s76, s50, 15
s_lshl_b32 s77, s78, 11
s_waitcnt lgkmcnt(15)
v_mfma_f32_32x32x16_bf16 v[2:17], v[176:179], v[34:37], v[2:17]
ds_read_b128 v[34:37], v83 offset:28672
s_add_u32 m0, s88, 0x11000
s_add_u32 s54, s87, 0x11000
buffer_load_dwordx4 v1, s[44:47], s54 offen offset:0 sc1 lds
s_add_u32 s76, s76, s77
s_add_u32 s76, s76, 0x400000
s_waitcnt lgkmcnt(15)
v_mfma_f32_32x32x16_bf16 v[2:17], v[180:183], v[38:41], v[2:17]
ds_read_b128 v[38:41], v83 offset:29696
buffer_load_dwordx4 v1, s[44:47], s54 offen offset:1024 sc1 lds
s_lshl_b32 s77, s50, 12
s_lshr_b32 s79, s78, 1
s_waitcnt lgkmcnt(15)
v_mfma_f32_32x32x16_bf16 v[2:17], v[184:187], v[42:45], v[2:17]
ds_read_b128 v[42:45], v83 offset:30720
buffer_load_dwordx4 v1, s[44:47], s54 offen offset:2048 sc1 lds
s_lshl_b32 s79, s79, 9
s_add_u32 s77, s77, s79
s_waitcnt lgkmcnt(15)
v_mfma_f32_32x32x16_bf16 v[2:17], v[188:191], v[46:49], v[2:17]
ds_read_b128 v[46:49], v83 offset:31744
buffer_load_dwordx4 v1, s[44:47], s54 offen offset:3072 sc1 lds
s_and_b32 s79, s78, 1
s_lshl_b32 s79, s79, 3
s_waitcnt lgkmcnt(15)
v_mfma_f32_32x32x16_bf16 v[2:17], v[192:195], v[50:53], v[2:17]
s_add_u32 s77, s77, s79
s_add_u32 s77, s77, 0x70000
s_waitcnt lgkmcnt(14)
v_mfma_f32_32x32x16_bf16 v[2:17], v[196:199], v[54:57], v[2:17]
v_mov_b32 v88, 0x3f803f80
v_mov_b32 v84, 0
s_waitcnt lgkmcnt(13)
v_mfma_f32_32x32x16_bf16 v[2:17], v[200:203], v[58:61], v[2:17]
v_mov_b32 v85, 0
v_mov_b32 v86, 0
s_waitcnt lgkmcnt(12)
v_mfma_f32_32x32x16_bf16 v[2:17], v[204:207], v[62:65], v[2:17]
v_mov_b32 v87, 0
v_lshlrev_b32 v89, 4, v0
s_waitcnt lgkmcnt(11)
v_mfma_f32_32x32x16_bf16 v[2:17], v[208:211], v[66:69], v[2:17]
v_lshlrev_b32 v108, 16, v100
v_and_b32 v109, s82, v100
s_waitcnt lgkmcnt(10)
v_mfma_f32_32x32x16_bf16 v[2:17], v[212:215], v[70:73], v[2:17]
v_lshlrev_b32 v110, 16, v101
v_and_b32 v111, s82, v101
s_waitcnt lgkmcnt(9)
v_mfma_f32_32x32x16_bf16 v[2:17], v[216:219], v[74:77], v[2:17]
v_lshlrev_b32 v112, 16, v102
v_and_b32 v113, s82, v102
s_waitcnt lgkmcnt(8)
v_mfma_f32_32x32x16_bf16 v[2:17], v[220:223], v[78:81], v[2:17]
v_lshlrev_b32 v114, 16, v103
v_and_b32 v115, s82, v103
s_waitcnt lgkmcnt(7)
v_mfma_f32_32x32x16_bf16 v[2:17], v[224:227], v[18:21], v[2:17]
v_lshlrev_b32 v116, 16, v104
v_and_b32 v117, s82, v104
s_waitcnt lgkmcnt(6)
v_mfma_f32_32x32x16_bf16 v[2:17], v[228:231], v[22:25], v[2:17]
v_lshlrev_b32 v118, 16, v105
v_and_b32 v119, s82, v105
s_waitcnt lgkmcnt(5)
v_mfma_f32_32x32x16_bf16 v[2:17], v[232:235], v[26:29], v[2:17]
v_lshlrev_b32 v120, 16, v106
v_and_b32 v121, s82, v106
s_waitcnt lgkmcnt(4)
v_mfma_f32_32x32x16_bf16 v[2:17], v[236:239], v[30:33], v[2:17]
v_lshlrev_b32 v122, 16, v107
v_and_b32 v123, s82, v107
s_waitcnt lgkmcnt(3)
v_mfma_f32_32x32x16_bf16 v[2:17], v[240:243], v[34:37], v[2:17]
s_waitcnt lgkmcnt(2)
v_mfma_f32_32x32x16_bf16 v[2:17], v[244:247], v[38:41], v[2:17]
s_waitcnt lgkmcnt(1)
v_mfma_f32_32x32x16_bf16 v[2:17], v[248:251], v[42:45], v[2:17]
s_waitcnt lgkmcnt(0)
v_mfma_f32_32x32x16_bf16 v[2:17], v[252:255], v[46:49], v[2:17]
s_nop 7
s_nop 3
v_cvt_pk_bf16_f32 v26, 0, v2
v_cvt_pk_bf16_f32 v27, 0, v3
v_cvt_pk_bf16_f32 v28, 0, v4
v_cvt_pk_bf16_f32 v29, 0, v5
v_cvt_pk_bf16_f32 v30, 0, v6
v_cvt_pk_bf16_f32 v31, 0, v7
v_cvt_pk_bf16_f32 v32, 0, v8
v_cvt_pk_bf16_f32 v33, 0, v9
v_cvt_pk_bf16_f32 v34, 0, v10
v_cvt_pk_bf16_f32 v35, 0, v11
v_cvt_pk_bf16_f32 v36, 0, v12
v_cvt_pk_bf16_f32 v37, 0, v13
v_cvt_pk_bf16_f32 v38, 0, v14
v_cvt_pk_bf16_f32 v39, 0, v15
v_cvt_pk_bf16_f32 v40, 0, v16
v_cvt_pk_bf16_f32 v41, 0, v17
v_pk_add_f32 v[26:27], v[26:27], v[108:109]
v_pk_add_f32 v[28:29], v[28:29], v[110:111]
v_pk_add_f32 v[30:31], v[30:31], v[112:113]
v_pk_add_f32 v[32:33], v[32:33], v[114:115]
v_pk_add_f32 v[34:35], v[34:35], v[116:117]
v_pk_add_f32 v[36:37], v[36:37], v[118:119]
v_pk_add_f32 v[38:39], v[38:39], v[120:121]
v_pk_add_f32 v[40:41], v[40:41], v[122:123]
v_cvt_pk_bf16_f32 v18, v26, v27
v_cvt_pk_bf16_f32 v19, v28, v29
v_cvt_pk_bf16_f32 v20, v30, v31
v_cvt_pk_bf16_f32 v21, v32, v33
v_cvt_pk_bf16_f32 v22, v34, v35
v_cvt_pk_bf16_f32 v23, v36, v37
v_cvt_pk_bf16_f32 v24, v38, v39
v_cvt_pk_bf16_f32 v25, v40, v41
buffer_store_dwordx4 v[18:21], v1, s[44:47], s76 offen sc1
buffer_store_dwordx4 v[22:25], v1, s[44:47], s76 offen offset:1024 sc1
v_dot2_f32_bf16 v84, v18, v88, v84
v_dot2_f32_bf16 v85, v18, v18, v85
v_dot2_f32_bf16 v86, v19, v88, v86
v_dot2_f32_bf16 v87, v19, v19, v87
v_dot2_f32_bf16 v84, v20, v88, v84
v_dot2_f32_bf16 v85, v20, v20, v85
v_dot2_f32_bf16 v86, v21, v88, v86
v_dot2_f32_bf16 v87, v21, v21, v87
v_dot2_f32_bf16 v84, v22, v88, v84
v_dot2_f32_bf16 v85, v22, v22, v85
v_dot2_f32_bf16 v86, v23, v88, v86
v_dot2_f32_bf16 v87, v23, v23, v87
v_dot2_f32_bf16 v84, v24, v88, v84
v_dot2_f32_bf16 v85, v24, v24, v85
v_dot2_f32_bf16 v86, v25, v88, v86
v_dot2_f32_bf16 v87, v25, v25, v87
s_nop 1
v_add_f32 v84, v84, v86
v_add_f32 v85, v85, v87
v_mov_b32 v86, v84
v_mov_b32 v87, v85
s_nop 0
v_permlane32_swap_b32 v84, v86
v_permlane32_swap_b32 v85, v87
v_add_f32 v84, v84, v86
v_add_f32 v85, v85, v87
s_mov_b64 exec, 0xffffffff
buffer_store_dwordx2 v[84:85], v89, s[44:47], s77 offen sc1
s_mov_b64 exec, -1
s_waitcnt lgkmcnt(0)
s_barrier
s_add_u32 m0, s88, 0x18000
s_add_u32 s54, s87, 0x18000
buffer_load_dwordx4 v1, s[44:47], s54 offen offset:0 sc1 lds
buffer_load_dwordx4 v1, s[44:47], s54 offen offset:1024 sc1 lds
buffer_load_dwordx4 v1, s[44:47], s54 offen offset:2048 sc1 lds
buffer_load_dwordx4 v1, s[44:47], s54 offen offset:3072 sc1 lds
s_add_u32 m0, s88, 0x19000
s_add_u32 s54, s87, 0x19000
buffer_load_dwordx4 v1, s[44:47], s54 offen offset:0 sc1 lds
buffer_load_dwordx4 v1, s[44:47], s54 offen offset:1024 sc1 lds
buffer_load_dwordx4 v1, s[44:47], s54 offen offset:2048 sc1 lds
buffer_load_dwordx4 v1, s[44:47], s54 offen offset:3072 sc1 lds
s_lshl_b32 s76, s72, 2
s_add_u32 s76, s76, s49
v_and_b32 v121, 31, v0
v_lshrrev_b32 v120, 5, v0
v_lshlrev_b32 v121, 4, v121
v_lshl_add_u32 v121, v120, 11, v121
v_add_u32 v114, 0x10000, v1
v_and_b32 v115, 31, v0
v_lshlrev_b32 v115, 4, v115
v_add_u32 v115, 0x25040, v115
s_mul_i32 s77, s76, 114688
s_mul_i32 s55, s73, 7168
s_add_u32 s77, s77, s55
s_add_u32 s77, s77, 0xc00000
s_lshl_b32 s78, s76, 12
s_add_u32 s78, s78, 0x70000
s_lshl_b32 s79, s76, 15
s_add_u32 s79, s79, 0x400000
s_mul_i32 s60, s74, 229376
s_mul_i32 s62, s49, 28672
s_add_u32 s60, s60, s62
s_add_u32 s60, s60, 0xc00000
s_bitcmp1_b32 s49, 0
s_cselect_b32 s62, 0x1c000, 0
s_cselect_b32 s61, 0, 0x1c000
s_add_u32 s61, s61, s60
s_add_u32 s60, s60, s62
s_mov_b32 s63, 0
buffer_load_dwordx4 v[82:85], v121, s[44:47], s78 offen offset:0 sc1
buffer_load_dwordx4 v[86:89], v121, s[44:47], s78 offen offset:512 sc1
buffer_load_dwordx4 v[90:93], v121, s[44:47], s78 offen offset:1024 sc1
buffer_load_dwordx4 v[94:97], v121, s[44:47], s78 offen offset:1536 sc1
buffer_load_dwordx4 v[128:131], v1, s[44:47], s79 offen offset:0 sc1
buffer_load_dwordx4 v[132:135], v1, s[44:47], s79 offen offset:1024 sc1
buffer_load_dwordx4 v[136:139], v1, s[44:47], s79 offen offset:2048 sc1
buffer_load_dwordx4 v[140:143], v1, s[44:47], s79 offen offset:3072 sc1
s_add_u32 s62, s79, 0x1000
buffer_load_dwordx4 v[144:147], v1, s[44:47], s62 offen offset:0 sc1
buffer_load_dwordx4 v[148:151], v1, s[44:47], s62 offen offset:1024 sc1
buffer_load_dwordx4 v[152:155], v1, s[44:47], s62 offen offset:2048 sc1
buffer_load_dwordx4 v[156:159], v1, s[44:47], s62 offen offset:3072 sc1
s_add_u32 s62, s79, 0x2000
buffer_load_dwordx4 v[160:163], v1, s[44:47], s62 offen offset:0 sc1
buffer_load_dwordx4 v[164:167], v1, s[44:47], s62 offen offset:1024 sc1
buffer_load_dwordx4 v[168:171], v1, s[44:47], s62 offen offset:2048 sc1
buffer_load_dwordx4 v[172:175], v1, s[44:47], s62 offen offset:3072 sc1
s_add_u32 s84, s60, 0x1000
s_add_u32 s85, s61, 0x1000
s_add_u32 s86, s60, 0x2000
s_add_u32 s91, s61, 0x2000
s_add_u32 s92, s60, 0x3000
s_add_u32 s93, s61, 0x3000
s_add_u32 s94, s60, 0x4000
s_add_u32 s95, s61, 0x4000
s_add_u32 s96, s60, 0x5000
s_add_u32 s97, s61, 0x5000
s_add_u32 s98, s60, 0x6000
s_add_u32 s99, s61, 0x6000
s_waitcnt vmcnt(15)
v_max3_u32 v120, v82, v83, v84
s_waitcnt vmcnt(14)
v_max3_u32 v120, v120, v85, v86
v_max3_u32 v120, v120, v87, v88
s_waitcnt vmcnt(13)
v_max3_u32 v120, v120, v89, v90
v_max3_u32 v120, v120, v91, v92
s_waitcnt vmcnt(12)
v_max3_u32 v120, v120, v93, v94
v_max3_u32 v120, v120, v95, v96
s_waitcnt vmcnt(11)
v_max3_u32 v120, v120, v97, v128
v_max3_u32 v120, v120, v129, v130
s_waitcnt vmcnt(10)
v_max3_u32 v120, v120, v131, v132
v_max3_u32 v120, v120, v133, v134
s_waitcnt vmcnt(9)
v_max3_u32 v120, v120, v135, v136
v_max3_u32 v120, v120, v137, v138
s_waitcnt vmcnt(8)
v_max3_u32 v120, v120, v139, v140
v_max3_u32 v120, v120, v141, v142
v_max_u32 v120, v120, v143
v_cmp_eq_u32 vcc, -1, v120
s_nop 4
s_cbranch_vccnz hretry_34
hok_33:
ds_read_b128 v[50:53], v1 offset:0
ds_read_b128 v[54:57], v1 offset:32768
ds_read_b128 v[58:61], v1 offset:1024
ds_read_b128 v[62:65], v1 offset:33792
ds_read_b128 v[66:69], v1 offset:2048
ds_read_b128 v[70:73], v1 offset:34816
ds_read_b128 v[74:77], v1 offset:3072
ds_read_b128 v[78:81], v1 offset:35840
s_add_u32 s62, s79, 0x3000
buffer_load_dwordx4 v[176:179], v1, s[44:47], s62 offen offset:0 sc1
s_waitcnt lgkmcnt(4)
v_mfma_f32_32x32x16_bf16 v[2:17], v[50:53], v[128:131], 0
ds_read_b128 v[50:53], v1 offset:4096
v_mfma_f32_32x32x16_bf16 v[18:33], v[54:57], v[128:131], 0
ds_read_b128 v[54:57], v1 offset:36864
buffer_load_dwordx4 v[180:183], v1, s[44:47], s62 offen offset:1024 sc1
v_mfma_f32_32x32x16_bf16 v[2:17], v[58:61], v[132:135], v[2:17]
ds_read_b128 v[58:61], v1 offset:5120
v_add_f32 v82, v82, v90
v_add_f32 v83, v83, v91
v_mfma_f32_32x32x16_bf16 v[18:33], v[62:65], v[132:135], v[18:33]
ds_read_b128 v[62:65], v1 offset:37888
v_add_f32 v84, v84, v92
v_add_f32 v85, v85, v93
buffer_load_dwordx4 v[184:187], v1, s[44:47], s62 offen offset:2048 sc1
s_waitcnt lgkmcnt(4)
v_mfma_f32_32x32x16_bf16 v[2:17], v[66:69], v[136:139], v[2:17]
ds_read_b128 v[66:69], v1 offset:6144
v_add_f32 v86, v86, v94
v_add_f32 v87, v87, v95
v_mfma_f32_32x32x16_bf16 v[18:33], v[70:73], v[136:139], v[18:33]
ds_read_b128 v[70:73], v1 offset:38912
v_add_f32 v88, v88, v96
v_add_f32 v89, v89, v97
buffer_load_dwordx4 v[188:191], v1, s[44:47], s62 offen offset:3072 sc1
v_mfma_f32_32x32x16_bf16 v[2:17], v[74:77], v[140:143], v[2:17]
ds_read_b128 v[74:77], v1 offset:7168
v_add_f32 v82, v82, v86
v_add_f32 v83, v83, v87
v_mfma_f32_32x32x16_bf16 v[18:33], v[78:81], v[140:143], v[18:33]
ds_read_b128 v[78:81], v1 offset:39936
v_add_f32 v84, v84, v88
v_add_f32 v85, v85, v89
s_add_u32 s62, s79, 0x4000
buffer_load_dwordx4 v[192:195], v1, s[44:47], s62 offen offset:0 sc1
s_waitcnt vmcnt(11)
s_waitcnt lgkmcnt(4)
v_mfma_f32_32x32x16_bf16 v[2:17], v[50:53], v[144:147], v[2:17]
ds_read_b128 v[50:53], v1 offset:8192
v_add_f32 v82, v82, v84
v_add_f32 v83, v83, v85
v_mfma_f32_32x32x16_bf16 v[18:33], v[54:57], v[144:147], v[18:33]
ds_read_b128 v[54:57], v1 offset:40960
v_mov_b32 v84, v82
v_mov_b32 v85, v83
buffer_load_dwordx4 v[196:199], v1, s[44:47], s62 offen offset:1024 sc1
v_mfma_f32_32x32x16_bf16 v[2:17], v[58:61], v[148:151], v[2:17]
ds_read_b128 v[58:61], v1 offset:9216
v_permlane32_swap_b32 v82, v84
v_permlane32_swap_b32 v83, v85
v_mfma_f32_32x32x16_bf16 v[18:33], v[62:65], v[148:151], v[18:33]
ds_read_b128 v[62:65], v1 offset:41984
v_add_f32 v82, v82, v84
v_add_f32 v83, v83, v85
buffer_load_dwordx4 v[200:203], v1, s[44:47], s62 offen offset:2048 sc1
s_waitcnt vmcnt(11)
s_waitcnt lgkmcnt(4)
v_mfma_f32_32x32x16_bf16 v[2:17], v[66:69], v[152:155], v[2:17]
ds_read_b128 v[66:69], v1 offset:10240
v_mul_f32 v82, 0x3b000000, v82
v_mul_f32 v83, 0x3b000000, v83
v_mfma_f32_32x32x16_bf16 v[18:33], v[70:73], v[152:155], v[18:33]
ds_read_b128 v[70:73], v1 offset:43008
v_fma_f32 v83, -v82, v82, v83
v_max_f32 v83, 0, v83
buffer_load_dwordx4 v[204:207], v1, s[44:47], s62 offen offset:3072 sc1
v_mfma_f32_32x32x16_bf16 v[2:17], v[74:77], v[156:159], v[2:17]
ds_read_b128 v[74:77], v1 offset:11264
v_cvt_pk_bf16_f32 v84, v82, v83
v_lshlrev_b32 v82, 16, v84
v_mfma_f32_32x32x16_bf16 v[18:33], v[78:81], v[156:159], v[18:33]
ds_read_b128 v[78:81], v1 offset:44032
v_and_b32 v83, s82, v84
v_add_f32 v83, 0x3727c5ac, v83
s_add_u32 s62, s79, 0x5000
buffer_load_dwordx4 v[208:211], v1, s[44:47], s62 offen offset:0 sc1
s_waitcnt vmcnt(11)
s_waitcnt lgkmcnt(4)
v_mfma_f32_32x32x16_bf16 v[2:17], v[50:53], v[160:163], v[2:17]
ds_read_b128 v[50:53], v1 offset:12288
v_cvt_pk_bf16_f32 v83, v83, v83
v_lshlrev_b32 v83, 16, v83
v_mfma_f32_32x32x16_bf16 v[18:33], v[54:57], v[160:163], v[18:33]
ds_read_b128 v[54:57], v1 offset:45056
v_sqrt_f32 v83, v83
v_sub_f32 v110, 0, v82
buffer_load_dwordx4 v[212:215], v1, s[44:47], s62 offen offset:1024 sc1
v_mfma_f32_32x32x16_bf16 v[2:17], v[58:61], v[164:167], v[2:17]
ds_read_b128 v[58:61], v1 offset:13312
v_cvt_pk_bf16_f32 v83, v83, v83
v_lshlrev_b32 v83, 16, v83
v_mfma_f32_32x32x16_bf16 v[18:33], v[62:65], v[164:167], v[18:33]
ds_read_b128 v[62:65], v1 offset:46080
v_rcp_f32 v84, v83
v_mul_f32 v113, v83, v83
buffer_load_dwordx4 v[216:219], v1, s[44:47], s62 offen offset:2048 sc1
s_waitcnt vmcnt(11)
s_waitcnt lgkmcnt(4)
v_mfma_f32_32x32x16_bf16 v[2:17], v[66:69], v[168:171], v[2:17]
ds_read_b128 v[66:69], v1 offset:14336
v_mul_f32 v111, 0xc0135761, v84
v_mul_f32 v85, v84, v84
v_mfma_f32_32x32x16_bf16 v[18:33], v[70:73], v[168:171], v[18:33]
ds_read_b128 v[70:73], v1 offset:47104
v_mul_f32 v85, v85, v111
v_mul_f32 v112, 0x3d372713, v85
buffer_load_dwordx4 v[220:223], v1, s[44:47], s62 offen offset:3072 sc1
v_mfma_f32_32x32x16_bf16 v[2:17], v[74:77], v[172:175], v[2:17]
ds_read_b128 v[74:77], v1 offset:15360
v_cvt_pk_bf16_f32 v94, v110, v110
s_mov_b64 vcc, 0xffffffff
v_mfma_f32_32x32x16_bf16 v[18:33], v[78:81], v[172:175], v[18:33]
ds_read_b128 v[78:81], v1 offset:48128
v_cndmask_b32 v94, 0, v94, vcc
v_mov_b32 v95, v94
s_add_u32 s62, s79, 0x6000
buffer_load_dwordx4 v[224:227], v1, s[44:47], s62 offen offset:0 sc1
s_waitcnt vmcnt(11)
s_waitcnt lgkmcnt(4)
v_mfma_f32_32x32x16_bf16 v[2:17], v[50:53], v[176:179], v[2:17]
ds_read_b128 v[50:53], v1 offset:16384
v_mov_b32 v96, v94
v_mov_b32 v97, v94
v_mfma_f32_32x32x16_bf16 v[18:33], v[54:57], v[176:179], v[18:33]
ds_read_b128 v[54:57], v1 offset:49152
buffer_load_dwordx4 v[228:231], v1, s[44:47], s62 offen offset:1024 sc1
v_mfma_f32_32x32x16_bf16 v[2:17], v[58:61], v[180:183], v[2:17]
ds_read_b128 v[58:61], v1 offset:17408
v_mfma_f32_32x32x16_bf16 v[18:33], v[62:65], v[180:183], v[18:33]
ds_read_b128 v[62:65], v1 offset:50176
buffer_load_dwordx4 v[232:235], v1, s[44:47], s62 offen offset:2048 sc1
s_waitcnt vmcnt(11)
s_waitcnt lgkmcnt(4)
v_mfma_f32_32x32x16_bf16 v[2:17], v[66:69], v[184:187], v[2:17]
ds_read_b128 v[66:69], v1 offset:18432
v_mfma_f32_32x32x16_bf16 v[18:33], v[70:73], v[184:187], v[18:33]
ds_read_b128 v[70:73], v1 offset:51200
buffer_load_dwordx4 v[236:239], v1, s[44:47], s62 offen offset:3072 sc1
v_mfma_f32_32x32x16_bf16 v[2:17], v[74:77], v[188:191], v[2:17]
ds_read_b128 v[74:77], v1 offset:19456
v_mfma_f32_32x32x16_bf16 v[18:33], v[78:81], v[188:191], v[18:33]
ds_read_b128 v[78:81], v1 offset:52224
s_add_u32 s62, s79, 0x7000
buffer_load_dwordx4 v[240:243], v1, s[44:47], s62 offen offset:0 sc1
s_waitcnt vmcnt(11)
s_waitcnt lgkmcnt(4)
v_mfma_f32_32x32x16_bf16 v[2:17], v[50:53], v[192:195], v[2:17]
ds_read_b128 v[50:53], v1 offset:20480
v_mfma_f32_32x32x16_bf16 v[18:33], v[54:57], v[192:195], v[18:33]
ds_read_b128 v[54:57], v1 offset:53248
buffer_load_dwordx4 v[244:247], v1, s[44:47], s62 offen offset:1024 sc1
v_mfma_f32_32x32x16_bf16 v[2:17], v[58:61], v[196:199], v[2:17]
ds_read_b128 v[58:61], v1 offset:21504
v_mfma_f32_32x32x16_bf16 v[18:33], v[62:65], v[196:199], v[18:33]
ds_read_b128 v[62:65], v1 offset:54272
buffer_load_dwordx4 v[248:251], v1, s[44:47], s62 offen offset:2048 sc1
s_waitcnt vmcnt(11)
s_waitcnt lgkmcnt(4)
v_mfma_f32_32x32x16_bf16 v[2:17], v[66:69], v[200:203], v[2:17]
ds_read_b128 v[66:69], v1 offset:22528
v_mfma_f32_32x32x16_bf16 v[18:33], v[70:73], v[200:203], v[18:33]
ds_read_b128 v[70:73], v1 offset:55296
buffer_load_dwordx4 v[252:255], v1, s[44:47], s62 offen offset:3072 sc1
v_mfma_f32_32x32x16_bf16 v[2:17], v[74:77], v[204:207], v[2:17]
ds_read_b128 v[74:77], v1 offset:23552
v_mfma_f32_32x32x16_bf16 v[18:33], v[78:81], v[204:207], v[18:33]
ds_read_b128 v[78:81], v1 offset:56320
buffer_load_dwordx4 a[60:63], v1, s[44:47], s65 offen offset:2048 sc1
s_waitcnt vmcnt(11)
s_waitcnt lgkmcnt(4)
v_mfma_f32_32x32x16_bf16 v[2:17], v[50:53], v[208:211], v[2:17]
ds_read_b128 v[50:53], v1 offset:24576
v_mfma_f32_32x32x16_bf16 v[18:33], v[54:57], v[208:211], v[18:33]
ds_read_b128 v[54:57], v1 offset:57344
s_add_u32 s81, s89, 0x4000
buffer_load_dwordx4 a[64:67], v1, s[44:47], s81 offen offset:0 sc1
v_mfma_f32_32x32x16_bf16 v[2:17], v[58:61], v[212:215], v[2:17]
ds_read_b128 v[58:61], v1 offset:25600
v_mfma_f32_32x32x16_bf16 v[18:33], v[62:65], v[212:215], v[18:33]
ds_read_b128 v[62:65], v1 offset:58368
s_add_u32 s65, s90, 0x4000
buffer_load_dwordx4 a[68:71], v1, s[44:47], s65 offen offset:0 sc1
s_waitcnt vmcnt(11)
s_waitcnt lgkmcnt(4)
v_mfma_f32_32x32x16_bf16 v[2:17], v[66:69], v[216:219], v[2:17]
ds_read_b128 v[66:69], v1 offset:26624
v_mfma_f32_32x32x16_bf16 v[18:33], v[70:73], v[216:219], v[18:33]
ds_read_b128 v[70:73], v1 offset:59392
buffer_load_dwordx4 a[72:75], v1, s[44:47], s81 offen offset:2048 sc1
v_mfma_f32_32x32x16_bf16 v[2:17], v[74:77], v[220:223], v[2:17]
ds_read_b128 v[74:77], v1 offset:27648
v_mfma_f32_32x32x16_bf16 v[18:33], v[78:81], v[220:223], v[18:33]
ds_read_b128 v[78:81], v1 offset:60416
buffer_load_dwordx4 a[76:79], v1, s[44:47], s65 offen offset:2048 sc1
s_waitcnt vmcnt(11)
s_waitcnt lgkmcnt(4)
v_mfma_f32_32x32x16_bf16 v[2:17], v[50:53], v[224:227], v[2:17]
ds_read_b128 v[50:53], v1 offset:28672
v_mfma_f32_32x32x16_bf16 v[18:33], v[54:57], v[224:227], v[18:33]
ds_read_b128 v[54:57], v1 offset:61440
s_add_u32 s81, s89, 0x5000
buffer_load_dwordx4 a[80:83], v1, s[44:47], s81 offen offset:0 sc1
v_mfma_f32_32x32x16_bf16 v[2:17], v[58:61], v[228:231], v[2:17]
ds_read_b128 v[58:61], v1 offset:29696
v_mfma_f32_32x32x16_bf16 v[18:33], v[62:65], v[228:231], v[18:33]
ds_read_b128 v[62:65], v1 offset:62464
s_add_u32 s65, s90, 0x5000
buffer_load_dwordx4 a[84:87], v1, s[44:47], s65 offen offset:0 sc1
s_waitcnt vmcnt(11)
s_waitcnt lgkmcnt(4)
v_mfma_f32_32x32x16_bf16 v[2:17], v[66:69], v[232:235], v[2:17]
ds_read_b128 v[66:69], v1 offset:30720
v_mfma_f32_32x32x16_bf16 v[18:33], v[70:73], v[232:235], v[18:33]
ds_read_b128 v[70:73], v1 offset:63488
buffer_load_dwordx4 a[88:91], v1, s[44:47], s81 offen offset:2048 sc1
v_mfma_f32_32x32x16_bf16 v[2:17], v[74:77], v[236:239], v[2:17]
ds_read_b128 v[74:77], v1 offset:31744
v_mfma_f32_32x32x16_bf16 v[18:33], v[78:81], v[236:239], v[18:33]
ds_read_b128 v[78:81], v1 offset:64512
buffer_load_dwordx4 a[92:95], v1, s[44:47], s65 offen offset:2048 sc1
s_waitcnt vmcnt(11)
s_waitcnt lgkmcnt(4)
v_mfma_f32_32x32x16_bf16 v[2:17], v[50:53], v[240:243], v[2:17]
ds_read_b128 v[50:53], v115 offset:0
v_mfma_f32_32x32x16_bf16 v[18:33], v[54:57], v[240:243], v[18:33]
ds_read_b128 v[54:57], v115 offset:512
s_add_u32 s81, s89, 0x6000
buffer_load_dwordx4 a[96:99], v1, s[44:47], s81 offen offset:0 sc1
v_mfma_f32_32x32x16_bf16 v[2:17], v[58:61], v[244:247], v[2:17]
ds_read_b128 v[58:61], v115 offset:1024
v_mfma_f32_32x32x16_bf16 v[18:33], v[62:65], v[244:247], v[18:33]
ds_read_b128 v[62:65], v114 offset:0
s_add_u32 s65, s90, 0x6000
buffer_load_dwordx4 a[100:103], v1, s[44:47], s65 offen offset:0 sc1
s_waitcnt vmcnt(11)
s_waitcnt lgkmcnt(4)
v_mfma_f32_32x32x16_bf16 v[2:17], v[66:69], v[248:251], v[2:17]
ds_read_b128 v[66:69], v114 offset:1024
v_mfma_f32_32x32x16_bf16 v[18:33], v[70:73], v[248:251], v[18:33]
ds_read_b128 v[70:73], v114 offset:2048
buffer_load_dwordx4 a[104:107], v1, s[44:47], s81 offen offset:2048 sc1
v_mfma_f32_32x32x16_bf16 v[2:17], v[74:77], v[252:255], v[2:17]
ds_read_b128 v[74:77], v114 offset:3072
v_mfma_f32_32x32x16_bf16 v[18:33], v[78:81], v[252:255], v[18:33]
ds_read_b128 v[78:81], v114 offset:4096
s_waitcnt lgkmcnt(4)
v_mfma_f32_32x32x16_bf16 v[2:17], v[50:53], v[94:97], v[2:17]
ds_read_b128 v[50:53], v114 offset:5120
v_mfma_f32_32x32x16_bf16 v[18:33], v[54:57], v[94:97], v[18:33]
ds_read_b128 v[54:57], v114 offset:6144
s_nop 7
s_nop 0
v_cmp_u_f32 vcc, v2, v2
s_nop 4
s_cbranch_vccnz hslow_35
hgo_36:
s_barrier
v_mfma_f32_32x32x16_bf16 v[34:49], v[58:61], v[94:97], 0
ds_read_b128 v[58:61], v114 offset:7168
v_mfma_f32_32x32x16_bf16 v[34:49], v[62:65], v[128:131], v[34:49]
ds_read_b128 v[62:65], v114 offset:8192
s_nop 2
v_mul_f32 v106, v2, v2
v_mul_f32 v107, v3, v3
v_mul_f32 v108, v4, v4
s_waitcnt lgkmcnt(4)
v_mfma_f32_32x32x16_bf16 v[34:49], v[66:69], v[132:135], v[34:49]
ds_read_b128 v[66:69], v114 offset:9216
v_mul_f32 v109, v5, v5
v_fma_f32 v106, v106, v112, v111
v_fma_f32 v107, v107, v112, v111
s_add_u32 m0, s88, 0x0
s_add_u32 s78, s87, 0x20000
buffer_load_dwordx4 v1, s[44:47], s78 offen offset:0 sc1 lds
v_mfma_f32_32x32x16_bf16 v[34:49], v[70:73], v[136:139], v[34:49]
ds_read_b128 v[70:73], v114 offset:10240
v_fma_f32 v108, v108, v112, v111
v_fma_f32 v109, v109, v112, v111
v_mul_f32 v106, v106, v2
v_mfma_f32_32x32x16_bf16 v[34:49], v[74:77], v[140:143], v[34:49]
ds_read_b128 v[74:77], v114 offset:11264
v_mul_f32 v107, v107, v3
v_mul_f32 v108, v108, v4
v_mul_f32 v109, v109, v5
buffer_load_dwordx4 v1, s[44:47], s78 offen offset:1024 sc1 lds
v_mfma_f32_32x32x16_bf16 v[34:49], v[78:81], v[144:147], v[34:49]
ds_read_b128 v[78:81], v114 offset:12288
v_exp_f32 v106, v106
v_mul_f32 v102, v10, v2
v_exp_f32 v107, v107
s_waitcnt lgkmcnt(4)
v_mfma_f32_32x32x16_bf16 v[34:49], v[50:53], v[148:151], v[34:49]
ds_read_b128 v[50:53], v114 offset:13312
v_mul_f32 v103, v11, v3
v_exp_f32 v108, v108
v_mul_f32 v104, v12, v4
buffer_load_dwordx4 v1, s[44:47], s78 offen offset:2048 sc1 lds
v_mfma_f32_32x32x16_bf16 v[34:49], v[54:57], v[152:155], v[34:49]
ds_read_b128 v[54:57], v114 offset:14336
v_exp_f32 v109, v109
v_mul_f32 v105, v13, v5
v_fma_f32 v106, v106, v113, v113
v_mfma_f32_32x32x16_bf16 v[34:49], v[58:61], v[156:159], v[34:49]
ds_read_b128 v[58:61], v114 offset:15360
v_fma_f32 v107, v107, v113, v113
v_fma_f32 v108, v108, v113, v113
v_fma_f32 v109, v109, v113, v113
buffer_load_dwordx4 v1, s[44:47], s78 offen offset:3072 sc1 lds
v_mfma_f32_32x32x16_bf16 v[34:49], v[62:65], v[160:163], v[34:49]
ds_read_b128 v[62:65], v114 offset:16384
v_rcp_f32 v106, v106
v_rcp_f32 v107, v107
v_rcp_f32 v108, v108
s_waitcnt lgkmcnt(4)
v_mfma_f32_32x32x16_bf16 v[34:49], v[66:69], v[164:167], v[34:49]
ds_read_b128 v[66:69], v114 offset:17408
v_rcp_f32 v109, v109
v_mul_f32 v102, v102, v106
v_mul_f32 v103, v103, v107
s_add_u32 m0, s88, 0x1000
s_add_u32 s78, s87, 0x21000
buffer_load_dwordx4 v1, s[44:47], s78 offen offset:0 sc1 lds
v_mfma_f32_32x32x16_bf16 v[34:49], v[70:73], v[168:171], v[34:49]
ds_read_b128 v[70:73], v114 offset:18432
v_mul_f32 v104, v104, v108
v_mul_f32 v105, v105, v109
v_cvt_pk_bf16_f32 v116, v102, v103
v_mfma_f32_32x32x16_bf16 v[34:49], v[74:77], v[172:175], v[34:49]
ds_read_b128 v[74:77], v114 offset:19456
v_cvt_pk_bf16_f32 v117, v104, v105
v_mul_f32 v106, v6, v6
v_mul_f32 v107, v7, v7
buffer_load_dwordx4 v1, s[44:47], s78 offen offset:1024 sc1 lds
v_mfma_f32_32x32x16_bf16 v[34:49], v[78:81], v[176:179], v[34:49]
ds_read_b128 v[78:81], v114 offset:20480
v_mul_f32 v108, v8, v8
v_mul_f32 v109, v9, v9
v_fma_f32 v106, v106, v112, v111
s_waitcnt lgkmcnt(4)
v_mfma_f32_32x32x16_bf16 v[34:49], v[50:53], v[180:183], v[34:49]
ds_read_b128 v[50:53], v114 offset:21504
v_fma_f32 v107, v107, v112, v111
v_fma_f32 v108, v108, v112, v111
v_fma_f32 v109, v109, v112, v111
buffer_load_dwordx4 v1, s[44:47], s78 offen offset:2048 sc1 lds
v_mfma_f32_32x32x16_bf16 v[34:49], v[54:57], v[184:187], v[34:49]
ds_read_b128 v[54:57], v114 offset:22528
v_mul_f32 v106, v106, v6
v_mul_f32 v107, v107, v7
v_mul_f32 v108, v108, v8
v_mfma_f32_32x32x16_bf16 v[34:49], v[58:61], v[188:191], v[34:49]
ds_read_b128 v[58:61], v114 offset:23552
v_mul_f32 v109, v109, v9
v_exp_f32 v106, v106
v_mul_f32 v102, v14, v6
buffer_load_dwordx4 v1, s[44:47], s78 offen offset:3072 sc1 lds
v_mfma_f32_32x32x16_bf16 v[34:49], v[62:65], v[192:195], v[34:49]
ds_read_b128 v[62:65], v114 offset:24576
v_exp_f32 v107, v107
v_mul_f32 v103, v15, v7
v_exp_f32 v108, v108
s_waitcnt lgkmcnt(4)
v_mfma_f32_32x32x16_bf16 v[34:49], v[66:69], v[196:199], v[34:49]
ds_read_b128 v[66:69], v114 offset:25600
v_mul_f32 v104, v16, v8
v_exp_f32 v109, v109
v_mul_f32 v105, v17, v9
s_add_u32 m0, s88, 0x8000
s_add_u32 s78, s87, 0x28000
buffer_load_dwordx4 v1, s[44:47], s78 offen offset:0 sc1 lds
v_mfma_f32_32x32x16_bf16 v[34:49], v[70:73], v[200:203], v[34:49]
ds_read_b128 v[70:73], v114 offset:26624
v_fma_f32 v106, v106, v113, v113
v_fma_f32 v107, v107, v113, v113
v_fma_f32 v108, v108, v113, v113
v_mfma_f32_32x32x16_bf16 v[34:49], v[74:77], v[204:207], v[34:49]
ds_read_b128 v[74:77], v114 offset:27648
v_fma_f32 v109, v109, v113, v113
v_rcp_f32 v106, v106
v_rcp_f32 v107, v107
buffer_load_dwordx4 v1, s[44:47], s78 offen offset:1024 sc1 lds
v_mfma_f32_32x32x16_bf16 v[34:49], v[78:81], v[208:211], v[34:49]
ds_read_b128 v[78:81], v114 offset:28672
v_rcp_f32 v108, v108
v_rcp_f32 v109, v109
v_mul_f32 v102, v102, v106
s_waitcnt lgkmcnt(4)
v_mfma_f32_32x32x16_bf16 v[34:49], v[50:53], v[212:215], v[34:49]
ds_read_b128 v[50:53], v114 offset:29696
v_mul_f32 v103, v103, v107
v_mul_f32 v104, v104, v108
v_mul_f32 v105, v105, v109
buffer_load_dwordx4 v1, s[44:47], s78 offen offset:2048 sc1 lds
v_mfma_f32_32x32x16_bf16 v[34:49], v[54:57], v[216:219], v[34:49]
ds_read_b128 v[54:57], v114 offset:30720
v_cvt_pk_bf16_f32 v118, v102, v103
v_cvt_pk_bf16_f32 v119, v104, v105
buffer_store_dwordx4 v[116:119], v1, s[44:47], s77 offen sc1
v_mfma_f32_32x32x16_bf16 v[34:49], v[58:61], v[220:223], v[34:49]
ds_read_b128 v[58:61], v114 offset:31744
s_add_u32 s77, s77, 1024
v_mul_f32 v106, v18, v18
buffer_load_dwordx4 v1, s[44:47], s78 offen offset:3072 sc1 lds
v_mfma_f32_32x32x16_bf16 v[34:49], v[62:65], v[224:227], v[34:49]
ds_read_b128 v[62:65], v115 offset:1536
v_mul_f32 v107, v19, v19
v_mul_f32 v108, v20, v20
v_mul_f32 v109, v21, v21
s_waitcnt lgkmcnt(4)
v_mfma_f32_32x32x16_bf16 v[34:49], v[66:69], v[228:231], v[34:49]
ds_read_b128 v[66:69], v114 offset:32768
v_fma_f32 v106, v106, v112, v111
v_fma_f32 v107, v107, v112, v111
v_fma_f32 v108, v108, v112, v111
s_add_u32 m0, s88, 0x9000
s_add_u32 s78, s87, 0x29000
buffer_load_dwordx4 v1, s[44:47], s78 offen offset:0 sc1 lds
v_mfma_f32_32x32x16_bf16 v[34:49], v[70:73], v[232:235], v[34:49]
ds_read_b128 v[70:73], v114 offset:33792
v_fma_f32 v109, v109, v112, v111
v_mul_f32 v106, v106, v18
v_mul_f32 v107, v107, v19
v_mfma_f32_32x32x16_bf16 v[34:49], v[74:77], v[236:239], v[34:49]
ds_read_b128 v[74:77], v114 offset:34816
v_mul_f32 v108, v108, v20
v_mul_f32 v109, v109, v21
v_exp_f32 v106, v106
buffer_load_dwordx4 v1, s[44:47], s78 offen offset:1024 sc1 lds
v_mfma_f32_32x32x16_bf16 v[34:49], v[78:81], v[240:243], v[34:49]
ds_read_b128 v[78:81], v114 offset:35840
v_mul_f32 v102, v26, v18
v_exp_f32 v107, v107
v_mul_f32 v103, v27, v19
s_waitcnt lgkmcnt(4)
v_mfma_f32_32x32x16_bf16 v[34:49], v[50:53], v[244:247], v[34:49]
ds_read_b128 v[50:53], v114 offset:36864
v_exp_f32 v108, v108
v_mul_f32 v104, v28, v20
v_exp_f32 v109, v109
buffer_load_dwordx4 v1, s[44:47], s78 offen offset:2048 sc1 lds
v_mfma_f32_32x32x16_bf16 v[34:49], v[54:57], v[248:251], v[34:49]
ds_read_b128 v[54:57], v114 offset:37888
v_mul_f32 v105, v29, v21
v_fma_f32 v106, v106, v113, v113
v_fma_f32 v107, v107, v113, v113
v_mfma_f32_32x32x16_bf16 v[34:49], v[58:61], v[252:255], v[34:49]
ds_read_b128 v[58:61], v114 offset:38912
v_fma_f32 v108, v108, v113, v113
v_fma_f32 v109, v109, v113, v113
v_rcp_f32 v106, v106
buffer_load_dwordx4 v1, s[44:47], s78 offen offset:3072 sc1 lds
v_mfma_f32_32x32x16_bf16 v[2:17], v[62:65], v[94:97], 0
ds_read_b128 v[62:65], v114 offset:39936
s_waitcnt lgkmcnt(4)
v_mfma_f32_32x32x16_bf16 v[2:17], v[66:69], v[128:131], v[2:17]
ds_read_b128 v[66:69], v114 offset:40960
v_rcp_f32 v107, v107
v_rcp_f32 v108, v108
v_rcp_f32 v109, v109
v_mfma_f32_32x32x16_bf16 v[2:17], v[70:73], v[132:135], v[2:17]
ds_read_b128 v[70:73], v114 offset:41984
v_mul_f32 v102, v102, v106
v_mul_f32 v103, v103, v107
v_mul_f32 v104, v104, v108
buffer_load_dwordx4 a[108:111], v1, s[44:47], s65 offen offset:2048 sc1
v_mfma_f32_32x32x16_bf16 v[2:17], v[74:77], v[136:139], v[2:17]
ds_read_b128 v[74:77], v114 offset:43008
v_mul_f32 v105, v105, v109
v_cvt_pk_bf16_f32 v116, v102, v103
v_cvt_pk_bf16_f32 v117, v104, v105
v_mfma_f32_32x32x16_bf16 v[2:17], v[78:81], v[140:143], v[2:17]
ds_read_b128 v[78:81], v114 offset:44032
v_mul_f32 v106, v22, v22
v_mul_f32 v107, v23, v23
v_mul_f32 v108, v24, v24
s_add_u32 s81, s89, 0x7000
buffer_load_dwordx4 a[112:115], v1, s[44:47], s81 offen offset:0 sc1
s_waitcnt lgkmcnt(4)
v_mfma_f32_32x32x16_bf16 v[2:17], v[50:53], v[144:147], v[2:17]
ds_read_b128 v[50:53], v114 offset:45056
v_mul_f32 v109, v25, v25
v_fma_f32 v106, v106, v112, v111
v_fma_f32 v107, v107, v112, v111
v_mfma_f32_32x32x16_bf16 v[2:17], v[54:57], v[148:151], v[2:17]
ds_read_b128 v[54:57], v114 offset:46080
v_fma_f32 v108, v108, v112, v111
v_fma_f32 v109, v109, v112, v111
v_mul_f32 v106, v106, v22
v_mfma_f32_32x32x16_bf16 v[2:17], v[58:61], v[152:155], v[2:17]
ds_read_b128 v[58:61], v114 offset:47104
v_mul_f32 v107, v107, v23
v_mul_f32 v108, v108, v24
v_mul_f32 v109, v109, v25
v_mfma_f32_32x32x16_bf16 v[2:17], v[62:65], v[156:159], v[2:17]
ds_read_b128 v[62:65], v114 offset:48128
v_exp_f32 v106, v106
v_mul_f32 v102, v30, v22
v_exp_f32 v107, v107
s_waitcnt lgkmcnt(4)
v_mfma_f32_32x32x16_bf16 v[2:17], v[66:69], v[160:163], v[2:17]
ds_read_b128 v[66:69], v114 offset:49152
v_mul_f32 v103, v31, v23
v_exp_f32 v108, v108
v_mul_f32 v104, v32, v24
v_mfma_f32_32x32x16_bf16 v[2:17], v[70:73], v[164:167], v[2:17]
ds_read_b128 v[70:73], v114 offset:50176
v_exp_f32 v109, v109
v_mul_f32 v105, v33, v25
v_fma_f32 v106, v106, v113, v113
v_mfma_f32_32x32x16_bf16 v[2:17], v[74:77], v[168:171], v[2:17]
ds_read_b128 v[74:77], v114 offset:51200
v_fma_f32 v107, v107, v113, v113
v_fma_f32 v108, v108, v113, v113
v_fma_f32 v109, v109, v113, v113
v_mfma_f32_32x32x16_bf16 v[2:17], v[78:81], v[172:175], v[2:17]
ds_read_b128 v[78:81], v114 offset:52224
v_rcp_f32 v106, v106
v_rcp_f32 v107, v107
v_rcp_f32 v108, v108
s_waitcnt lgkmcnt(4)
v_mfma_f32_32x32x16_bf16 v[2:17], v[50:53], v[176:179], v[2:17]
ds_read_b128 v[50:53], v114 offset:53248
v_rcp_f32 v109, v109
v_mul_f32 v102, v102, v106
v_mul_f32 v103, v103, v107
v_mfma_f32_32x32x16_bf16 v[2:17], v[54:57], v[180:183], v[2:17]
ds_read_b128 v[54:57], v114 offset:54272
v_mul_f32 v104, v104, v108
v_mul_f32 v105, v105, v109
v_cvt_pk_bf16_f32 v118, v102, v103
v_mfma_f32_32x32x16_bf16 v[2:17], v[58:61], v[184:187], v[2:17]
ds_read_b128 v[58:61], v114 offset:55296
v_cvt_pk_bf16_f32 v119, v104, v105
buffer_store_dwordx4 v[116:119], v1, s[44:47], s77 offen sc1
s_add_u32 s77, s77, 1024
v_mfma_f32_32x32x16_bf16 v[2:17], v[62:65], v[188:191], v[2:17]
ds_read_b128 v[62:65], v114 offset:56320
v_mul_f32 v106, v34, v34
v_mul_f32 v107, v35, v35
s_waitcnt lgkmcnt(4)
v_mfma_f32_32x32x16_bf16 v[2:17], v[66:69], v[192:195], v[2:17]
ds_read_b128 v[66:69], v114 offset:57344
v_mul_f32 v108, v36, v36
v_mul_f32 v109, v37, v37
v_fma_f32 v106, v106, v112, v111
v_mfma_f32_32x32x16_bf16 v[2:17], v[70:73], v[196:199], v[2:17]
ds_read_b128 v[70:73], v114 offset:58368
v_fma_f32 v107, v107, v112, v111
v_fma_f32 v108, v108, v112, v111
v_fma_f32 v109, v109, v112, v111
v_mfma_f32_32x32x16_bf16 v[2:17], v[74:77], v[200:203], v[2:17]
ds_read_b128 v[74:77], v114 offset:59392
v_mul_f32 v106, v106, v34
v_mul_f32 v107, v107, v35
v_mul_f32 v108, v108, v36
v_mfma_f32_32x32x16_bf16 v[2:17], v[78:81], v[204:207], v[2:17]
ds_read_b128 v[78:81], v114 offset:60416
v_mul_f32 v109, v109, v37
v_exp_f32 v106, v106
v_mul_f32 v102, v42, v34
s_waitcnt lgkmcnt(4)
v_mfma_f32_32x32x16_bf16 v[2:17], v[50:53], v[208:211], v[2:17]
ds_read_b128 v[50:53], v114 offset:61440
v_exp_f32 v107, v107
v_mul_f32 v103, v43, v35
v_exp_f32 v108, v108
v_mfma_f32_32x32x16_bf16 v[2:17], v[54:57], v[212:215], v[2:17]
ds_read_b128 v[54:57], v114 offset:62464
v_mul_f32 v104, v44, v36
v_exp_f32 v109, v109
v_mul_f32 v105, v45, v37
v_mfma_f32_32x32x16_bf16 v[2:17], v[58:61], v[216:219], v[2:17]
ds_read_b128 v[58:61], v114 offset:63488
v_fma_f32 v106, v106, v113, v113
v_fma_f32 v107, v107, v113, v113
v_fma_f32 v108, v108, v113, v113
v_mfma_f32_32x32x16_bf16 v[2:17], v[62:65], v[220:223], v[2:17]
ds_read_b128 v[62:65], v114 offset:64512
v_fma_f32 v109, v109, v113, v113
v_rcp_f32 v106, v106
v_rcp_f32 v107, v107
s_waitcnt vmcnt(12)
s_barrier
s_waitcnt lgkmcnt(4)
v_mfma_f32_32x32x16_bf16 v[2:17], v[66:69], v[224:227], v[2:17]
ds_read_b128 v[66:69], v115 offset:2048
v_rcp_f32 v108, v108
v_rcp_f32 v109, v109
v_mul_f32 v102, v102, v106
v_mfma_f32_32x32x16_bf16 v[2:17], v[70:73], v[228:231], v[2:17]
ds_read_b128 v[70:73], v1 offset:0
v_mul_f32 v103, v103, v107
v_mul_f32 v104, v104, v108
v_mul_f32 v105, v105, v109
s_add_u32 m0, s88, 0x10000
s_add_u32 s78, s87, 0x30000
buffer_load_dwordx4 v1, s[44:47], s78 offen offset:0 sc1 lds
v_mfma_f32_32x32x16_bf16 v[2:17], v[74:77], v[232:235], v[2:17]
ds_read_b128 v[74:77], v1 offset:1024
v_cvt_pk_bf16_f32 v116, v102, v103
v_cvt_pk_bf16_f32 v117, v104, v105
v_mul_f32 v106, v38, v38
v_mfma_f32_32x32x16_bf16 v[2:17], v[78:81], v[236:239], v[2:17]
ds_read_b128 v[78:81], v1 offset:2048
v_mul_f32 v107, v39, v39
v_mul_f32 v108, v40, v40
v_mul_f32 v109, v41, v41
buffer_load_dwordx4 v1, s[44:47], s78 offen offset:1024 sc1 lds
s_waitcnt lgkmcnt(4)
v_mfma_f32_32x32x16_bf16 v[2:17], v[50:53], v[240:243], v[2:17]
ds_read_b128 v[50:53], v1 offset:3072
v_fma_f32 v106, v106, v112, v111
v_fma_f32 v107, v107, v112, v111
v_fma_f32 v108, v108, v112, v111
v_mfma_f32_32x32x16_bf16 v[2:17], v[54:57], v[244:247], v[2:17]
ds_read_b128 v[54:57], v1 offset:4096
v_fma_f32 v109, v109, v112, v111
v_mul_f32 v106, v106, v38
v_mul_f32 v107, v107, v39
buffer_load_dwordx4 v1, s[44:47], s78 offen offset:2048 sc1 lds
v_mfma_f32_32x32x16_bf16 v[2:17], v[58:61], v[248:251], v[2:17]
ds_read_b128 v[58:61], v1 offset:5120
v_mul_f32 v108, v108, v40
v_mul_f32 v109, v109, v41
v_exp_f32 v106, v106
v_mfma_f32_32x32x16_bf16 v[2:17], v[62:65], v[252:255], v[2:17]
ds_read_b128 v[62:65], v1 offset:6144
v_mul_f32 v102, v46, v38
v_exp_f32 v107, v107
v_mul_f32 v103, v47, v39
buffer_load_dwordx4 v1, s[44:47], s78 offen offset:3072 sc1 lds
s_waitcnt lgkmcnt(4)
v_mfma_f32_32x32x16_bf16 v[18:33], v[66:69], v[94:97], 0
ds_read_b128 v[66:69], v1 offset:7168
v_mfma_f32_32x32x16_bf16 v[18:33], v[70:73], v[128:131], v[18:33]
ds_read_b128 v[70:73], v1 offset:8192
v_exp_f32 v108, v108
v_mul_f32 v104, v48, v40
v_exp_f32 v109, v109
v_mfma_f32_32x32x16_bf16 v[18:33], v[74:77], v[132:135], v[18:33]
ds_read_b128 v[74:77], v1 offset:9216
v_mul_f32 v105, v49, v41
v_fma_f32 v106, v106, v113, v113
v_fma_f32 v107, v107, v113, v113
s_add_u32 m0, s88, 0x11000
s_add_u32 s78, s87, 0x31000
buffer_load_dwordx4 v1, s[44:47], s78 offen offset:0 sc1 lds
v_mfma_f32_32x32x16_bf16 v[18:33], v[78:81], v[136:139], v[18:33]
ds_read_b128 v[78:81], v1 offset:10240
v_fma_f32 v108, v108, v113, v113
v_fma_f32 v109, v109, v113, v113
v_rcp_f32 v106, v106
s_waitcnt lgkmcnt(4)
v_mfma_f32_32x32x16_bf16 v[18:33], v[50:53], v[140:143], v[18:33]
ds_read_b128 v[50:53], v1 offset:11264
v_rcp_f32 v107, v107
v_rcp_f32 v108, v108
v_rcp_f32 v109, v109
buffer_load_dwordx4 v1, s[44:47], s78 offen offset:1024 sc1 lds
v_mfma_f32_32x32x16_bf16 v[18:33], v[54:57], v[144:147], v[18:33]
ds_read_b128 v[54:57], v1 offset:12288
v_mul_f32 v102, v102, v106
v_mul_f32 v103, v103, v107
v_mul_f32 v104, v104, v108
v_mfma_f32_32x32x16_bf16 v[18:33], v[58:61], v[148:151], v[18:33]
ds_read_b128 v[58:61], v1 offset:13312
v_mul_f32 v105, v105, v109
v_cvt_pk_bf16_f32 v118, v102, v103
v_cvt_pk_bf16_f32 v119, v104, v105
buffer_load_dwordx4 v1, s[44:47], s78 offen offset:2048 sc1 lds
v_mfma_f32_32x32x16_bf16 v[18:33], v[62:65], v[152:155], v[18:33]
ds_read_b128 v[62:65], v1 offset:14336
buffer_store_dwordx4 v[116:119], v1, s[44:47], s77 offen sc1
s_add_u32 s77, s77, 1024
s_waitcnt lgkmcnt(4)
v_mfma_f32_32x32x16_bf16 v[18:33], v[66:69], v[156:159], v[18:33]
ds_read_b128 v[66:69], v1 offset:15360
v_mul_f32 v106, v2, v2
v_mul_f32 v107, v3, v3
v_mul_f32 v108, v4, v4
buffer_load_dwordx4 v1, s[44:47], s78 offen offset:3072 sc1 lds
v_mfma_f32_32x32x16_bf16 v[18:33], v[70:73], v[160:163], v[18:33]
ds_read_b128 v[70:73], v1 offset:16384
v_mul_f32 v109, v5, v5
v_fma_f32 v106, v106, v112, v111
v_fma_f32 v107, v107, v112, v111
v_mfma_f32_32x32x16_bf16 v[18:33], v[74:77], v[164:167], v[18:33]
ds_read_b128 v[74:77], v1 offset:17408
v_fma_f32 v108, v108, v112, v111
v_fma_f32 v109, v109, v112, v111
v_mul_f32 v106, v106, v2
s_add_u32 s65, s90, 0x7000
buffer_load_dwordx4 a[116:119], v1, s[44:47], s65 offen offset:0 sc1
v_mfma_f32_32x32x16_bf16 v[18:33], v[78:81], v[168:171], v[18:33]
ds_read_b128 v[78:81], v1 offset:18432
v_mul_f32 v107, v107, v3
v_mul_f32 v108, v108, v4
v_mul_f32 v109, v109, v5
s_waitcnt lgkmcnt(4)
v_mfma_f32_32x32x16_bf16 v[18:33], v[50:53], v[172:175], v[18:33]
ds_read_b128 v[50:53], v1 offset:19456
v_exp_f32 v106, v106
v_mul_f32 v102, v10, v2
v_exp_f32 v107, v107
buffer_load_dwordx4 a[120:123], v1, s[44:47], s81 offen offset:2048 sc1
v_mfma_f32_32x32x16_bf16 v[18:33], v[54:57], v[176:179], v[18:33]
ds_read_b128 v[54:57], v1 offset:20480
v_mul_f32 v103, v11, v3
v_exp_f32 v108, v108
v_mul_f32 v104, v12, v4
v_mfma_f32_32x32x16_bf16 v[18:33], v[58:61], v[180:183], v[18:33]
ds_read_b128 v[58:61], v1 offset:21504
v_exp_f32 v109, v109
v_mul_f32 v105, v13, v5
v_fma_f32 v106, v106, v113, v113
buffer_load_dwordx4 a[124:127], v1, s[44:47], s65 offen offset:2048 sc1
v_mfma_f32_32x32x16_bf16 v[18:33], v[62:65], v[184:187], v[18:33]
ds_read_b128 v[62:65], v1 offset:22528
v_fma_f32 v107, v107, v113, v113
v_fma_f32 v108, v108, v113, v113
v_fma_f32 v109, v109, v113, v113
s_waitcnt lgkmcnt(4)
v_mfma_f32_32x32x16_bf16 v[18:33], v[66:69], v[188:191], v[18:33]
ds_read_b128 v[66:69], v1 offset:23552
v_rcp_f32 v106, v106
v_rcp_f32 v107, v107
v_rcp_f32 v108, v108
s_add_u32 s81, s89, 0x8000
buffer_load_dwordx4 a[128:131], v1, s[44:47], s81 offen offset:0 sc1
v_mfma_f32_32x32x16_bf16 v[18:33], v[70:73], v[192:195], v[18:33]
ds_read_b128 v[70:73], v1 offset:24576
v_rcp_f32 v109, v109
v_mul_f32 v102, v102, v106
v_mul_f32 v103, v103, v107
v_mfma_f32_32x32x16_bf16 v[18:33], v[74:77], v[196:199], v[18:33]
ds_read_b128 v[74:77], v1 offset:25600
v_mul_f32 v104, v104, v108
v_mul_f32 v105, v105, v109
v_cvt_pk_bf16_f32 v116, v102, v103
s_add_u32 s65, s90, 0x8000
buffer_load_dwordx4 a[132:135], v1, s[44:47], s65 offen offset:0 sc1
v_mfma_f32_32x32x16_bf16 v[18:33], v[78:81], v[200:203], v[18:33]
ds_read_b128 v[78:81], v1 offset:26624
v_cvt_pk_bf16_f32 v117, v104, v105
v_mul_f32 v106, v6, v6
v_mul_f32 v107, v7, v7
s_waitcnt lgkmcnt(4)
v_mfma_f32_32x32x16_bf16 v[18:33], v[50:53], v[204:207], v[18:33]
ds_read_b128 v[50:53], v1 offset:27648
v_mul_f32 v108, v8, v8
v_mul_f32 v109, v9, v9
v_fma_f32 v106, v106, v112, v111
buffer_load_dwordx4 a[136:139], v1, s[44:47], s81 offen offset:2048 sc1
v_mfma_f32_32x32x16_bf16 v[18:33], v[54:57], v[208:211], v[18:33]
ds_read_b128 v[54:57], v1 offset:28672
v_fma_f32 v107, v107, v112, v111
v_fma_f32 v108, v108, v112, v111
v_fma_f32 v109, v109, v112, v111
v_mfma_f32_32x32x16_bf16 v[18:33], v[58:61], v[212:215], v[18:33]
ds_read_b128 v[58:61], v1 offset:29696
v_mul_f32 v106, v106, v6
v_mul_f32 v107, v107, v7
v_mul_f32 v108, v108, v8
buffer_load_dwordx4 a[140:143], v1, s[44:47], s65 offen offset:2048 sc1
v_mfma_f32_32x32x16_bf16 v[18:33], v[62:65], v[216:219], v[18:33]
ds_read_b128 v[62:65], v1 offset:30720
v_mul_f32 v109, v109, v9
v_exp_f32 v106, v106
v_mul_f32 v102, v14, v6
s_waitcnt lgkmcnt(4)
v_mfma_f32_32x32x16_bf16 v[18:33], v[66:69], v[220:223], v[18:33]
ds_read_b128 v[66:69], v1 offset:31744
v_exp_f32 v107, v107
v_mul_f32 v103, v15, v7
v_exp_f32 v108, v108
s_add_u32 s81, s89, 0x9000
buffer_load_dwordx4 a[144:147], v1, s[44:47], s81 offen offset:0 sc1
s_waitcnt vmcnt(20)
s_barrier
v_mfma_f32_32x32x16_bf16 v[18:33], v[70:73], v[224:227], v[18:33]
ds_read_b128 v[70:73], v115 offset:2560
v_mul_f32 v104, v16, v8
v_exp_f32 v109, v109
v_mul_f32 v105, v17, v9
v_mfma_f32_32x32x16_bf16 v[18:33], v[74:77], v[228:231], v[18:33]
ds_read_b128 v[74:77], v1 offset:32768
v_fma_f32 v106, v106, v113, v113
v_fma_f32 v107, v107, v113, v113
v_fma_f32 v108, v108, v113, v113
s_add_u32 s65, s90, 0x9000
buffer_load_dwordx4 a[148:151], v1, s[44:47], s65 offen offset:0 sc1
v_mfma_f32_32x32x16_bf16 v[18:33], v[78:81], v[232:235], v[18:33]
ds_read_b128 v[78:81], v1 offset:33792
v_fma_f32 v109, v109, v113, v113
v_rcp_f32 v106, v106
v_rcp_f32 v107, v107
s_waitcnt lgkmcnt(4)
v_mfma_f32_32x32x16_bf16 v[18:33], v[50:53], v[236:239], v[18:33]
ds_read_b128 v[50:53], v1 offset:34816
v_rcp_f32 v108, v108
v_rcp_f32 v109, v109
v_mul_f32 v102, v102, v106
buffer_load_dwordx4 a[152:155], v1, s[44:47], s81 offen offset:2048 sc1
v_mfma_f32_32x32x16_bf16 v[18:33], v[54:57], v[240:243], v[18:33]
ds_read_b128 v[54:57], v1 offset:35840
v_mul_f32 v103, v103, v107
v_mul_f32 v104, v104, v108
v_mul_f32 v105, v105, v109
v_mfma_f32_32x32x16_bf16 v[18:33], v[58:61], v[244:247], v[18:33]
ds_read_b128 v[58:61], v1 offset:36864
v_cvt_pk_bf16_f32 v118, v102, v103
v_cvt_pk_bf16_f32 v119, v104, v105
buffer_store_dwordx4 v[116:119], v1, s[44:47], s77 offen sc1
buffer_load_dwordx4 a[156:159], v1, s[44:47], s65 offen offset:2048 sc1
v_mfma_f32_32x32x16_bf16 v[18:33], v[62:65], v[248:251], v[18:33]
ds_read_b128 v[62:65], v1 offset:37888
s_add_u32 s77, s77, 1024
s_waitcnt lgkmcnt(4)
v_mfma_f32_32x32x16_bf16 v[18:33], v[66:69], v[252:255], v[18:33]
ds_read_b128 v[66:69], v1 offset:38912
s_add_u32 s81, s89, 0xa000
buffer_load_dwordx4 a[160:163], v1, s[44:47], s81 offen offset:0 sc1
v_mfma_f32_32x32x16_bf16 v[34:49], v[70:73], v[94:97], 0
ds_read_b128 v[70:73], v1 offset:39936
v_mfma_f32_32x32x16_bf16 v[34:49], v[74:77], v[128:131], v[34:49]
ds_read_b128 v[74:77], v1 offset:40960
s_nop 4
v_mul_f32 v106, v18, v18
v_mul_f32 v107, v19, v19
v_mul_f32 v108, v20, v20
v_mfma_f32_32x32x16_bf16 v[34:49], v[78:81], v[132:135], v[34:49]
ds_read_b128 v[78:81], v1 offset:41984
v_mul_f32 v109, v21, v21
v_fma_f32 v106, v106, v112, v111
v_fma_f32 v107, v107, v112, v111
s_add_u32 s65, s90, 0xa000
buffer_load_dwordx4 a[164:167], v1, s[44:47], s65 offen offset:0 sc1
s_waitcnt lgkmcnt(4)
v_mfma_f32_32x32x16_bf16 v[34:49], v[50:53], v[136:139], v[34:49]
ds_read_b128 v[50:53], v1 offset:43008
v_fma_f32 v108, v108, v112, v111
v_fma_f32 v109, v109, v112, v111
v_mul_f32 v106, v106, v18
v_mfma_f32_32x32x16_bf16 v[34:49], v[54:57], v[140:143], v[34:49]
ds_read_b128 v[54:57], v1 offset:44032
v_mul_f32 v107, v107, v19
v_mul_f32 v108, v108, v20
v_mul_f32 v109, v109, v21
buffer_load_dwordx4 a[168:171], v1, s[44:47], s81 offen offset:2048 sc1
v_mfma_f32_32x32x16_bf16 v[34:49], v[58:61], v[144:147], v[34:49]
ds_read_b128 v[58:61], v1 offset:45056
v_exp_f32 v106, v106
v_mul_f32 v102, v26, v18
v_exp_f32 v107, v107
v_mfma_f32_32x32x16_bf16 v[34:49], v[62:65], v[148:151], v[34:49]
ds_read_b128 v[62:65], v1 offset:46080
v_mul_f32 v103, v27, v19
v_exp_f32 v108, v108
v_mul_f32 v104, v28, v20
buffer_load_dwordx4 a[172:175], v1, s[44:47], s65 offen offset:2048 sc1
s_waitcnt lgkmcnt(4)
v_mfma_f32_32x32x16_bf16 v[34:49], v[66:69], v[152:155], v[34:49]
ds_read_b128 v[66:69], v1 offset:47104
v_exp_f32 v109, v109
v_mul_f32 v105, v29, v21
v_fma_f32 v106, v106, v113, v113
v_mfma_f32_32x32x16_bf16 v[34:49], v[70:73], v[156:159], v[34:49]
ds_read_b128 v[70:73], v1 offset:48128
v_fma_f32 v107, v107, v113, v113
v_fma_f32 v108, v108, v113, v113
v_fma_f32 v109, v109, v113, v113
s_add_u32 s81, s89, 0xb000
buffer_load_dwordx4 a[176:179], v1, s[44:47], s81 offen offset:0 sc1
v_mfma_f32_32x32x16_bf16 v[34:49], v[74:77], v[160:163], v[34:49]
ds_read_b128 v[74:77], v1 offset:49152
v_rcp_f32 v106, v106
v_rcp_f32 v107, v107
v_rcp_f32 v108, v108
v_mfma_f32_32x32x16_bf16 v[34:49], v[78:81], v[164:167], v[34:49]
ds_read_b128 v[78:81], v1 offset:50176
v_rcp_f32 v109, v109
v_mul_f32 v102, v102, v106
v_mul_f32 v103, v103, v107
s_add_u32 s65, s90, 0xb000
buffer_load_dwordx4 a[180:183], v1, s[44:47], s65 offen offset:0 sc1
s_waitcnt lgkmcnt(4)
v_mfma_f32_32x32x16_bf16 v[34:49], v[50:53], v[168:171], v[34:49]
ds_read_b128 v[50:53], v1 offset:51200
v_mul_f32 v104, v104, v108
v_mul_f32 v105, v105, v109
v_cvt_pk_bf16_f32 v116, v102, v103
v_mfma_f32_32x32x16_bf16 v[34:49], v[54:57], v[172:175], v[34:49]
ds_read_b128 v[54:57], v1 offset:52224
v_cvt_pk_bf16_f32 v117, v104, v105
v_mul_f32 v106, v22, v22
v_mul_f32 v107, v23, v23
buffer_load_dwordx4 a[184:187], v1, s[44:47], s81 offen offset:2048 sc1
v_mfma_f32_32x32x16_bf16 v[34:49], v[58:61], v[176:179], v[34:49]
ds_read_b128 v[58:61], v1 offset:53248
v_mul_f32 v108, v24, v24
v_mul_f32 v109, v25, v25
v_fma_f32 v106, v106, v112, v111
v_mfma_f32_32x32x16_bf16 v[34:49], v[62:65], v[180:183], v[34:49]
ds_read_b128 v[62:65], v1 offset:54272
v_fma_f32 v107, v107, v112, v111
v_fma_f32 v108, v108, v112, v111
v_fma_f32 v109, v109, v112, v111
buffer_load_dwordx4 a[188:191], v1, s[44:47], s65 offen offset:2048 sc1
s_waitcnt lgkmcnt(4)
v_mfma_f32_32x32x16_bf16 v[34:49], v[66:69], v[184:187], v[34:49]
ds_read_b128 v[66:69], v1 offset:55296
v_mul_f32 v106, v106, v22
v_mul_f32 v107, v107, v23
v_mul_f32 v108, v108, v24
v_mfma_f32_32x32x16_bf16 v[34:49], v[70:73], v[188:191], v[34:49]
ds_read_b128 v[70:73], v1 offset:56320
v_mul_f32 v109, v109, v25
v_exp_f32 v106, v106
v_mul_f32 v102, v30, v22
buffer_load_dwordx4 a[224:227], v1, s[44:47], s60 offen offset:0 sc1
v_mfma_f32_32x32x16_bf16 v[34:49], v[74:77], v[192:195], v[34:49]
ds_read_b128 v[74:77], v1 offset:57344
v_exp_f32 v107, v107
v_mul_f32 v103, v31, v23
v_exp_f32 v108, v108
v_mfma_f32_32x32x16_bf16 v[34:49], v[78:81], v[196:199], v[34:49]
ds_read_b128 v[78:81], v1 offset:58368
v_mul_f32 v104, v32, v24
v_exp_f32 v109, v109
v_mul_f32 v105, v33, v25
buffer_load_dwordx4 a[228:231], v1, s[44:47], s61 offen offset:0 sc1
s_waitcnt lgkmcnt(4)
v_mfma_f32_32x32x16_bf16 v[34:49], v[50:53], v[200:203], v[34:49]
ds_read_b128 v[50:53], v1 offset:59392
v_fma_f32 v106, v106, v113, v113
v_fma_f32 v107, v107, v113, v113
v_fma_f32 v108, v108, v113, v113
v_mfma_f32_32x32x16_bf16 v[34:49], v[54:57], v[204:207], v[34:49]
ds_read_b128 v[54:57], v1 offset:60416
v_fma_f32 v109, v109, v113, v113
v_rcp_f32 v106, v106
v_rcp_f32 v107, v107
buffer_load_dwordx4 a[232:235], v1, s[44:47], s84 offen offset:3072 sc1
v_mfma_f32_32x32x16_bf16 v[34:49], v[58:61], v[208:211], v[34:49]
ds_read_b128 v[58:61], v1 offset:61440
v_rcp_f32 v108, v108
v_rcp_f32 v109, v109
v_mul_f32 v102, v102, v106
v_mfma_f32_32x32x16_bf16 v[34:49], v[62:65], v[212:215], v[34:49]
ds_read_b128 v[62:65], v1 offset:62464
v_mul_f32 v103, v103, v107
v_mul_f32 v104, v104, v108
v_mul_f32 v105, v105, v109
buffer_load_dwordx4 a[236:239], v1, s[44:47], s85 offen offset:3072 sc1
s_waitcnt lgkmcnt(4)
v_mfma_f32_32x32x16_bf16 v[34:49], v[66:69], v[216:219], v[34:49]
ds_read_b128 v[66:69], v1 offset:63488
v_cvt_pk_bf16_f32 v118, v102, v103
v_cvt_pk_bf16_f32 v119, v104, v105
buffer_store_dwordx4 v[116:119], v1, s[44:47], s77 offen sc1
v_mfma_f32_32x32x16_bf16 v[34:49], v[70:73], v[220:223], v[34:49]
ds_read_b128 v[70:73], v1 offset:64512
s_add_u32 s77, s77, 1024
buffer_load_dwordx4 a[240:243], v1, s[44:47], s92 offen offset:2048 sc1
s_waitcnt vmcnt(26)
s_barrier
v_mfma_f32_32x32x16_bf16 v[34:49], v[74:77], v[224:227], v[34:49]
ds_read_b128 v[74:77], v115 offset:3072
v_mfma_f32_32x32x16_bf16 v[34:49], v[78:81], v[228:231], v[34:49]
ds_read_b128 v[78:81], v114 offset:0
buffer_load_dwordx4 a[244:247], v1, s[44:47], s93 offen offset:2048 sc1
s_waitcnt lgkmcnt(4)
v_mfma_f32_32x32x16_bf16 v[34:49], v[50:53], v[232:235], v[34:49]
ds_read_b128 v[50:53], v114 offset:1024
v_mfma_f32_32x32x16_bf16 v[34:49], v[54:57], v[236:239], v[34:49]
ds_read_b128 v[54:57], v114 offset:2048
buffer_load_dwordx4 a[248:251], v1, s[44:47], s96 offen offset:1024 sc1
v_mfma_f32_32x32x16_bf16 v[34:49], v[58:61], v[240:243], v[34:49]
ds_read_b128 v[58:61], v114 offset:3072
v_mfma_f32_32x32x16_bf16 v[34:49], v[62:65], v[244:247], v[34:49]
ds_read_b128 v[62:65], v114 offset:4096
buffer_load_dwordx4 a[252:255], v1, s[44:47], s97 offen offset:1024 sc1
s_waitcnt lgkmcnt(4)
v_mfma_f32_32x32x16_bf16 v[34:49], v[66:69], v[248:251], v[34:49]
ds_read_b128 v[66:69], v114 offset:5120
v_mfma_f32_32x32x16_bf16 v[34:49], v[70:73], v[252:255], v[34:49]
ds_read_b128 v[70:73], v114 offset:6144
v_mfma_f32_32x32x16_bf16 v[2:17], v[74:77], v[94:97], 0
ds_read_b128 v[74:77], v114 offset:7168
v_mfma_f32_32x32x16_bf16 v[2:17], v[78:81], v[128:131], v[2:17]
ds_read_b128 v[78:81], v114 offset:8192
s_nop 6
v_mul_f32 v106, v34, v34
v_mul_f32 v107, v35, v35
v_mul_f32 v108, v36, v36
s_waitcnt lgkmcnt(4)
v_mfma_f32_32x32x16_bf16 v[2:17], v[50:53], v[132:135], v[2:17]
ds_read_b128 v[50:53], v114 offset:9216
v_mul_f32 v109, v37, v37
v_fma_f32 v106, v106, v112, v111
v_fma_f32 v107, v107, v112, v111
v_mfma_f32_32x32x16_bf16 v[2:17], v[54:57], v[136:139], v[2:17]
ds_read_b128 v[54:57], v114 offset:10240
v_fma_f32 v108, v108, v112, v111
v_fma_f32 v109, v109, v112, v111
v_mul_f32 v106, v106, v34
v_mfma_f32_32x32x16_bf16 v[2:17], v[58:61], v[140:143], v[2:17]
ds_read_b128 v[58:61], v114 offset:11264
v_mul_f32 v107, v107, v35
v_mul_f32 v108, v108, v36
v_mul_f32 v109, v109, v37
v_mfma_f32_32x32x16_bf16 v[2:17], v[62:65], v[144:147], v[2:17]
ds_read_b128 v[62:65], v114 offset:12288
v_exp_f32 v106, v106
v_mul_f32 v102, v42, v34
v_exp_f32 v107, v107
s_waitcnt lgkmcnt(4)
v_mfma_f32_32x32x16_bf16 v[2:17], v[66:69], v[148:151], v[2:17]
ds_read_b128 v[66:69], v114 offset:13312
v_mul_f32 v103, v43, v35
v_exp_f32 v108, v108
v_mul_f32 v104, v44, v36
v_mfma_f32_32x32x16_bf16 v[2:17], v[70:73], v[152:155], v[2:17]
ds_read_b128 v[70:73], v114 offset:14336
v_exp_f32 v109, v109
v_mul_f32 v105, v45, v37
v_fma_f32 v106, v106, v113, v113
v_mfma_f32_32x32x16_bf16 v[2:17], v[74:77], v[156:159], v[2:17]
ds_read_b128 v[74:77], v114 offset:15360
v_fma_f32 v107, v107, v113, v113
v_fma_f32 v108, v108, v113, v113
v_fma_f32 v109, v109, v113, v113
v_mfma_f32_32x32x16_bf16 v[2:17], v[78:81], v[160:163], v[2:17]
ds_read_b128 v[78:81], v114 offset:16384
v_rcp_f32 v106, v106
v_rcp_f32 v107, v107
v_rcp_f32 v108, v108
buffer_load_dwordx4 v[128:131], v1, s[44:47], s60 offen offset:1024 sc1
s_waitcnt lgkmcnt(4)
v_mfma_f32_32x32x16_bf16 v[2:17], v[50:53], v[164:167], v[2:17]
ds_read_b128 v[50:53], v114 offset:17408
v_rcp_f32 v109, v109
v_mul_f32 v102, v102, v106
v_mul_f32 v103, v103, v107
v_mfma_f32_32x32x16_bf16 v[2:17], v[54:57], v[168:171], v[2:17]
ds_read_b128 v[54:57], v114 offset:18432
v_mul_f32 v104, v104, v108
v_mul_f32 v105, v105, v109
v_cvt_pk_bf16_f32 v116, v102, v103
buffer_load_dwordx4 v[132:135], v1, s[44:47], s61 offen offset:1024 sc1
v_mfma_f32_32x32x16_bf16 v[2:17], v[58:61], v[172:175], v[2:17]
ds_read_b128 v[58:61], v114 offset:19456
v_cvt_pk_bf16_f32 v117, v104, v105
v_mul_f32 v106, v38, v38
v_mul_f32 v107, v39, v39
v_mfma_f32_32x32x16_bf16 v[2:17], v[62:65], v[176:179], v[2:17]
ds_read_b128 v[62:65], v114 offset:20480
v_mul_f32 v108, v40, v40
v_mul_f32 v109, v41, v41
v_fma_f32 v106, v106, v112, v111
buffer_load_dwordx4 v[136:139], v1, s[44:47], s86 offen offset:0 sc1
s_waitcnt lgkmcnt(4)
v_mfma_f32_32x32x16_bf16 v[2:17], v[66:69], v[180:183], v[2:17]
ds_read_b128 v[66:69], v114 offset:21504
v_fma_f32 v107, v107, v112, v111
v_fma_f32 v108, v108, v112, v111
v_fma_f32 v109, v109, v112, v111
v_mfma_f32_32x32x16_bf16 v[2:17], v[70:73], v[184:187], v[2:17]
ds_read_b128 v[70:73], v114 offset:22528
v_mul_f32 v106, v106, v38
v_mul_f32 v107, v107, v39
v_mul_f32 v108, v108, v40
buffer_load_dwordx4 v[140:143], v1, s[44:47], s91 offen offset:0 sc1
v_mfma_f32_32x32x16_bf16 v[2:17], v[74:77], v[188:191], v[2:17]
ds_read_b128 v[74:77], v114 offset:23552
v_mul_f32 v109, v109, v41
v_exp_f32 v106, v106
v_mul_f32 v102, v46, v38
v_mfma_f32_32x32x16_bf16 v[2:17], v[78:81], v[192:195], v[2:17]
ds_read_b128 v[78:81], v114 offset:24576
v_exp_f32 v107, v107
v_mul_f32 v103, v47, v39
v_exp_f32 v108, v108
buffer_load_dwordx4 v[144:147], v1, s[44:47], s92 offen offset:3072 sc1
s_waitcnt lgkmcnt(4)
v_mfma_f32_32x32x16_bf16 v[2:17], v[50:53], v[196:199], v[2:17]
ds_read_b128 v[50:53], v114 offset:25600
v_mul_f32 v104, v48, v40
v_exp_f32 v109, v109
v_mul_f32 v105, v49, v41
buffer_load_dwordx4 v[160:163], v1, s[44:47], s60 offen offset:2048 sc1
v_mfma_f32_32x32x16_bf16 v[2:17], v[54:57], v[200:203], v[2:17]
ds_read_b128 v[54:57], v114 offset:26624
v_fma_f32 v106, v106, v113, v113
v_fma_f32 v107, v107, v113, v113
v_fma_f32 v108, v108, v113, v113
buffer_load_dwordx4 v[148:151], v1, s[44:47], s93 offen offset:3072 sc1
v_mfma_f32_32x32x16_bf16 v[2:17], v[58:61], v[204:207], v[2:17]
ds_read_b128 v[58:61], v114 offset:27648
v_fma_f32 v109, v109, v113, v113
v_rcp_f32 v106, v106
v_rcp_f32 v107, v107
buffer_load_dwordx4 v[164:167], v1, s[44:47], s61 offen offset:2048 sc1
v_mfma_f32_32x32x16_bf16 v[2:17], v[62:65], v[208:211], v[2:17]
ds_read_b128 v[62:65], v114 offset:28672
v_rcp_f32 v108, v108
v_rcp_f32 v109, v109
v_mul_f32 v102, v102, v106
buffer_load_dwordx4 v[152:155], v1, s[44:47], s96 offen offset:2048 sc1
s_waitcnt lgkmcnt(4)
v_mfma_f32_32x32x16_bf16 v[2:17], v[66:69], v[212:215], v[2:17]
ds_read_b128 v[66:69], v114 offset:29696
v_mul_f32 v103, v103, v107
v_mul_f32 v104, v104, v108
v_mul_f32 v105, v105, v109
buffer_load_dwordx4 v[168:171], v1, s[44:47], s86 offen offset:1024 sc1
v_mfma_f32_32x32x16_bf16 v[2:17], v[70:73], v[216:219], v[2:17]
ds_read_b128 v[70:73], v114 offset:30720
v_cvt_pk_bf16_f32 v118, v102, v103
v_cvt_pk_bf16_f32 v119, v104, v105
buffer_store_dwordx4 v[116:119], v1, s[44:47], s77 offen sc1
buffer_load_dwordx4 v[156:159], v1, s[44:47], s97 offen offset:2048 sc1
v_mfma_f32_32x32x16_bf16 v[2:17], v[74:77], v[220:223], v[2:17]
ds_read_b128 v[74:77], v114 offset:31744
s_add_u32 s77, s77, 1024
buffer_load_dwordx4 v[172:175], v1, s[44:47], s91 offen offset:1024 sc1
v_mfma_f32_32x32x16_bf16 v[2:17], v[78:81], v[224:227], v[2:17]
s_waitcnt lgkmcnt(3)
v_mfma_f32_32x32x16_bf16 v[2:17], v[50:53], v[228:231], v[2:17]
buffer_load_dwordx4 v[176:179], v1, s[44:47], s94 offen offset:0 sc1
v_mfma_f32_32x32x16_bf16 v[2:17], v[54:57], v[232:235], v[2:17]
v_mfma_f32_32x32x16_bf16 v[2:17], v[58:61], v[236:239], v[2:17]
buffer_load_dwordx4 v[180:183], v1, s[44:47], s95 offen offset:0 sc1
v_mfma_f32_32x32x16_bf16 v[2:17], v[62:65], v[240:243], v[2:17]
s_waitcnt lgkmcnt(2)
v_mfma_f32_32x32x16_bf16 v[2:17], v[66:69], v[244:247], v[2:17]
buffer_load_dwordx4 v[184:187], v1, s[44:47], s96 offen offset:3072 sc1
s_waitcnt lgkmcnt(1)
v_mfma_f32_32x32x16_bf16 v[2:17], v[70:73], v[248:251], v[2:17]
s_waitcnt lgkmcnt(0)
v_mfma_f32_32x32x16_bf16 v[2:17], v[74:77], v[252:255], v[2:17]
buffer_load_dwordx4 v[188:191], v1, s[44:47], s97 offen offset:3072 sc1
s_waitcnt vmcnt(25)
v_mfma_f32_32x32x16_bf16 v[224:239], a[0:3], a[224:227], 0
s_nop 7
s_nop 0
v_mul_f32 v106, v2, v2
v_mul_f32 v107, v3, v3
v_mul_f32 v108, v4, v4
v_mul_f32 v109, v5, v5
v_fma_f32 v106, v106, v112, v111
v_fma_f32 v107, v107, v112, v111
buffer_load_dwordx4 v[192:195], v1, s[44:47], s60 offen offset:3072 sc1
v_mfma_f32_32x32x16_bf16 v[50:65], a[4:7], a[224:227], 0
v_fma_f32 v108, v108, v112, v111
v_fma_f32 v109, v109, v112, v111
v_mul_f32 v106, v106, v2
v_mul_f32 v107, v107, v3
v_mul_f32 v108, v108, v4
v_mul_f32 v109, v109, v5
s_waitcnt vmcnt(25)
v_mfma_f32_32x32x16_bf16 v[240:255], a[0:3], a[228:231], 0
v_exp_f32 v106, v106
v_mul_f32 v102, v10, v2
v_exp_f32 v107, v107
v_mul_f32 v103, v11, v3
v_exp_f32 v108, v108
v_mul_f32 v104, v12, v4
buffer_load_dwordx4 v[196:199], v1, s[44:47], s61 offen offset:3072 sc1
v_mfma_f32_32x32x16_bf16 v[66:81], a[4:7], a[228:231], 0
v_exp_f32 v109, v109
v_mul_f32 v105, v13, v5
v_fma_f32 v106, v106, v113, v113
v_fma_f32 v107, v107, v113, v113
v_fma_f32 v108, v108, v113, v113
v_fma_f32 v109, v109, v113, v113
s_waitcnt vmcnt(25)
v_mfma_f32_32x32x16_bf16 v[224:239], a[56:59], a[232:235], v[224:239]
v_rcp_f32 v106, v106
v_rcp_f32 v107, v107
v_rcp_f32 v108, v108
v_rcp_f32 v109, v109
v_mul_f32 v102, v102, v106
v_mul_f32 v103, v103, v107
buffer_load_dwordx4 v[200:203], v1, s[44:47], s86 offen offset:2048 sc1
v_mfma_f32_32x32x16_bf16 v[50:65], a[60:63], a[232:235], v[50:65]
v_mul_f32 v104, v104, v108
v_mul_f32 v105, v105, v109
v_cvt_pk_bf16_f32 v116, v102, v103
v_cvt_pk_bf16_f32 v117, v104, v105
v_mul_f32 v106, v6, v6
v_mul_f32 v107, v7, v7
s_waitcnt vmcnt(25)
v_mfma_f32_32x32x16_bf16 v[240:255], a[56:59], a[236:239], v[240:255]
v_mul_f32 v108, v8, v8
v_mul_f32 v109, v9, v9
v_fma_f32 v106, v106, v112, v111
v_fma_f32 v107, v107, v112, v111
v_fma_f32 v108, v108, v112, v111
v_fma_f32 v109, v109, v112, v111
buffer_load_dwordx4 v[204:207], v1, s[44:47], s91 offen offset:2048 sc1
v_mfma_f32_32x32x16_bf16 v[66:81], a[60:63], a[236:239], v[66:81]
v_mul_f32 v106, v106, v6
v_mul_f32 v107, v107, v7
v_mul_f32 v108, v108, v8
v_mul_f32 v109, v109, v9
v_exp_f32 v106, v106
v_mul_f32 v102, v14, v6
s_waitcnt vmcnt(24)
v_mfma_f32_32x32x16_bf16 v[224:239], a[112:115], a[240:243], v[224:239]
v_exp_f32 v107, v107
v_mul_f32 v103, v15, v7
v_exp_f32 v108, v108
v_mul_f32 v104, v16, v8
v_exp_f32 v109, v109
v_mul_f32 v105, v17, v9
buffer_load_dwordx4 v[208:211], v1, s[44:47], s94 offen offset:1024 sc1
v_mfma_f32_32x32x16_bf16 v[50:65], a[116:119], a[240:243], v[50:65]
v_fma_f32 v106, v106, v113, v113
v_fma_f32 v107, v107, v113, v113
v_fma_f32 v108, v108, v113, v113
v_fma_f32 v109, v109, v113, v113
v_rcp_f32 v106, v106
v_rcp_f32 v107, v107
s_waitcnt vmcnt(24)
v_mfma_f32_32x32x16_bf16 v[240:255], a[112:115], a[244:247], v[240:255]
v_rcp_f32 v108, v108
v_rcp_f32 v109, v109
v_mul_f32 v102, v102, v106
v_mul_f32 v103, v103, v107
v_mul_f32 v104, v104, v108
v_mul_f32 v105, v105, v109
buffer_load_dwordx4 v[212:215], v1, s[44:47], s95 offen offset:1024 sc1
v_mfma_f32_32x32x16_bf16 v[66:81], a[116:119], a[244:247], v[66:81]
v_cvt_pk_bf16_f32 v118, v102, v103
v_cvt_pk_bf16_f32 v119, v104, v105
buffer_store_dwordx4 v[116:119], v1, s[44:47], s77 offen sc1
s_add_u32 s77, s77, 1024
s_waitcnt vmcnt(25)
v_mfma_f32_32x32x16_bf16 v[224:239], a[168:171], a[248:251], v[224:239]
s_waitcnt vmcnt(23)
v_max3_u32 v120, v128, v129, v130
s_waitcnt vmcnt(22)
v_max3_u32 v120, v120, v131, v132
v_max3_u32 v120, v120, v133, v134
s_waitcnt vmcnt(21)
v_max3_u32 v120, v120, v135, v136
v_max3_u32 v120, v120, v137, v138
s_waitcnt vmcnt(20)
v_max3_u32 v120, v120, v139, v140
buffer_load_dwordx4 v[216:219], v1, s[44:47], s98 offen offset:0 sc1
v_mfma_f32_32x32x16_bf16 v[50:65], a[172:175], a[248:251], v[50:65]
v_max3_u32 v120, v120, v141, v142
s_waitcnt vmcnt(20)
v_max3_u32 v120, v120, v143, v144
v_max3_u32 v120, v120, v145, v146
s_waitcnt vmcnt(18)
v_max3_u32 v120, v120, v147, v148
v_max3_u32 v120, v120, v149, v150
s_waitcnt vmcnt(16)
v_max3_u32 v120, v120, v151, v152
v_mfma_f32_32x32x16_bf16 v[240:255], a[168:171], a[252:255], v[240:255]
v_max3_u32 v120, v120, v153, v154
s_waitcnt vmcnt(13)
v_max3_u32 v120, v120, v155, v156
v_max3_u32 v120, v120, v157, v158
v_max_u32 v120, v120, v159
v_cmp_eq_u32 vcc, -1, v120
buffer_load_dwordx4 v[220:223], v1, s[44:47], s99 offen offset:0 sc1
v_mfma_f32_32x32x16_bf16 v[66:81], a[172:175], a[252:255], v[66:81]
s_nop 2
s_cbranch_vccnz aretry_40
aretryok_39:
v_mfma_f32_32x32x16_bf16 v[224:239], a[8:11], v[128:131], v[224:239]
buffer_load_dwordx4 v[82:85], v1, s[44:47], s84 offen offset:0 sc1
v_mfma_f32_32x32x16_bf16 v[50:65], a[12:15], v[128:131], v[50:65]
v_mfma_f32_32x32x16_bf16 v[240:255], a[8:11], v[132:135], v[240:255]
buffer_load_dwordx4 v[86:89], v1, s[44:47], s85 offen offset:0 sc1
v_mfma_f32_32x32x16_bf16 v[66:81], a[12:15], v[132:135], v[66:81]
v_mfma_f32_32x32x16_bf16 v[224:239], a[64:67], v[136:139], v[224:239]
buffer_load_dwordx4 v[90:93], v1, s[44:47], s86 offen offset:3072 sc1
v_mfma_f32_32x32x16_bf16 v[50:65], a[68:71], v[136:139], v[50:65]
v_mfma_f32_32x32x16_bf16 v[240:255], a[64:67], v[140:143], v[240:255]
buffer_load_dwordx4 v[94:97], v1, s[44:47], s91 offen offset:3072 sc1
v_mfma_f32_32x32x16_bf16 v[66:81], a[68:71], v[140:143], v[66:81]
v_mfma_f32_32x32x16_bf16 v[224:239], a[120:123], v[144:147], v[224:239]
buffer_load_dwordx4 v[98:101], v1, s[44:47], s94 offen offset:2048 sc1
v_max3_u32 v120, v160, v161, v162
v_max3_u32 v120, v120, v163, v164
v_max3_u32 v120, v120, v165, v166
v_mfma_f32_32x32x16_bf16 v[50:65], a[124:127], v[144:147], v[50:65]
v_max3_u32 v120, v120, v167, v168
v_max3_u32 v120, v120, v169, v170
s_waitcnt vmcnt(18)
v_max3_u32 v120, v120, v171, v172
v_mfma_f32_32x32x16_bf16 v[240:255], a[120:123], v[148:151], v[240:255]
buffer_load_dwordx4 v[102:105], v1, s[44:47], s95 offen offset:2048 sc1
v_max3_u32 v120, v120, v173, v174
s_waitcnt vmcnt(18)
v_max3_u32 v120, v120, v175, v176
v_max3_u32 v120, v120, v177, v178
v_mfma_f32_32x32x16_bf16 v[66:81], a[124:127], v[148:151], v[66:81]
s_waitcnt vmcnt(17)
v_max3_u32 v120, v120, v179, v180
v_max3_u32 v120, v120, v181, v182
s_waitcnt vmcnt(16)
v_max3_u32 v120, v120, v183, v184
v_mfma_f32_32x32x16_bf16 v[224:239], a[176:179], v[152:155], v[224:239]
buffer_load_dwordx4 v[106:109], v1, s[44:47], s98 offen offset:1024 sc1
v_max3_u32 v120, v120, v185, v186
s_waitcnt vmcnt(16)
v_max3_u32 v120, v120, v187, v188
v_max3_u32 v120, v120, v189, v190
v_mfma_f32_32x32x16_bf16 v[50:65], a[180:183], v[152:155], v[50:65]
v_max_u32 v120, v120, v191
v_cmp_eq_u32 vcc, -1, v120
v_mfma_f32_32x32x16_bf16 v[240:255], a[176:179], v[156:159], v[240:255]
buffer_load_dwordx4 v[110:113], v1, s[44:47], s99 offen offset:1024 sc1
v_mfma_f32_32x32x16_bf16 v[66:81], a[180:183], v[156:159], v[66:81]
s_nop 1
s_cbranch_vccnz aretry_42
aretryok_41:
v_mfma_f32_32x32x16_bf16 v[224:239], a[16:19], v[160:163], v[224:239]
buffer_load_dwordx4 v[128:131], v1, s[44:47], s84 offen offset:1024 sc1
v_mfma_f32_32x32x16_bf16 v[50:65], a[20:23], v[160:163], v[50:65]
s_and_b32 s54, s49, 1
s_lshl_b32 s55, s74, 1
s_add_u32 s54, s54, s55
s_lshr_b32 s55, s49, 1
s_lshl_b32 s56, s75, 1
s_add_u32 s56, s56, s55
s_lshl_b32 s57, s54, 15
s_lshl_b32 s58, s56, 11
s_add_u32 s57, s57, s58
s_add_u32 s57, s57, 0x400000
buffer_load_dwordx4 v[2:5], v1, s[44:47], s57 offen sc1
buffer_load_dwordx4 v[6:9], v1, s[44:47], s57 offen offset:1024 sc1
s_lshl_b32 s78, s54, 15
s_lshl_b32 s58, s56, 6
s_add_u32 s78, s78, s58
v_mfma_f32_32x32x16_bf16 v[240:255], a[16:19], v[164:167], v[240:255]
buffer_load_dwordx4 v[132:135], v1, s[44:47], s85 offen offset:1024 sc1
v_mfma_f32_32x32x16_bf16 v[66:81], a[20:23], v[164:167], v[66:81]
v_mfma_f32_32x32x16_bf16 v[224:239], a[72:75], v[168:171], v[224:239]
buffer_load_dwordx4 v[136:139], v1, s[44:47], s92 offen offset:0 sc1
v_mfma_f32_32x32x16_bf16 v[50:65], a[76:79], v[168:171], v[50:65]
v_mfma_f32_32x32x16_bf16 v[240:255], a[72:75], v[172:175], v[240:255]
buffer_load_dwordx4 v[140:143], v1, s[44:47], s93 offen offset:0 sc1
v_mfma_f32_32x32x16_bf16 v[66:81], a[76:79], v[172:175], v[66:81]
v_mfma_f32_32x32x16_bf16 v[224:239], a[128:131], v[176:179], v[224:239]
buffer_load_dwordx4 v[144:147], v1, s[44:47], s94 offen offset:3072 sc1
s_waitcnt vmcnt(23)
v_max3_u32 v120, v192, v193, v194
s_waitcnt vmcnt(22)
v_max3_u32 v120, v120, v195, v196
v_max3_u32 v120, v120, v197, v198
v_mfma_f32_32x32x16_bf16 v[50:65], a[132:135], v[176:179], v[50:65]
s_waitcnt vmcnt(21)
v_max3_u32 v120, v120, v199, v200
v_max3_u32 v120, v120, v201, v202
s_waitcnt vmcnt(20)
v_max3_u32 v120, v120, v203, v204
v_mfma_f32_32x32x16_bf16 v[240:255], a[128:131], v[180:183], v[240:255]
buffer_load_dwordx4 v[148:151], v1, s[44:47], s95 offen offset:3072 sc1
v_max3_u32 v120, v120, v205, v206
s_waitcnt vmcnt(20)
v_max3_u32 v120, v120, v207, v208
v_max3_u32 v120, v120, v209, v210
v_mfma_f32_32x32x16_bf16 v[66:81], a[132:135], v[180:183], v[66:81]
s_waitcnt vmcnt(19)
v_max3_u32 v120, v120, v211, v212
v_max3_u32 v120, v120, v213, v214
s_waitcnt vmcnt(17)
v_max3_u32 v120, v120, v215, v216
v_mfma_f32_32x32x16_bf16 v[224:239], a[184:187], v[184:187], v[224:239]
buffer_load_dwordx4 v[152:155], v1, s[44:47], s98 offen offset:2048 sc1
v_max3_u32 v120, v120, v217, v218
s_waitcnt vmcnt(17)
v_max3_u32 v120, v120, v219, v220
v_max3_u32 v120, v120, v221, v222
v_mfma_f32_32x32x16_bf16 v[50:65], a[188:191], v[184:187], v[50:65]
v_max_u32 v120, v120, v223
v_cmp_eq_u32 vcc, -1, v120
v_mfma_f32_32x32x16_bf16 v[240:255], a[184:187], v[188:191], v[240:255]
buffer_load_dwordx4 v[156:159], v1, s[44:47], s99 offen offset:2048 sc1
v_mfma_f32_32x32x16_bf16 v[66:81], a[188:191], v[188:191], v[66:81]
s_nop 1
s_cbranch_vccnz aretry_44
aretryok_43:
v_mfma_f32_32x32x16_bf16 v[224:239], a[24:27], v[192:195], v[224:239]
v_mfma_f32_32x32x16_bf16 v[50:65], a[28:31], v[192:195], v[50:65]
v_mfma_f32_32x32x16_bf16 v[240:255], a[24:27], v[196:199], v[240:255]
v_mfma_f32_32x32x16_bf16 v[66:81], a[28:31], v[196:199], v[66:81]
v_mfma_f32_32x32x16_bf16 v[224:239], a[80:83], v[200:203], v[224:239]
v_mfma_f32_32x32x16_bf16 v[50:65], a[84:87], v[200:203], v[50:65]
v_mfma_f32_32x32x16_bf16 v[240:255], a[80:83], v[204:207], v[240:255]
v_mfma_f32_32x32x16_bf16 v[66:81], a[84:87], v[204:207], v[66:81]
v_mfma_f32_32x32x16_bf16 v[224:239], a[136:139], v[208:211], v[224:239]
s_waitcnt vmcnt(17)
v_max3_u32 v120, v82, v83, v84
s_waitcnt vmcnt(16)
v_max3_u32 v120, v120, v85, v86
v_max3_u32 v120, v120, v87, v88
v_mfma_f32_32x32x16_bf16 v[50:65], a[140:143], v[208:211], v[50:65]
s_waitcnt vmcnt(15)
v_max3_u32 v120, v120, v89, v90
v_max3_u32 v120, v120, v91, v92
s_waitcnt vmcnt(14)
v_max3_u32 v120, v120, v93, v94
v_mfma_f32_32x32x16_bf16 v[240:255], a[136:139], v[212:215], v[240:255]
v_max3_u32 v120, v120, v95, v96
s_waitcnt vmcnt(13)
v_max3_u32 v120, v120, v97, v98
v_max3_u32 v120, v120, v99, v100
v_mfma_f32_32x32x16_bf16 v[66:81], a[140:143], v[212:215], v[66:81]
s_waitcnt vmcnt(12)
v_max3_u32 v120, v120, v101, v102
v_max3_u32 v120, v120, v103, v104
s_waitcnt vmcnt(11)
v_max3_u32 v120, v120, v105, v106
v_mfma_f32_32x32x16_bf16 v[224:239], a[192:195], v[216:219], v[224:239]
v_max3_u32 v120, v120, v107, v108
s_waitcnt vmcnt(10)
v_max3_u32 v120, v120, v109, v110
v_max3_u32 v120, v120, v111, v112
v_mfma_f32_32x32x16_bf16 v[50:65], a[196:199], v[216:219], v[50:65]
v_max_u32 v120, v120, v113
v_cmp_eq_u32 vcc, -1, v120
v_mfma_f32_32x32x16_bf16 v[240:255], a[192:195], v[220:223], v[240:255]
v_mfma_f32_32x32x16_bf16 v[66:81], a[196:199], v[220:223], v[66:81]
s_nop 2
s_cbranch_vccnz aretry_46
aretryok_45:
v_mfma_f32_32x32x16_bf16 v[224:239], a[32:35], v[82:85], v[224:239]
buffer_load_dwordx4 v[160:163], v1, s[44:47], s84 offen offset:2048 sc1
s_lshl_b32 s54, s49, 12
s_xor_b32 s55, s54, 4096
v_mfma_f32_32x32x16_bf16 v[50:65], a[36:39], v[82:85], v[50:65]
buffer_load_dwordx4 v[164:167], v1, s[44:47], s85 offen offset:2048 sc1
v_add_u32 v34, s55, v1
s_xor_b32 s55, s54, 8192
v_mfma_f32_32x32x16_bf16 v[240:255], a[32:35], v[86:89], v[240:255]
buffer_load_dwordx4 v[168:171], v1, s[44:47], s92 offen offset:1024 sc1
v_add_u32 v35, s55, v1
s_xor_b32 s55, s54, 12288
v_mfma_f32_32x32x16_bf16 v[66:81], a[36:39], v[86:89], v[66:81]
buffer_load_dwordx4 v[172:175], v1, s[44:47], s93 offen offset:1024 sc1
s_add_u32 s55, s55, 98304
v_mfma_f32_32x32x16_bf16 v[224:239], a[88:91], v[90:93], v[224:239]
buffer_load_dwordx4 v[176:179], v1, s[44:47], s96 offen offset:0 sc1
v_add_u32 v36, s55, v1
v_add_u32 v37, s54, v1
v_mfma_f32_32x32x16_bf16 v[50:65], a[92:95], v[90:93], v[50:65]
buffer_load_dwordx4 v[180:183], v1, s[44:47], s97 offen offset:0 sc1
v_add_u32 v38, 98304, v37
v_and_b32 v39, 31, v0
v_mfma_f32_32x32x16_bf16 v[240:255], a[88:91], v[94:97], v[240:255]
buffer_load_dwordx4 v[184:187], v1, s[44:47], s98 offen offset:3072 sc1
v_lshrrev_b32 v40, 5, v0
v_lshlrev_b32 v40, 4, v40
v_mfma_f32_32x32x16_bf16 v[66:81], a[92:95], v[94:97], v[66:81]
buffer_load_dwordx4 v[188:191], v1, s[44:47], s99 offen offset:3072 sc1
v_lshl_add_u32 v39, v39, 10, v40
v_mfma_f32_32x32x16_bf16 v[224:239], a[144:147], v[98:101], v[224:239]
s_waitcnt vmcnt(17)
v_max3_u32 v120, v128, v129, v130
s_waitcnt vmcnt(14)
v_max3_u32 v120, v120, v131, v132
v_max3_u32 v120, v120, v133, v134
v_mfma_f32_32x32x16_bf16 v[50:65], a[148:151], v[98:101], v[50:65]
s_waitcnt vmcnt(13)
v_max3_u32 v120, v120, v135, v136
v_max3_u32 v120, v120, v137, v138
s_waitcnt vmcnt(12)
v_max3_u32 v120, v120, v139, v140
v_mfma_f32_32x32x16_bf16 v[240:255], a[144:147], v[102:105], v[240:255]
v_max3_u32 v120, v120, v141, v142
s_waitcnt vmcnt(11)
v_max3_u32 v120, v120, v143, v144
v_max3_u32 v120, v120, v145, v146
v_mfma_f32_32x32x16_bf16 v[66:81], a[148:151], v[102:105], v[66:81]
s_waitcnt vmcnt(10)
v_max3_u32 v120, v120, v147, v148
v_max3_u32 v120, v120, v149, v150
s_waitcnt vmcnt(9)
v_max3_u32 v120, v120, v151, v152
v_mfma_f32_32x32x16_bf16 v[224:239], a[200:203], v[106:109], v[224:239]
v_max3_u32 v120, v120, v153, v154
s_waitcnt vmcnt(8)
v_max3_u32 v120, v120, v155, v156
v_max3_u32 v120, v120, v157, v158
v_mfma_f32_32x32x16_bf16 v[50:65], a[204:207], v[106:109], v[50:65]
v_max_u32 v120, v120, v159
v_cmp_eq_u32 vcc, -1, v120
v_mfma_f32_32x32x16_bf16 v[240:255], a[200:203], v[110:113], v[240:255]
v_mfma_f32_32x32x16_bf16 v[66:81], a[204:207], v[110:113], v[66:81]
s_nop 2
s_cbranch_vccnz aretry_48
aretryok_47:
v_mfma_f32_32x32x16_bf16 v[224:239], a[40:43], v[128:131], v[224:239]
v_max3_u32 v40, v2, v3, v4
v_max3_u32 v40, v40, v5, v6
v_max3_u32 v40, v40, v7, v8
v_mfma_f32_32x32x16_bf16 v[50:65], a[44:47], v[128:131], v[50:65]
v_max_u32 v40, v40, v9
v_cmp_eq_u32 vcc, -1, v40
v_mfma_f32_32x32x16_bf16 v[240:255], a[40:43], v[132:135], v[240:255]
s_nop 3
s_cbranch_vccnz rretry_50
rretryok_49:
v_mfma_f32_32x32x16_bf16 v[66:81], a[44:47], v[132:135], v[66:81]
v_lshlrev_b32 v18, 16, v2
v_and_b32 v19, s82, v2
v_lshlrev_b32 v20, 16, v3
v_mfma_f32_32x32x16_bf16 v[224:239], a[96:99], v[136:139], v[224:239]
v_and_b32 v21, s82, v3
v_lshlrev_b32 v22, 16, v4
v_and_b32 v23, s82, v4
v_mfma_f32_32x32x16_bf16 v[50:65], a[100:103], v[136:139], v[50:65]
v_lshlrev_b32 v24, 16, v5
v_and_b32 v25, s82, v5
v_mfma_f32_32x32x16_bf16 v[240:255], a[96:99], v[140:143], v[240:255]
v_lshlrev_b32 v26, 16, v6
v_and_b32 v27, s82, v6
v_lshlrev_b32 v28, 16, v7
v_mfma_f32_32x32x16_bf16 v[66:81], a[100:103], v[140:143], v[66:81]
v_and_b32 v29, s82, v7
v_lshlrev_b32 v30, 16, v8
v_and_b32 v31, s82, v8
v_mfma_f32_32x32x16_bf16 v[224:239], a[152:155], v[144:147], v[224:239]
v_lshlrev_b32 v32, 16, v9
v_and_b32 v33, s82, v9
s_waitcnt vmcnt(7)
v_max3_u32 v120, v160, v161, v162
s_waitcnt vmcnt(6)
v_max3_u32 v120, v120, v163, v164
v_max3_u32 v120, v120, v165, v166
v_mfma_f32_32x32x16_bf16 v[50:65], a[156:159], v[144:147], v[50:65]
s_waitcnt vmcnt(5)
v_max3_u32 v120, v120, v167, v168
v_max3_u32 v120, v120, v169, v170
s_waitcnt vmcnt(4)
v_max3_u32 v120, v120, v171, v172
v_mfma_f32_32x32x16_bf16 v[240:255], a[152:155], v[148:151], v[240:255]
v_max3_u32 v120, v120, v173, v174
s_waitcnt vmcnt(3)
v_max3_u32 v120, v120, v175, v176
v_max3_u32 v120, v120, v177, v178
v_mfma_f32_32x32x16_bf16 v[66:81], a[156:159], v[148:151], v[66:81]
s_waitcnt vmcnt(2)
v_max3_u32 v120, v120, v179, v180
v_max3_u32 v120, v120, v181, v182
s_waitcnt vmcnt(1)
v_max3_u32 v120, v120, v183, v184
v_mfma_f32_32x32x16_bf16 v[224:239], a[208:211], v[152:155], v[224:239]
v_max3_u32 v120, v120, v185, v186
s_waitcnt vmcnt(0)
v_max3_u32 v120, v120, v187, v188
v_max3_u32 v120, v120, v189, v190
v_mfma_f32_32x32x16_bf16 v[50:65], a[212:215], v[152:155], v[50:65]
v_max_u32 v120, v120, v191
v_cmp_eq_u32 vcc, -1, v120
v_mfma_f32_32x32x16_bf16 v[240:255], a[208:211], v[156:159], v[240:255]
v_mfma_f32_32x32x16_bf16 v[66:81], a[212:215], v[156:159], v[66:81]
s_nop 2
v_cmp_u_f32 s[54:55], v224, v224
s_nop 6
v_cmp_u_f32 s[56:57], v240, v240
s_or_b64 s[54:55], s[54:55], s[56:57]
s_cmp_eq_u64 s[54:55], 0
s_cbranch_scc0 dslow_37
dgo_38:
s_nop 3
s_cbranch_vccnz aretry_52
aretryok_51:
v_mfma_f32_32x32x16_bf16 v[240:255], a[48:51], v[164:167], v[240:255]
v_mfma_f32_32x32x16_bf16 v[240:255], a[104:107], v[172:175], v[240:255]
v_mfma_f32_32x32x16_bf16 v[240:255], a[160:163], v[180:183], v[240:255]
v_mfma_f32_32x32x16_bf16 v[240:255], a[216:219], v[188:191], v[240:255]
v_mfma_f32_32x32x16_bf16 v[50:65], a[52:55], v[160:163], v[50:65]
v_mfma_f32_32x32x16_bf16 v[50:65], a[108:111], v[168:171], v[50:65]
s_nop 7
s_nop 1
ds_write_b128 v34, v[240:243] offset:0
v_mfma_f32_32x32x16_bf16 v[50:65], a[164:167], v[176:179], v[50:65]
ds_write_b128 v34, v[244:247] offset:1024
v_mfma_f32_32x32x16_bf16 v[50:65], a[220:223], v[184:187], v[50:65]
ds_write_b128 v34, v[248:251] offset:2048
ds_write_b128 v34, v[252:255] offset:3072
v_mfma_f32_32x32x16_bf16 v[66:81], a[52:55], v[164:167], v[66:81]
v_mfma_f32_32x32x16_bf16 v[66:81], a[108:111], v[172:175], v[66:81]
s_nop 7
ds_write_b128 v35, v[50:53] offset:16384
v_mfma_f32_32x32x16_bf16 v[66:81], a[164:167], v[180:183], v[66:81]
ds_write_b128 v35, v[54:57] offset:17408
v_mfma_f32_32x32x16_bf16 v[66:81], a[220:223], v[188:191], v[66:81]
ds_write_b128 v35, v[58:61] offset:18432
ds_write_b128 v35, v[62:65] offset:19456
v_mfma_f32_32x32x16_bf16 v[224:239], a[48:51], v[160:163], v[224:239]
v_mfma_f32_32x32x16_bf16 v[224:239], a[104:107], v[168:171], v[224:239]
s_nop 7
ds_write_b128 v36, v[66:69] offset:0
v_mfma_f32_32x32x16_bf16 v[224:239], a[160:163], v[176:179], v[224:239]
ds_write_b128 v36, v[70:73] offset:1024
v_mfma_f32_32x32x16_bf16 v[224:239], a[216:219], v[184:187], v[224:239]
ds_write_b128 v36, v[74:77] offset:2048
ds_write_b128 v36, v[78:81] offset:3072
s_waitcnt lgkmcnt(0)
s_barrier
ds_read_b128 v[128:131], v37 offset:0
ds_read_b128 v[132:135], v37 offset:1024
ds_read_b128 v[136:139], v37 offset:2048
ds_read_b128 v[140:143], v37 offset:3072
ds_read_b128 v[144:147], v37 offset:16384
ds_read_b128 v[148:151], v37 offset:17408
ds_read_b128 v[152:155], v37 offset:18432
ds_read_b128 v[156:159], v37 offset:19456
ds_read_b128 v[160:163], v38 offset:0
ds_read_b128 v[164:167], v38 offset:1024
ds_read_b128 v[168:171], v38 offset:2048
ds_read_b128 v[172:175], v38 offset:3072
s_waitcnt lgkmcnt(11)
v_pk_add_f32 v[224:225], v[224:225], v[128:129]
v_pk_add_f32 v[226:227], v[226:227], v[130:131]
s_waitcnt lgkmcnt(10)
v_pk_add_f32 v[228:229], v[228:229], v[132:133]
v_pk_add_f32 v[230:231], v[230:231], v[134:135]
s_waitcnt lgkmcnt(9)
v_pk_add_f32 v[232:233], v[232:233], v[136:137]
v_pk_add_f32 v[234:235], v[234:235], v[138:139]
s_waitcnt lgkmcnt(8)
v_pk_add_f32 v[236:237], v[236:237], v[140:141]
v_pk_add_f32 v[238:239], v[238:239], v[142:143]
s_waitcnt lgkmcnt(3)
v_pk_add_f32 v[144:145], v[144:145], v[160:161]
v_pk_add_f32 v[146:147], v[146:147], v[162:163]
s_waitcnt lgkmcnt(2)
v_pk_add_f32 v[148:149], v[148:149], v[164:165]
v_pk_add_f32 v[150:151], v[150:151], v[166:167]
s_waitcnt lgkmcnt(1)
v_pk_add_f32 v[152:153], v[152:153], v[168:169]
v_pk_add_f32 v[154:155], v[154:155], v[170:171]
s_waitcnt lgkmcnt(0)
v_pk_add_f32 v[156:157], v[156:157], v[172:173]
v_pk_add_f32 v[158:159], v[158:159], v[174:175]
v_pk_add_f32 v[224:225], v[224:225], v[144:145]
v_pk_add_f32 v[226:227], v[226:227], v[146:147]
v_pk_add_f32 v[228:229], v[228:229], v[148:149]
v_pk_add_f32 v[230:231], v[230:231], v[150:151]
v_pk_add_f32 v[232:233], v[232:233], v[152:153]
v_pk_add_f32 v[234:235], v[234:235], v[154:155]
v_pk_add_f32 v[236:237], v[236:237], v[156:157]
v_pk_add_f32 v[238:239], v[238:239], v[158:159]
v_cvt_pk_bf16_f32 v176, 0, v224
v_cvt_pk_bf16_f32 v177, 0, v225
v_cvt_pk_bf16_f32 v178, 0, v226
v_cvt_pk_bf16_f32 v179, 0, v227
v_cvt_pk_bf16_f32 v180, 0, v228
v_cvt_pk_bf16_f32 v181, 0, v229
v_cvt_pk_bf16_f32 v182, 0, v230
v_cvt_pk_bf16_f32 v183, 0, v231
v_cvt_pk_bf16_f32 v184, 0, v232
v_cvt_pk_bf16_f32 v185, 0, v233
v_cvt_pk_bf16_f32 v186, 0, v234
v_cvt_pk_bf16_f32 v187, 0, v235
v_cvt_pk_bf16_f32 v188, 0, v236
v_cvt_pk_bf16_f32 v189, 0, v237
v_cvt_pk_bf16_f32 v190, 0, v238
v_cvt_pk_bf16_f32 v191, 0, v239
v_pk_add_f32 v[176:177], v[176:177], v[18:19]
v_pk_add_f32 v[178:179], v[178:179], v[20:21]
v_pk_add_f32 v[180:181], v[180:181], v[22:23]
v_pk_add_f32 v[182:183], v[182:183], v[24:25]
v_pk_add_f32 v[184:185], v[184:185], v[26:27]
v_pk_add_f32 v[186:187], v[186:187], v[28:29]
v_pk_add_f32 v[188:189], v[188:189], v[30:31]
v_pk_add_f32 v[190:191], v[190:191], v[32:33]
v_cvt_pk_bf16_f32 v42, v176, v177
v_cvt_pk_bf16_f32 v43, v178, v179
v_cvt_pk_bf16_f32 v44, v180, v181
v_cvt_pk_bf16_f32 v45, v182, v183
v_cvt_pk_bf16_f32 v46, v184, v185
v_cvt_pk_bf16_f32 v47, v186, v187
v_cvt_pk_bf16_f32 v48, v188, v189
v_cvt_pk_bf16_f32 v49, v190, v191
v_permlane32_swap_b32 v42, v44
v_permlane32_swap_b32 v43, v45
v_permlane32_swap_b32 v46, v48
v_permlane32_swap_b32 v47, v49
buffer_store_dwordx4 v[42:45], v39, s[40:43], s78 offen offset:0
buffer_store_dwordx4 v[46:49], v39, s[40:43], s78 offen offset:32
s_waitcnt vmcnt(0)
s_endpgm
L_main_end:
kvlate_5:
v_mov_b32 v62, 0
v_mov_b32 v63, 0
v_mov_b32 v64, 0
v_mov_b32 v65, 0
v_mov_b32 v66, 0
v_mov_b32 v67, 0
v_mov_b32 v68, 0
v_mov_b32 v69, 0
v_mov_b32 v70, 0
v_mov_b32 v71, 0
v_mov_b32 v72, 0
v_mov_b32 v73, 0
v_mov_b32 v74, 0
v_mov_b32 v75, 0
v_mov_b32 v76, 0
v_mov_b32 v77, 0
v_lshrrev_b32 v53, 5, v0
s_lshl_b32 s54, s49, 3
s_add_u32 s54, s54, 0x24000
v_lshl_add_u32 v53, v53, 2, s54
s_lshl_b32 s54, s49, 11
s_add_u32 s54, s54, 0x22000
v_add_u32 v52, s54, v1
v_lshrrev_b32 v83, 5, v0
v_lshlrev_b32 v83, 4, v83
v_and_or_b32 v83, v0, 15, v83
v_lshlrev_b32 v83, 4, v83
v_add_u32 v78, 0x22000, v1
s_mul_i32 s54, s49, 2304
s_add_u32 s54, s54, 0x10000
v_add_u32 v79, s54, v1
v_lshlrev_b32 v80, 2, v0
s_add_u32 s54, s54, 2048
v_add_u32 v80, s54, v80
v_accvgpr_read_b32 v50, a246
v_mul_f32 v34, v50, v34
v_mul_f32 v35, v50, v35
v_cvt_pk_bf16_f32 v18, v34, v35
v_mul_f32 v36, v50, v36
v_mul_f32 v37, v50, v37
v_cvt_pk_bf16_f32 v19, v36, v37
v_mul_f32 v38, v50, v38
v_mul_f32 v39, v50, v39
v_cvt_pk_bf16_f32 v20, v38, v39
v_mul_f32 v40, v50, v40
v_mul_f32 v41, v50, v41
v_cvt_pk_bf16_f32 v21, v40, v41
v_mul_f32 v42, v50, v42
v_mul_f32 v43, v50, v43
v_cvt_pk_bf16_f32 v22, v42, v43
v_mul_f32 v44, v50, v44
v_mul_f32 v45, v50, v45
v_cvt_pk_bf16_f32 v23, v44, v45
v_mul_f32 v46, v50, v46
v_mul_f32 v47, v50, v47
v_cvt_pk_bf16_f32 v24, v46, v47
v_mul_f32 v48, v50, v48
v_mul_f32 v49, v50, v49
v_cvt_pk_bf16_f32 v25, v48, v49
ds_write_b128 v52, v[18:21]
ds_write_b128 v52, v[22:25] offset:1024
v_mov_b32 v50, 0
v_mov_b32 v51, 0
v_dot2_f32_bf16 v50, v18, v18, v50
v_dot2_f32_bf16 v51, v22, v22, v51
v_dot2_f32_bf16 v50, v19, v19, v50
v_dot2_f32_bf16 v51, v23, v23, v51
v_dot2_f32_bf16 v50, v20, v20, v50
v_dot2_f32_bf16 v51, v24, v24, v51
v_dot2_f32_bf16 v50, v21, v21, v50
v_dot2_f32_bf16 v51, v25, v25, v51
s_nop 2
v_permlane32_swap_b32 v50, v51
v_add_f32 v50, v50, v51
s_nop 1
v_max_f32_dpp v50, v50, v50 row_ror:8 row_mask:0xf bank_mask:0xf
s_nop 1
v_max_f32_dpp v50, v50, v50 row_ror:4 row_mask:0xf bank_mask:0xf
s_nop 1
v_max_f32_dpp v50, v50, v50 row_ror:2 row_mask:0xf bank_mask:0xf
s_nop 1
v_max_f32_dpp v50, v50, v50 row_ror:1 row_mask:0xf bank_mask:0xf
v_mov_b32 v51, v50
s_nop 1
v_permlane16_swap_b32 v50, v51
v_max_f32 v50, v50, v51
ds_write_b32 v53, v50
s_mov_b32 s68, 0
kvpoll_53:
s_lshl_b32 s54, s49, 13
s_add_u32 s54, s54, 0xc0000
buffer_load_dwordx4 v[26:29], v30, s[44:47], s54 offen sc1
s_add_u32 s68, s68, 1
s_xor_b32 s57, s52, 0x9e3779b9
s_waitcnt vmcnt(0)
v_cmp_eq_u32 vcc, s52, v26
v_cmp_eq_u32 s[58:59], s53, v27
s_and_b64 s[58:59], vcc, s[58:59]
v_cmp_eq_u32 vcc, s57, v29
s_and_b64 vcc, vcc, s[58:59]
s_cmp_eq_u64 vcc, -1
s_cbranch_scc1 kvpollok_54
s_cmp_lt_u32 s68, 0x30d40
s_cbranch_scc0 L_abort
s_sleep 1
s_branch kvpoll_53
kvpollok_54:
buffer_load_dwordx4 a[0:3], v1, s[44:47], s60 offen offset:0 sc1
buffer_load_dwordx4 a[4:7], v1, s[44:47], s60 offen offset:1024 sc1
buffer_load_dwordx4 a[8:11], v1, s[44:47], s60 offen offset:2048 sc1
buffer_load_dwordx4 a[12:15], v1, s[44:47], s60 offen offset:3072 sc1
s_mov_b64 exec, s[70:71]
buffer_load_dwordx4 a[64:67], v31, s[44:47], s64 offen offset:0 sc1
buffer_load_dwordx4 a[68:71], v31, s[44:47], s64 offen offset:512 sc1
s_mov_b64 exec, -1
s_mov_b64 exec, s[70:71]
buffer_load_dwordx4 a[72:75], v31, s[44:47], s64 offen offset:1024 sc1
buffer_load_dwordx4 a[76:79], v31, s[44:47], s64 offen offset:1536 sc1
s_mov_b64 exec, -1
s_mov_b64 exec, s[70:71]
buffer_load_dwordx4 a[80:83], v31, s[44:47], s64 offen offset:2048 sc1
buffer_load_dwordx4 a[84:87], v31, s[44:47], s64 offen offset:2560 sc1
s_mov_b64 exec, -1
s_mov_b64 exec, s[70:71]
buffer_load_dwordx4 a[88:91], v31, s[44:47], s64 offen offset:3072 sc1
buffer_load_dwordx4 a[92:95], v31, s[44:47], s64 offen offset:3584 sc1
s_mov_b64 exec, -1
v_max_f32_dpp v28, v28, v28 row_ror:8 row_mask:0xf bank_mask:0xf
s_nop 1
v_max_f32_dpp v28, v28, v28 row_ror:4 row_mask:0xf bank_mask:0xf
s_nop 1
v_max_f32_dpp v28, v28, v28 row_ror:2 row_mask:0xf bank_mask:0xf
s_nop 1
v_max_f32_dpp v28, v28, v28 row_ror:1 row_mask:0xf bank_mask:0xf
v_mov_b32 v82, v28
s_nop 1
v_permlane16_swap_b32 v28, v82
v_max_f32 v28, v28, v82
v_mov_b32 v82, v28
s_nop 1
v_permlane32_swap_b32 v28, v82
v_max_f32 v28, v28, v82
s_lshl_b32 s54, s49, 2
s_add_u32 s54, s54, 0x24020
v_mov_b32 v82, s54
ds_write_b32 v82, v28
s_branch kvjoined_6
ptslow_11:
v_lshlrev_b32 v120, 3, v0
s_lshl_b32 s54, s73, 9
s_add_u32 s54, s54, 0x2000
s_mov_b32 s68, 0
poll_55:
buffer_load_dwordx2 v[124:125], v120, s[44:47], s54 offen sc1
s_add_u32 s68, s68, 1
s_waitcnt vmcnt(0)
v_cmp_eq_u32 vcc, s52, v124
v_cmp_eq_u32 s[58:59], s53, v125
s_and_b64 vcc, vcc, s[58:59]
s_cmp_eq_u64 vcc, -1
s_cbranch_scc1 pollok_56
s_cmp_lt_u32 s68, 0x30d40
s_cbranch_scc0 L_abort
s_sleep 1
s_branch poll_55
pollok_56:
s_lshl_b32 s54, s51, 2
s_add_u32 s54, s54, s49
s_lshl_b32 s54, s54, 2
s_add_u32 s54, s54, 3
v_lshlrev_b32 v120, 6, v0
v_add_u32 v120, s54, v120
v_subrev_u32 v121, 16, v0
v_min_u32 v121, 27, v121
v_lshrrev_b32 v122, 1, v121
s_mul_i32 s54, s49, 14
v_add_u32 v122, s54, v122
v_lshlrev_b32 v122, 4, v122
v_and_b32 v121, 1, v121
s_lshl_b32 s54, s75, 1
v_add_u32 v121, s54, v121
v_add_u32 v121, v121, v122
v_cmp_gt_u32 vcc, 16, v0
s_nop 1
v_cndmask_b32 v120, v121, v120, vcc
v_lshlrev_b32 v120, 3, v120
s_mov_b32 s54, 0x2000
s_mov_b32 s68, 0
poll_57:
buffer_load_dwordx2 v[124:125], v120, s[44:47], s54 offen sc1
s_add_u32 s68, s68, 1
s_waitcnt vmcnt(0)
v_cmp_eq_u32 vcc, s52, v124
v_cmp_eq_u32 s[58:59], s53, v125
s_and_b64 vcc, vcc, s[58:59]
s_cmp_eq_u64 vcc, -1
s_cbranch_scc1 pollok_58
s_cmp_lt_u32 s68, 0x30d40
s_cbranch_scc0 L_abort
s_sleep 1
s_branch poll_57
pollok_58:
v_lshlrev_b32 v120, 3, v0
s_lshl_b32 s54, s49, 11
s_add_u32 s54, s54, 0x2000
s_mov_b32 s68, 0
poll_59:
buffer_load_dwordx2 v[124:125], v120, s[44:47], s54 offen sc1
s_add_u32 s68, s68, 1
s_waitcnt vmcnt(0)
v_cmp_eq_u32 vcc, s52, v124
v_cmp_eq_u32 s[58:59], s53, v125
s_and_b64 vcc, vcc, s[58:59]
s_cmp_eq_u64 vcc, -1
s_cbranch_scc1 pollok_60
s_cmp_lt_u32 s68, 0x30d40
s_cbranch_scc0 L_abort
s_sleep 1
s_branch poll_59
pollok_60:
v_lshlrev_b32 v120, 3, v0
s_lshl_b32 s54, s49, 11
s_add_u32 s54, s54, 0x2200
s_mov_b32 s68, 0
poll_61:
buffer_load_dwordx2 v[124:125], v120, s[44:47], s54 offen sc1
s_add_u32 s68, s68, 1
s_waitcnt vmcnt(0)
v_cmp_eq_u32 vcc, s52, v124
v_cmp_eq_u32 s[58:59], s53, v125
s_and_b64 vcc, vcc, s[58:59]
s_cmp_eq_u64 vcc, -1
s_cbranch_scc1 pollok_62
s_cmp_lt_u32 s68, 0x30d40
s_cbranch_scc0 L_abort
s_sleep 1
s_branch poll_61
pollok_62:
v_lshlrev_b32 v120, 3, v0
s_lshl_b32 s54, s49, 11
s_add_u32 s54, s54, 0x2400
s_mov_b32 s68, 0
poll_63:
buffer_load_dwordx2 v[124:125], v120, s[44:47], s54 offen sc1
s_add_u32 s68, s68, 1
s_waitcnt vmcnt(0)
v_cmp_eq_u32 vcc, s52, v124
v_cmp_eq_u32 s[58:59], s53, v125
s_and_b64 vcc, vcc, s[58:59]
s_cmp_eq_u64 vcc, -1
s_cbranch_scc1 pollok_64
s_cmp_lt_u32 s68, 0x30d40
s_cbranch_scc0 L_abort
s_sleep 1
s_branch poll_63
pollok_64:
v_lshlrev_b32 v120, 3, v0
s_lshl_b32 s54, s49, 11
s_add_u32 s54, s54, 0x2600
s_mov_b32 s68, 0
poll_65:
buffer_load_dwordx2 v[124:125], v120, s[44:47], s54 offen sc1
s_add_u32 s68, s68, 1
s_waitcnt vmcnt(0)
v_cmp_eq_u32 vcc, s52, v124
v_cmp_eq_u32 s[58:59], s53, v125
s_and_b64 vcc, vcc, s[58:59]
s_cmp_eq_u64 vcc, -1
s_cbranch_scc1 pollok_66
s_cmp_lt_u32 s68, 0x30d40
s_cbranch_scc0 L_abort
s_sleep 1
s_branch poll_65
pollok_66:
s_branch ptok_12
slowpre_13:
v_and_b32 v96, 31, v0
v_lshlrev_b32 v96, 2, v96
v_add_u32 v96, 0x24040, v96
buffer_load_dwordx4 a[16:19], v1, s[44:47], s61 offen offset:0 sc1
buffer_load_dwordx4 a[20:23], v1, s[44:47], s61 offen offset:1024 sc1
buffer_load_dwordx4 a[24:27], v1, s[44:47], s61 offen offset:2048 sc1
buffer_load_dwordx4 a[28:31], v1, s[44:47], s61 offen offset:3072 sc1
buffer_load_dwordx4 a[32:35], v1, s[44:47], s62 offen offset:0 sc1
buffer_load_dwordx4 a[36:39], v1, s[44:47], s62 offen offset:1024 sc1
buffer_load_dwordx4 a[40:43], v1, s[44:47], s62 offen offset:2048 sc1
buffer_load_dwordx4 a[44:47], v1, s[44:47], s62 offen offset:3072 sc1
buffer_load_dwordx4 a[48:51], v1, s[44:47], s63 offen offset:0 sc1
buffer_load_dwordx4 a[52:55], v1, s[44:47], s63 offen offset:1024 sc1
buffer_load_dwordx4 a[56:59], v1, s[44:47], s63 offen offset:2048 sc1
buffer_load_dwordx4 a[60:63], v1, s[44:47], s63 offen offset:3072 sc1
v_add_u32 v82, 0x22000, v1
v_and_b32 v85, 31, v0
v_lshlrev_b32 v85, 2, v85
s_lshl_b32 s54, s49, 10
s_add_u32 s54, s54, 0x24040
v_add_u32 v86, s54, v85
s_mov_b32 s55, 0
slowh_15:
s_waitcnt lgkmcnt(0)
ds_read_b128 v[58:61], v82
v_mov_b32 v84, 0xff800000
s_waitcnt vmcnt(23) lgkmcnt(0)
v_mfma_f32_32x32x16_bf16 v[2:17], a[0:3], v[58:61], 0
s_nop 7
s_nop 3
v_max3_f32 v84, v84, v2, v3
v_max3_f32 v84, v84, v4, v5
v_max3_f32 v84, v84, v6, v7
v_max3_f32 v84, v84, v8, v9
v_max3_f32 v84, v84, v10, v11
v_max3_f32 v84, v84, v12, v13
v_max3_f32 v84, v84, v14, v15
v_max3_f32 v84, v84, v16, v17
s_waitcnt vmcnt(22)
v_mfma_f32_32x32x16_bf16 v[2:17], a[4:7], v[58:61], 0
s_nop 7
s_nop 3
v_max3_f32 v84, v84, v2, v3
v_max3_f32 v84, v84, v4, v5
v_max3_f32 v84, v84, v6, v7
v_max3_f32 v84, v84, v8, v9
v_max3_f32 v84, v84, v10, v11
v_max3_f32 v84, v84, v12, v13
v_max3_f32 v84, v84, v14, v15
v_max3_f32 v84, v84, v16, v17
s_waitcnt vmcnt(21)
v_mfma_f32_32x32x16_bf16 v[2:17], a[8:11], v[58:61], 0
s_nop 7
s_nop 3
v_max3_f32 v84, v84, v2, v3
v_max3_f32 v84, v84, v4, v5
v_max3_f32 v84, v84, v6, v7
v_max3_f32 v84, v84, v8, v9
v_max3_f32 v84, v84, v10, v11
v_max3_f32 v84, v84, v12, v13
v_max3_f32 v84, v84, v14, v15
v_max3_f32 v84, v84, v16, v17
s_waitcnt vmcnt(20)
v_mfma_f32_32x32x16_bf16 v[2:17], a[12:15], v[58:61], 0
s_nop 7
s_nop 3
v_max3_f32 v84, v84, v2, v3
v_max3_f32 v84, v84, v4, v5
v_max3_f32 v84, v84, v6, v7
v_max3_f32 v84, v84, v8, v9
v_max3_f32 v84, v84, v10, v11
v_max3_f32 v84, v84, v12, v13
v_max3_f32 v84, v84, v14, v15
v_max3_f32 v84, v84, v16, v17
s_waitcnt vmcnt(11)
v_mfma_f32_32x32x16_bf16 v[2:17], a[16:19], v[58:61], 0
s_nop 7
s_nop 3
v_max3_f32 v84, v84, v2, v3
v_max3_f32 v84, v84, v4, v5
v_max3_f32 v84, v84, v6, v7
v_max3_f32 v84, v84, v8, v9
v_max3_f32 v84, v84, v10, v11
v_max3_f32 v84, v84, v12, v13
v_max3_f32 v84, v84, v14, v15
v_max3_f32 v84, v84, v16, v17
s_waitcnt vmcnt(10)
v_mfma_f32_32x32x16_bf16 v[2:17], a[20:23], v[58:61], 0
s_nop 7
s_nop 3
v_max3_f32 v84, v84, v2, v3
v_max3_f32 v84, v84, v4, v5
v_max3_f32 v84, v84, v6, v7
v_max3_f32 v84, v84, v8, v9
v_max3_f32 v84, v84, v10, v11
v_max3_f32 v84, v84, v12, v13
v_max3_f32 v84, v84, v14, v15
v_max3_f32 v84, v84, v16, v17
s_waitcnt vmcnt(9)
v_mfma_f32_32x32x16_bf16 v[2:17], a[24:27], v[58:61], 0
s_nop 7
s_nop 3
v_max3_f32 v84, v84, v2, v3
v_max3_f32 v84, v84, v4, v5
v_max3_f32 v84, v84, v6, v7
v_max3_f32 v84, v84, v8, v9
v_max3_f32 v84, v84, v10, v11
v_max3_f32 v84, v84, v12, v13
v_max3_f32 v84, v84, v14, v15
v_max3_f32 v84, v84, v16, v17
s_waitcnt vmcnt(8)
v_mfma_f32_32x32x16_bf16 v[2:17], a[28:31], v[58:61], 0
s_nop 7
s_nop 3
v_max3_f32 v84, v84, v2, v3
v_max3_f32 v84, v84, v4, v5
v_max3_f32 v84, v84, v6, v7
v_max3_f32 v84, v84, v8, v9
v_max3_f32 v84, v84, v10, v11
v_max3_f32 v84, v84, v12, v13
v_max3_f32 v84, v84, v14, v15
v_max3_f32 v84, v84, v16, v17
s_waitcnt vmcnt(7)
v_mfma_f32_32x32x16_bf16 v[2:17], a[32:35], v[58:61], 0
s_nop 7
s_nop 3
v_max3_f32 v84, v84, v2, v3
v_max3_f32 v84, v84, v4, v5
v_max3_f32 v84, v84, v6, v7
v_max3_f32 v84, v84, v8, v9
v_max3_f32 v84, v84, v10, v11
v_max3_f32 v84, v84, v12, v13
v_max3_f32 v84, v84, v14, v15
v_max3_f32 v84, v84, v16, v17
s_waitcnt vmcnt(6)
v_mfma_f32_32x32x16_bf16 v[2:17], a[36:39], v[58:61], 0
s_nop 7
s_nop 3
v_max3_f32 v84, v84, v2, v3
v_max3_f32 v84, v84, v4, v5
v_max3_f32 v84, v84, v6, v7
v_max3_f32 v84, v84, v8, v9
v_max3_f32 v84, v84, v10, v11
v_max3_f32 v84, v84, v12, v13
v_max3_f32 v84, v84, v14, v15
v_max3_f32 v84, v84, v16, v17
s_waitcnt vmcnt(5)
v_mfma_f32_32x32x16_bf16 v[2:17], a[40:43], v[58:61], 0
s_nop 7
s_nop 3
v_max3_f32 v84, v84, v2, v3
v_max3_f32 v84, v84, v4, v5
v_max3_f32 v84, v84, v6, v7
v_max3_f32 v84, v84, v8, v9
v_max3_f32 v84, v84, v10, v11
v_max3_f32 v84, v84, v12, v13
v_max3_f32 v84, v84, v14, v15
v_max3_f32 v84, v84, v16, v17
s_waitcnt vmcnt(4)
v_mfma_f32_32x32x16_bf16 v[2:17], a[44:47], v[58:61], 0
s_nop 7
s_nop 3
v_max3_f32 v84, v84, v2, v3
v_max3_f32 v84, v84, v4, v5
v_max3_f32 v84, v84, v6, v7
v_max3_f32 v84, v84, v8, v9
v_max3_f32 v84, v84, v10, v11
v_max3_f32 v84, v84, v12, v13
v_max3_f32 v84, v84, v14, v15
v_max3_f32 v84, v84, v16, v17
s_waitcnt vmcnt(3)
v_mfma_f32_32x32x16_bf16 v[2:17], a[48:51], v[58:61], 0
s_nop 7
s_nop 3
v_max3_f32 v84, v84, v2, v3
v_max3_f32 v84, v84, v4, v5
v_max3_f32 v84, v84, v6, v7
v_max3_f32 v84, v84, v8, v9
v_max3_f32 v84, v84, v10, v11
v_max3_f32 v84, v84, v12, v13
v_max3_f32 v84, v84, v14, v15
v_max3_f32 v84, v84, v16, v17
s_waitcnt vmcnt(2)
v_mfma_f32_32x32x16_bf16 v[2:17], a[52:55], v[58:61], 0
s_nop 7
s_nop 3
v_max3_f32 v84, v84, v2, v3
v_max3_f32 v84, v84, v4, v5
v_max3_f32 v84, v84, v6, v7
v_max3_f32 v84, v84, v8, v9
v_max3_f32 v84, v84, v10, v11
v_max3_f32 v84, v84, v12, v13
v_max3_f32 v84, v84, v14, v15
v_max3_f32 v84, v84, v16, v17
s_waitcnt vmcnt(1)
v_mfma_f32_32x32x16_bf16 v[2:17], a[56:59], v[58:61], 0
s_nop 7
s_nop 3
v_max3_f32 v84, v84, v2, v3
v_max3_f32 v84, v84, v4, v5
v_max3_f32 v84, v84, v6, v7
v_max3_f32 v84, v84, v8, v9
v_max3_f32 v84, v84, v10, v11
v_max3_f32 v84, v84, v12, v13
v_max3_f32 v84, v84, v14, v15
v_max3_f32 v84, v84, v16, v17
s_waitcnt vmcnt(0)
v_mfma_f32_32x32x16_bf16 v[2:17], a[60:63], v[58:61], 0
s_nop 7
s_nop 3
v_max3_f32 v84, v84, v2, v3
v_max3_f32 v84, v84, v4, v5
v_max3_f32 v84, v84, v6, v7
v_max3_f32 v84, v84, v8, v9
v_max3_f32 v84, v84, v10, v11
v_max3_f32 v84, v84, v12, v13
v_max3_f32 v84, v84, v14, v15
v_max3_f32 v84, v84, v16, v17
v_mov_b32 v85, v84
s_nop 1
v_permlane32_swap_b32 v84, v85
v_max_f32 v84, v84, v85
ds_write_b32 v86, v84
v_add_u32 v82, 1024, v82
v_add_u32 v86, 128, v86
s_add_u32 s55, s55, 1
s_cmp_lt_u32 s55, 8
s_cbranch_scc1 slowh_15
s_waitcnt lgkmcnt(0)
s_barrier
ds_read_b32 v84, v96 offset:0
ds_read_b32 v85, v96 offset:1024
ds_read_b32 v86, v96 offset:2048
ds_read_b32 v87, v96 offset:3072
s_waitcnt lgkmcnt(1)
v_max3_f32 v84, v84, v85, v86
s_waitcnt lgkmcnt(0)
v_max_f32 v84, v84, v87
v_sub_f32 v62, 0, v84
v_sub_f32 v63, 0, v84
v_sub_f32 v64, 0, v84
v_sub_f32 v65, 0, v84
v_sub_f32 v66, 0, v84
v_sub_f32 v67, 0, v84
v_sub_f32 v68, 0, v84
v_sub_f32 v69, 0, v84
v_sub_f32 v70, 0, v84
v_sub_f32 v71, 0, v84
v_sub_f32 v72, 0, v84
v_sub_f32 v73, 0, v84
v_sub_f32 v74, 0, v84
v_sub_f32 v75, 0, v84
v_sub_f32 v76, 0, v84
v_sub_f32 v77, 0, v84
ds_read_b128 v[58:61], v78
s_branch slowback_14
negslow_16:
ds_read_b32 v84, v96 offset:128
ds_read_b32 v85, v96 offset:1152
ds_read_b32 v86, v96 offset:2176
ds_read_b32 v87, v96 offset:3200
s_waitcnt lgkmcnt(1)
v_max3_f32 v84, v84, v85, v86
s_waitcnt lgkmcnt(0)
v_max_f32 v84, v84, v87
v_sub_f32 v62, 0, v84
v_sub_f32 v63, 0, v84
v_sub_f32 v64, 0, v84
v_sub_f32 v65, 0, v84
v_sub_f32 v66, 0, v84
v_sub_f32 v67, 0, v84
v_sub_f32 v68, 0, v84
v_sub_f32 v69, 0, v84
v_sub_f32 v70, 0, v84
v_sub_f32 v71, 0, v84
v_sub_f32 v72, 0, v84
v_sub_f32 v73, 0, v84
v_sub_f32 v74, 0, v84
v_sub_f32 v75, 0, v84
v_sub_f32 v76, 0, v84
v_sub_f32 v77, 0, v84
s_branch negback_17
negslow_18:
ds_read_b32 v84, v96 offset:256
ds_read_b32 v85, v96 offset:1280
ds_read_b32 v86, v96 offset:2304
ds_read_b32 v87, v96 offset:3328
s_waitcnt lgkmcnt(1)
v_max3_f32 v84, v84, v85, v86
s_waitcnt lgkmcnt(0)
v_max_f32 v84, v84, v87
v_sub_f32 v62, 0, v84
v_sub_f32 v63, 0, v84
v_sub_f32 v64, 0, v84
v_sub_f32 v65, 0, v84
v_sub_f32 v66, 0, v84
v_sub_f32 v67, 0, v84
v_sub_f32 v68, 0, v84
v_sub_f32 v69, 0, v84
v_sub_f32 v70, 0, v84
v_sub_f32 v71, 0, v84
v_sub_f32 v72, 0, v84
v_sub_f32 v73, 0, v84
v_sub_f32 v74, 0, v84
v_sub_f32 v75, 0, v84
v_sub_f32 v76, 0, v84
v_sub_f32 v77, 0, v84
s_branch negback_19
negslow_20:
ds_read_b32 v84, v96 offset:384
ds_read_b32 v85, v96 offset:1408
ds_read_b32 v86, v96 offset:2432
ds_read_b32 v87, v96 offset:3456
s_waitcnt lgkmcnt(1)
v_max3_f32 v84, v84, v85, v86
s_waitcnt lgkmcnt(0)
v_max_f32 v84, v84, v87
v_sub_f32 v62, 0, v84
v_sub_f32 v63, 0, v84
v_sub_f32 v64, 0, v84
v_sub_f32 v65, 0, v84
v_sub_f32 v66, 0, v84
v_sub_f32 v67, 0, v84
v_sub_f32 v68, 0, v84
v_sub_f32 v69, 0, v84
v_sub_f32 v70, 0, v84
v_sub_f32 v71, 0, v84
v_sub_f32 v72, 0, v84
v_sub_f32 v73, 0, v84
v_sub_f32 v74, 0, v84
v_sub_f32 v75, 0, v84
v_sub_f32 v76, 0, v84
v_sub_f32 v77, 0, v84
s_branch negback_21
negslow_22:
ds_read_b32 v84, v96 offset:512
ds_read_b32 v85, v96 offset:1536
ds_read_b32 v86, v96 offset:2560
ds_read_b32 v87, v96 offset:3584
s_waitcnt lgkmcnt(1)
v_max3_f32 v84, v84, v85, v86
s_waitcnt lgkmcnt(0)
v_max_f32 v84, v84, v87
v_sub_f32 v62, 0, v84
v_sub_f32 v63, 0, v84
v_sub_f32 v64, 0, v84
v_sub_f32 v65, 0, v84
v_sub_f32 v66, 0, v84
v_sub_f32 v67, 0, v84
v_sub_f32 v68, 0, v84
v_sub_f32 v69, 0, v84
v_sub_f32 v70, 0, v84
v_sub_f32 v71, 0, v84
v_sub_f32 v72, 0, v84
v_sub_f32 v73, 0, v84
v_sub_f32 v74, 0, v84
v_sub_f32 v75, 0, v84
v_sub_f32 v76, 0, v84
v_sub_f32 v77, 0, v84
s_branch negback_23
negslow_24:
ds_read_b32 v84, v96 offset:640
ds_read_b32 v85, v96 offset:1664
ds_read_b32 v86, v96 offset:2688
ds_read_b32 v87, v96 offset:3712
s_waitcnt lgkmcnt(1)
v_max3_f32 v84, v84, v85, v86
s_waitcnt lgkmcnt(0)
v_max_f32 v84, v84, v87
v_sub_f32 v62, 0, v84
v_sub_f32 v63, 0, v84
v_sub_f32 v64, 0, v84
v_sub_f32 v65, 0, v84
v_sub_f32 v66, 0, v84
v_sub_f32 v67, 0, v84
v_sub_f32 v68, 0, v84
v_sub_f32 v69, 0, v84
v_sub_f32 v70, 0, v84
v_sub_f32 v71, 0, v84
v_sub_f32 v72, 0, v84
v_sub_f32 v73, 0, v84
v_sub_f32 v74, 0, v84
v_sub_f32 v75, 0, v84
v_sub_f32 v76, 0, v84
v_sub_f32 v77, 0, v84
s_branch negback_25
negslow_26:
ds_read_b32 v84, v96 offset:768
ds_read_b32 v85, v96 offset:1792
ds_read_b32 v86, v96 offset:2816
ds_read_b32 v87, v96 offset:3840
s_waitcnt lgkmcnt(1)
v_max3_f32 v84, v84, v85, v86
s_waitcnt lgkmcnt(0)
v_max_f32 v84, v84, v87
v_sub_f32 v62, 0, v84
v_sub_f32 v63, 0, v84
v_sub_f32 v64, 0, v84
v_sub_f32 v65, 0, v84
v_sub_f32 v66, 0, v84
v_sub_f32 v67, 0, v84
v_sub_f32 v68, 0, v84
v_sub_f32 v69, 0, v84
v_sub_f32 v70, 0, v84
v_sub_f32 v71, 0, v84
v_sub_f32 v72, 0, v84
v_sub_f32 v73, 0, v84
v_sub_f32 v74, 0, v84
v_sub_f32 v75, 0, v84
v_sub_f32 v76, 0, v84
v_sub_f32 v77, 0, v84
s_branch negback_27
negslow_28:
ds_read_b32 v84, v96 offset:896
ds_read_b32 v85, v96 offset:1920
ds_read_b32 v86, v96 offset:2944
ds_read_b32 v87, v96 offset:3968
s_waitcnt lgkmcnt(1)
v_max3_f32 v84, v84, v85, v86
s_waitcnt lgkmcnt(0)
v_max_f32 v84, v84, v87
v_sub_f32 v62, 0, v84
v_sub_f32 v63, 0, v84
v_sub_f32 v64, 0, v84
v_sub_f32 v65, 0, v84
v_sub_f32 v66, 0, v84
v_sub_f32 v67, 0, v84
v_sub_f32 v68, 0, v84
v_sub_f32 v69, 0, v84
v_sub_f32 v70, 0, v84
v_sub_f32 v71, 0, v84
v_sub_f32 v72, 0, v84
v_sub_f32 v73, 0, v84
v_sub_f32 v74, 0, v84
v_sub_f32 v75, 0, v84
v_sub_f32 v76, 0, v84
v_sub_f32 v77, 0, v84
s_branch negback_29
gretry_32:
s_mov_b32 s68, 0
gretryl_67:
s_add_u32 s68, s68, 1
s_cmp_lt_u32 s68, 0x30d40
s_cbranch_scc0 L_abort
s_sleep 1
buffer_load_dwordx4 v[50:53], v1, s[44:47], s76 offen offset:0 sc1
buffer_load_dwordx4 v[54:57], v1, s[44:47], s76 offen offset:1024 sc1
buffer_load_dwordx4 v[58:61], v1, s[44:47], s77 offen offset:0 sc1
buffer_load_dwordx4 v[62:65], v1, s[44:47], s77 offen offset:1024 sc1
buffer_load_dwordx4 v[66:69], v1, s[44:47], s78 offen offset:0 sc1
buffer_load_dwordx4 v[70:73], v1, s[44:47], s78 offen offset:1024 sc1
s_waitcnt vmcnt(5)
v_max3_u32 v84, v50, v51, v52
s_waitcnt vmcnt(4)
v_max3_u32 v84, v84, v53, v54
v_max3_u32 v84, v84, v55, v56
s_waitcnt vmcnt(3)
v_max3_u32 v84, v84, v57, v58
v_max3_u32 v84, v84, v59, v60
s_waitcnt vmcnt(2)
v_max3_u32 v84, v84, v61, v62
v_max3_u32 v84, v84, v63, v64
s_waitcnt vmcnt(1)
v_max3_u32 v84, v84, v65, v66
v_max3_u32 v84, v84, v67, v68
s_waitcnt vmcnt(0)
v_max3_u32 v84, v84, v69, v70
v_max3_u32 v84, v84, v71, v72
v_max_u32 v84, v84, v73
v_cmp_eq_u32 vcc, -1, v84
s_nop 4
s_cbranch_vccnz gretryl_67
s_branch gok_31
hretry_34:
s_mov_b32 s68, 0
hretryl_68:
s_add_u32 s68, s68, 1
s_cmp_lt_u32 s68, 0x30d40
s_cbranch_scc0 L_abort
s_sleep 1
buffer_load_dwordx4 v[82:85], v121, s[44:47], s78 offen offset:0 sc1
buffer_load_dwordx4 v[86:89], v121, s[44:47], s78 offen offset:512 sc1
buffer_load_dwordx4 v[90:93], v121, s[44:47], s78 offen offset:1024 sc1
buffer_load_dwordx4 v[94:97], v121, s[44:47], s78 offen offset:1536 sc1
buffer_load_dwordx4 v[128:131], v1, s[44:47], s79 offen offset:0 sc1
buffer_load_dwordx4 v[132:135], v1, s[44:47], s79 offen offset:1024 sc1
buffer_load_dwordx4 v[136:139], v1, s[44:47], s79 offen offset:2048 sc1
buffer_load_dwordx4 v[140:143], v1, s[44:47], s79 offen offset:3072 sc1
s_add_u32 s62, s79, 0x1000
s_waitcnt vmcnt(15)
buffer_load_dwordx4 v[144:147], v1, s[44:47], s62 offen offset:0 sc1
s_waitcnt vmcnt(15)
buffer_load_dwordx4 v[148:151], v1, s[44:47], s62 offen offset:1024 sc1
s_waitcnt vmcnt(15)
buffer_load_dwordx4 v[152:155], v1, s[44:47], s62 offen offset:2048 sc1
s_waitcnt vmcnt(15)
buffer_load_dwordx4 v[156:159], v1, s[44:47], s62 offen offset:3072 sc1
s_add_u32 s62, s79, 0x2000
s_waitcnt vmcnt(15)
buffer_load_dwordx4 v[160:163], v1, s[44:47], s62 offen offset:0 sc1
s_waitcnt vmcnt(15)
buffer_load_dwordx4 v[164:167], v1, s[44:47], s62 offen offset:1024 sc1
s_waitcnt vmcnt(15)
buffer_load_dwordx4 v[168:171], v1, s[44:47], s62 offen offset:2048 sc1
s_waitcnt vmcnt(15)
buffer_load_dwordx4 v[172:175], v1, s[44:47], s62 offen offset:3072 sc1
s_waitcnt vmcnt(15)
v_max3_u32 v120, v82, v83, v84
s_waitcnt vmcnt(14)
v_max3_u32 v120, v120, v85, v86
v_max3_u32 v120, v120, v87, v88
s_waitcnt vmcnt(13)
v_max3_u32 v120, v120, v89, v90
v_max3_u32 v120, v120, v91, v92
s_waitcnt vmcnt(12)
v_max3_u32 v120, v120, v93, v94
v_max3_u32 v120, v120, v95, v96
s_waitcnt vmcnt(11)
v_max3_u32 v120, v120, v97, v128
v_max3_u32 v120, v120, v129, v130
s_waitcnt vmcnt(10)
v_max3_u32 v120, v120, v131, v132
v_max3_u32 v120, v120, v133, v134
s_waitcnt vmcnt(9)
v_max3_u32 v120, v120, v135, v136
v_max3_u32 v120, v120, v137, v138
s_waitcnt vmcnt(8)
v_max3_u32 v120, v120, v139, v140
v_max3_u32 v120, v120, v141, v142
v_max_u32 v120, v120, v143
v_cmp_eq_u32 vcc, -1, v120
s_nop 4
s_cbranch_vccnz hretryl_68
s_branch hok_33
hslow_35:
s_mov_b32 s68, 0
hslowl_69:
s_add_u32 s68, s68, 1
s_cmp_lt_u32 s68, 0x30d40
s_cbranch_scc0 L_abort
buffer_load_dwordx4 v[82:85], v121, s[44:47], s78 offen offset:0 sc1
buffer_load_dwordx4 v[86:89], v121, s[44:47], s78 offen offset:512 sc1
buffer_load_dwordx4 v[90:93], v121, s[44:47], s78 offen offset:1024 sc1
buffer_load_dwordx4 v[94:97], v121, s[44:47], s78 offen offset:1536 sc1
buffer_load_dwordx4 v[128:131], v1, s[44:47], s79 offen offset:0 sc1
buffer_load_dwordx4 v[132:135], v1, s[44:47], s79 offen offset:1024 sc1
buffer_load_dwordx4 v[136:139], v1, s[44:47], s79 offen offset:2048 sc1
buffer_load_dwordx4 v[140:143], v1, s[44:47], s79 offen offset:3072 sc1
s_add_u32 s62, s79, 0x1000
buffer_load_dwordx4 v[144:147], v1, s[44:47], s62 offen offset:0 sc1
buffer_load_dwordx4 v[148:151], v1, s[44:47], s62 offen offset:1024 sc1
buffer_load_dwordx4 v[152:155], v1, s[44:47], s62 offen offset:2048 sc1
buffer_load_dwordx4 v[156:159], v1, s[44:47], s62 offen offset:3072 sc1
s_add_u32 s62, s79, 0x2000
buffer_load_dwordx4 v[160:163], v1, s[44:47], s62 offen offset:0 sc1
buffer_load_dwordx4 v[164:167], v1, s[44:47], s62 offen offset:1024 sc1
buffer_load_dwordx4 v[168:171], v1, s[44:47], s62 offen offset:2048 sc1
buffer_load_dwordx4 v[172:175], v1, s[44:47], s62 offen offset:3072 sc1
s_add_u32 s62, s79, 0x3000
buffer_load_dwordx4 v[176:179], v1, s[44:47], s62 offen offset:0 sc1
buffer_load_dwordx4 v[180:183], v1, s[44:47], s62 offen offset:1024 sc1
buffer_load_dwordx4 v[184:187], v1, s[44:47], s62 offen offset:2048 sc1
buffer_load_dwordx4 v[188:191], v1, s[44:47], s62 offen offset:3072 sc1
s_add_u32 s62, s79, 0x4000
buffer_load_dwordx4 v[192:195], v1, s[44:47], s62 offen offset:0 sc1
buffer_load_dwordx4 v[196:199], v1, s[44:47], s62 offen offset:1024 sc1
buffer_load_dwordx4 v[200:203], v1, s[44:47], s62 offen offset:2048 sc1
buffer_load_dwordx4 v[204:207], v1, s[44:47], s62 offen offset:3072 sc1
s_add_u32 s62, s79, 0x5000
buffer_load_dwordx4 v[208:211], v1, s[44:47], s62 offen offset:0 sc1
buffer_load_dwordx4 v[212:215], v1, s[44:47], s62 offen offset:1024 sc1
buffer_load_dwordx4 v[216:219], v1, s[44:47], s62 offen offset:2048 sc1
buffer_load_dwordx4 v[220:223], v1, s[44:47], s62 offen offset:3072 sc1
s_add_u32 s62, s79, 0x6000
buffer_load_dwordx4 v[224:227], v1, s[44:47], s62 offen offset:0 sc1
buffer_load_dwordx4 v[228:231], v1, s[44:47], s62 offen offset:1024 sc1
buffer_load_dwordx4 v[232:235], v1, s[44:47], s62 offen offset:2048 sc1
buffer_load_dwordx4 v[236:239], v1, s[44:47], s62 offen offset:3072 sc1
s_add_u32 s62, s79, 0x7000
buffer_load_dwordx4 v[240:243], v1, s[44:47], s62 offen offset:0 sc1
buffer_load_dwordx4 v[244:247], v1, s[44:47], s62 offen offset:1024 sc1
buffer_load_dwordx4 v[248:251], v1, s[44:47], s62 offen offset:2048 sc1
buffer_load_dwordx4 v[252:255], v1, s[44:47], s62 offen offset:3072 sc1
s_waitcnt vmcnt(35)
v_max3_u32 v120, v82, v83, v84
s_waitcnt vmcnt(34)
v_max3_u32 v120, v120, v85, v86
v_max3_u32 v120, v120, v87, v88
s_waitcnt vmcnt(33)
v_max3_u32 v120, v120, v89, v90
v_max3_u32 v120, v120, v91, v92
s_waitcnt vmcnt(32)
v_max3_u32 v120, v120, v93, v94
v_max3_u32 v120, v120, v95, v96
s_waitcnt vmcnt(31)
v_max3_u32 v120, v120, v97, v128
v_max3_u32 v120, v120, v129, v130
s_waitcnt vmcnt(30)
v_max3_u32 v120, v120, v131, v132
v_max3_u32 v120, v120, v133, v134
s_waitcnt vmcnt(29)
v_max3_u32 v120, v120, v135, v136
v_max3_u32 v120, v120, v137, v138
s_waitcnt vmcnt(28)
v_max3_u32 v120, v120, v139, v140
v_max3_u32 v120, v120, v141, v142
s_waitcnt vmcnt(27)
v_max3_u32 v120, v120, v143, v144
v_max3_u32 v120, v120, v145, v146
s_waitcnt vmcnt(26)
v_max3_u32 v120, v120, v147, v148
v_max3_u32 v120, v120, v149, v150
s_waitcnt vmcnt(25)
v_max3_u32 v120, v120, v151, v152
v_max3_u32 v120, v120, v153, v154
s_waitcnt vmcnt(24)
v_max3_u32 v120, v120, v155, v156
v_max3_u32 v120, v120, v157, v158
s_waitcnt vmcnt(23)
v_max3_u32 v120, v120, v159, v160
v_max3_u32 v120, v120, v161, v162
s_waitcnt vmcnt(22)
v_max3_u32 v120, v120, v163, v164
v_max3_u32 v120, v120, v165, v166
s_waitcnt vmcnt(21)
v_max3_u32 v120, v120, v167, v168
v_max3_u32 v120, v120, v169, v170
s_waitcnt vmcnt(20)
v_max3_u32 v120, v120, v171, v172
v_max3_u32 v120, v120, v173, v174
s_waitcnt vmcnt(19)
v_max3_u32 v120, v120, v175, v176
v_max3_u32 v120, v120, v177, v178
s_waitcnt vmcnt(18)
v_max3_u32 v120, v120, v179, v180
v_max3_u32 v120, v120, v181, v182
s_waitcnt vmcnt(17)
v_max3_u32 v120, v120, v183, v184
v_max3_u32 v120, v120, v185, v186
s_waitcnt vmcnt(16)
v_max3_u32 v120, v120, v187, v188
v_max3_u32 v120, v120, v189, v190
s_waitcnt vmcnt(15)
v_max3_u32 v120, v120, v191, v192
v_max3_u32 v120, v120, v193, v194
s_waitcnt vmcnt(14)
v_max3_u32 v120, v120, v195, v196
v_max3_u32 v120, v120, v197, v198
s_waitcnt vmcnt(13)
v_max3_u32 v120, v120, v199, v200
v_max3_u32 v120, v120, v201, v202
s_waitcnt vmcnt(12)
v_max3_u32 v120, v120, v203, v204
v_max3_u32 v120, v120, v205, v206
s_waitcnt vmcnt(11)
v_max3_u32 v120, v120, v207, v208
v_max3_u32 v120, v120, v209, v210
s_waitcnt vmcnt(10)
v_max3_u32 v120, v120, v211, v212
v_max3_u32 v120, v120, v213, v214
s_waitcnt vmcnt(9)
v_max3_u32 v120, v120, v215, v216
v_max3_u32 v120, v120, v217, v218
s_waitcnt vmcnt(8)
v_max3_u32 v120, v120, v219, v220
v_max3_u32 v120, v120, v221, v222
s_waitcnt vmcnt(7)
v_max3_u32 v120, v120, v223, v224
v_max3_u32 v120, v120, v225, v226
s_waitcnt vmcnt(6)
v_max3_u32 v120, v120, v227, v228
v_max3_u32 v120, v120, v229, v230
s_waitcnt vmcnt(5)
v_max3_u32 v120, v120, v231, v232
v_max3_u32 v120, v120, v233, v234
s_waitcnt vmcnt(4)
v_max3_u32 v120, v120, v235, v236
v_max3_u32 v120, v120, v237, v238
s_waitcnt vmcnt(3)
v_max3_u32 v120, v120, v239, v240
v_max3_u32 v120, v120, v241, v242
s_waitcnt vmcnt(2)
v_max3_u32 v120, v120, v243, v244
v_max3_u32 v120, v120, v245, v246
s_waitcnt vmcnt(1)
v_max3_u32 v120, v120, v247, v248
v_max3_u32 v120, v120, v249, v250
s_waitcnt vmcnt(0)
v_max3_u32 v120, v120, v251, v252
v_max3_u32 v120, v120, v253, v254
v_max_u32 v120, v120, v255
v_cmp_eq_u32 vcc, -1, v120
s_nop 4
s_cbranch_vccnz hslowl_69
s_waitcnt lgkmcnt(0)
s_mov_b32 s63, 1
ds_read_b128 v[50:53], v1 offset:0
ds_read_b128 v[54:57], v1 offset:32768
ds_read_b128 v[58:61], v1 offset:1024
ds_read_b128 v[62:65], v1 offset:33792
ds_read_b128 v[66:69], v1 offset:2048
ds_read_b128 v[70:73], v1 offset:34816
ds_read_b128 v[74:77], v1 offset:3072
ds_read_b128 v[78:81], v1 offset:35840
s_waitcnt lgkmcnt(4)
v_mfma_f32_32x32x16_bf16 v[2:17], v[50:53], v[128:131], 0
ds_read_b128 v[50:53], v1 offset:4096
v_mfma_f32_32x32x16_bf16 v[18:33], v[54:57], v[128:131], 0
ds_read_b128 v[54:57], v1 offset:36864
v_mfma_f32_32x32x16_bf16 v[2:17], v[58:61], v[132:135], v[2:17]
ds_read_b128 v[58:61], v1 offset:5120
v_add_f32 v82, v82, v90
v_add_f32 v83, v83, v91
v_mfma_f32_32x32x16_bf16 v[18:33], v[62:65], v[132:135], v[18:33]
ds_read_b128 v[62:65], v1 offset:37888
v_add_f32 v84, v84, v92
v_add_f32 v85, v85, v93
s_waitcnt lgkmcnt(4)
v_mfma_f32_32x32x16_bf16 v[2:17], v[66:69], v[136:139], v[2:17]
ds_read_b128 v[66:69], v1 offset:6144
v_add_f32 v86, v86, v94
v_add_f32 v87, v87, v95
v_mfma_f32_32x32x16_bf16 v[18:33], v[70:73], v[136:139], v[18:33]
ds_read_b128 v[70:73], v1 offset:38912
v_add_f32 v88, v88, v96
v_add_f32 v89, v89, v97
v_mfma_f32_32x32x16_bf16 v[2:17], v[74:77], v[140:143], v[2:17]
ds_read_b128 v[74:77], v1 offset:7168
v_add_f32 v82, v82, v86
v_add_f32 v83, v83, v87
v_mfma_f32_32x32x16_bf16 v[18:33], v[78:81], v[140:143], v[18:33]
ds_read_b128 v[78:81], v1 offset:39936
v_add_f32 v84, v84, v88
v_add_f32 v85, v85, v89
s_waitcnt lgkmcnt(4)
v_mfma_f32_32x32x16_bf16 v[2:17], v[50:53], v[144:147], v[2:17]
ds_read_b128 v[50:53], v1 offset:8192
v_add_f32 v82, v82, v84
v_add_f32 v83, v83, v85
v_mfma_f32_32x32x16_bf16 v[18:33], v[54:57], v[144:147], v[18:33]
ds_read_b128 v[54:57], v1 offset:40960
v_mov_b32 v84, v82
v_mov_b32 v85, v83
v_mfma_f32_32x32x16_bf16 v[2:17], v[58:61], v[148:151], v[2:17]
ds_read_b128 v[58:61], v1 offset:9216
v_permlane32_swap_b32 v82, v84
v_permlane32_swap_b32 v83, v85
v_mfma_f32_32x32x16_bf16 v[18:33], v[62:65], v[148:151], v[18:33]
ds_read_b128 v[62:65], v1 offset:41984
v_add_f32 v82, v82, v84
v_add_f32 v83, v83, v85
s_waitcnt lgkmcnt(4)
v_mfma_f32_32x32x16_bf16 v[2:17], v[66:69], v[152:155], v[2:17]
ds_read_b128 v[66:69], v1 offset:10240
v_mul_f32 v82, 0x3b000000, v82
v_mul_f32 v83, 0x3b000000, v83
v_mfma_f32_32x32x16_bf16 v[18:33], v[70:73], v[152:155], v[18:33]
ds_read_b128 v[70:73], v1 offset:43008
v_fma_f32 v83, -v82, v82, v83
v_max_f32 v83, 0, v83
v_mfma_f32_32x32x16_bf16 v[2:17], v[74:77], v[156:159], v[2:17]
ds_read_b128 v[74:77], v1 offset:11264
v_cvt_pk_bf16_f32 v84, v82, v83
v_lshlrev_b32 v82, 16, v84
v_mfma_f32_32x32x16_bf16 v[18:33], v[78:81], v[156:159], v[18:33]
ds_read_b128 v[78:81], v1 offset:44032
v_and_b32 v83, s82, v84
v_add_f32 v83, 0x3727c5ac, v83
s_waitcnt lgkmcnt(4)
v_mfma_f32_32x32x16_bf16 v[2:17], v[50:53], v[160:163], v[2:17]
ds_read_b128 v[50:53], v1 offset:12288
v_cvt_pk_bf16_f32 v83, v83, v83
v_lshlrev_b32 v83, 16, v83
v_mfma_f32_32x32x16_bf16 v[18:33], v[54:57], v[160:163], v[18:33]
ds_read_b128 v[54:57], v1 offset:45056
v_sqrt_f32 v83, v83
v_sub_f32 v110, 0, v82
v_mfma_f32_32x32x16_bf16 v[2:17], v[58:61], v[164:167], v[2:17]
ds_read_b128 v[58:61], v1 offset:13312
v_cvt_pk_bf16_f32 v83, v83, v83
v_lshlrev_b32 v83, 16, v83
v_mfma_f32_32x32x16_bf16 v[18:33], v[62:65], v[164:167], v[18:33]
ds_read_b128 v[62:65], v1 offset:46080
v_rcp_f32 v84, v83
v_mul_f32 v113, v83, v83
s_waitcnt lgkmcnt(4)
v_mfma_f32_32x32x16_bf16 v[2:17], v[66:69], v[168:171], v[2:17]
ds_read_b128 v[66:69], v1 offset:14336
v_mul_f32 v111, 0xc0135761, v84
v_mul_f32 v85, v84, v84
v_mfma_f32_32x32x16_bf16 v[18:33], v[70:73], v[168:171], v[18:33]
ds_read_b128 v[70:73], v1 offset:47104
v_mul_f32 v85, v85, v111
v_mul_f32 v112, 0x3d372713, v85
v_mfma_f32_32x32x16_bf16 v[2:17], v[74:77], v[172:175], v[2:17]
ds_read_b128 v[74:77], v1 offset:15360
v_cvt_pk_bf16_f32 v94, v110, v110
s_mov_b64 vcc, 0xffffffff
v_mfma_f32_32x32x16_bf16 v[18:33], v[78:81], v[172:175], v[18:33]
ds_read_b128 v[78:81], v1 offset:48128
v_cndmask_b32 v94, 0, v94, vcc
v_mov_b32 v95, v94
s_waitcnt lgkmcnt(4)
v_mfma_f32_32x32x16_bf16 v[2:17], v[50:53], v[176:179], v[2:17]
ds_read_b128 v[50:53], v1 offset:16384
v_mov_b32 v96, v94
v_mov_b32 v97, v94
v_mfma_f32_32x32x16_bf16 v[18:33], v[54:57], v[176:179], v[18:33]
ds_read_b128 v[54:57], v1 offset:49152
v_mfma_f32_32x32x16_bf16 v[2:17], v[58:61], v[180:183], v[2:17]
ds_read_b128 v[58:61], v1 offset:17408
v_mfma_f32_32x32x16_bf16 v[18:33], v[62:65], v[180:183], v[18:33]
ds_read_b128 v[62:65], v1 offset:50176
s_waitcnt lgkmcnt(4)
v_mfma_f32_32x32x16_bf16 v[2:17], v[66:69], v[184:187], v[2:17]
ds_read_b128 v[66:69], v1 offset:18432
v_mfma_f32_32x32x16_bf16 v[18:33], v[70:73], v[184:187], v[18:33]
ds_read_b128 v[70:73], v1 offset:51200
v_mfma_f32_32x32x16_bf16 v[2:17], v[74:77], v[188:191], v[2:17]
ds_read_b128 v[74:77], v1 offset:19456
v_mfma_f32_32x32x16_bf16 v[18:33], v[78:81], v[188:191], v[18:33]
ds_read_b128 v[78:81], v1 offset:52224
s_waitcnt lgkmcnt(4)
v_mfma_f32_32x32x16_bf16 v[2:17], v[50:53], v[192:195], v[2:17]
ds_read_b128 v[50:53], v1 offset:20480
v_mfma_f32_32x32x16_bf16 v[18:33], v[54:57], v[192:195], v[18:33]
ds_read_b128 v[54:57], v1 offset:53248
v_mfma_f32_32x32x16_bf16 v[2:17], v[58:61], v[196:199], v[2:17]
ds_read_b128 v[58:61], v1 offset:21504
v_mfma_f32_32x32x16_bf16 v[18:33], v[62:65], v[196:199], v[18:33]
ds_read_b128 v[62:65], v1 offset:54272
s_waitcnt lgkmcnt(4)
v_mfma_f32_32x32x16_bf16 v[2:17], v[66:69], v[200:203], v[2:17]
ds_read_b128 v[66:69], v1 offset:22528
v_mfma_f32_32x32x16_bf16 v[18:33], v[70:73], v[200:203], v[18:33]
ds_read_b128 v[70:73], v1 offset:55296
v_mfma_f32_32x32x16_bf16 v[2:17], v[74:77], v[204:207], v[2:17]
ds_read_b128 v[74:77], v1 offset:23552
v_mfma_f32_32x32x16_bf16 v[18:33], v[78:81], v[204:207], v[18:33]
ds_read_b128 v[78:81], v1 offset:56320
s_waitcnt lgkmcnt(4)
v_mfma_f32_32x32x16_bf16 v[2:17], v[50:53], v[208:211], v[2:17]
ds_read_b128 v[50:53], v1 offset:24576
v_mfma_f32_32x32x16_bf16 v[18:33], v[54:57], v[208:211], v[18:33]
ds_read_b128 v[54:57], v1 offset:57344
v_mfma_f32_32x32x16_bf16 v[2:17], v[58:61], v[212:215], v[2:17]
ds_read_b128 v[58:61], v1 offset:25600
v_mfma_f32_32x32x16_bf16 v[18:33], v[62:65], v[212:215], v[18:33]
ds_read_b128 v[62:65], v1 offset:58368
s_waitcnt lgkmcnt(4)
v_mfma_f32_32x32x16_bf16 v[2:17], v[66:69], v[216:219], v[2:17]
ds_read_b128 v[66:69], v1 offset:26624
v_mfma_f32_32x32x16_bf16 v[18:33], v[70:73], v[216:219], v[18:33]
ds_read_b128 v[70:73], v1 offset:59392
v_mfma_f32_32x32x16_bf16 v[2:17], v[74:77], v[220:223], v[2:17]
ds_read_b128 v[74:77], v1 offset:27648
v_mfma_f32_32x32x16_bf16 v[18:33], v[78:81], v[220:223], v[18:33]
ds_read_b128 v[78:81], v1 offset:60416
s_waitcnt lgkmcnt(4)
v_mfma_f32_32x32x16_bf16 v[2:17], v[50:53], v[224:227], v[2:17]
ds_read_b128 v[50:53], v1 offset:28672
v_mfma_f32_32x32x16_bf16 v[18:33], v[54:57], v[224:227], v[18:33]
ds_read_b128 v[54:57], v1 offset:61440
v_mfma_f32_32x32x16_bf16 v[2:17], v[58:61], v[228:231], v[2:17]
ds_read_b128 v[58:61], v1 offset:29696
v_mfma_f32_32x32x16_bf16 v[18:33], v[62:65], v[228:231], v[18:33]
ds_read_b128 v[62:65], v1 offset:62464
s_waitcnt lgkmcnt(4)
v_mfma_f32_32x32x16_bf16 v[2:17], v[66:69], v[232:235], v[2:17]
ds_read_b128 v[66:69], v1 offset:30720
v_mfma_f32_32x32x16_bf16 v[18:33], v[70:73], v[232:235], v[18:33]
ds_read_b128 v[70:73], v1 offset:63488
v_mfma_f32_32x32x16_bf16 v[2:17], v[74:77], v[236:239], v[2:17]
ds_read_b128 v[74:77], v1 offset:31744
v_mfma_f32_32x32x16_bf16 v[18:33], v[78:81], v[236:239], v[18:33]
ds_read_b128 v[78:81], v1 offset:64512
s_waitcnt lgkmcnt(4)
v_mfma_f32_32x32x16_bf16 v[2:17], v[50:53], v[240:243], v[2:17]
ds_read_b128 v[50:53], v115 offset:0
v_mfma_f32_32x32x16_bf16 v[18:33], v[54:57], v[240:243], v[18:33]
ds_read_b128 v[54:57], v115 offset:512
v_mfma_f32_32x32x16_bf16 v[2:17], v[58:61], v[244:247], v[2:17]
ds_read_b128 v[58:61], v115 offset:1024
v_mfma_f32_32x32x16_bf16 v[18:33], v[62:65], v[244:247], v[18:33]
ds_read_b128 v[62:65], v114 offset:0
s_waitcnt lgkmcnt(4)
v_mfma_f32_32x32x16_bf16 v[2:17], v[66:69], v[248:251], v[2:17]
ds_read_b128 v[66:69], v114 offset:1024
v_mfma_f32_32x32x16_bf16 v[18:33], v[70:73], v[248:251], v[18:33]
ds_read_b128 v[70:73], v114 offset:2048
v_mfma_f32_32x32x16_bf16 v[2:17], v[74:77], v[252:255], v[2:17]
ds_read_b128 v[74:77], v114 offset:3072
v_mfma_f32_32x32x16_bf16 v[18:33], v[78:81], v[252:255], v[18:33]
ds_read_b128 v[78:81], v114 offset:4096
s_waitcnt lgkmcnt(4)
v_mfma_f32_32x32x16_bf16 v[2:17], v[50:53], v[94:97], v[2:17]
ds_read_b128 v[50:53], v114 offset:5120
v_mfma_f32_32x32x16_bf16 v[18:33], v[54:57], v[94:97], v[18:33]
ds_read_b128 v[54:57], v114 offset:6144
s_branch hgo_36
aretry_40:
s_mov_b32 s68, 0
aretryl_70:
s_add_u32 s68, s68, 1
s_cmp_lt_u32 s68, 0x30d40
s_cbranch_scc0 L_abort
s_sleep 1
buffer_load_dwordx4 v[128:131], v1, s[44:47], s60 offen offset:1024 sc1
buffer_load_dwordx4 v[132:135], v1, s[44:47], s61 offen offset:1024 sc1
buffer_load_dwordx4 v[136:139], v1, s[44:47], s86 offen offset:0 sc1
buffer_load_dwordx4 v[140:143], v1, s[44:47], s91 offen offset:0 sc1
buffer_load_dwordx4 v[144:147], v1, s[44:47], s92 offen offset:3072 sc1
buffer_load_dwordx4 v[148:151], v1, s[44:47], s93 offen offset:3072 sc1
buffer_load_dwordx4 v[152:155], v1, s[44:47], s96 offen offset:2048 sc1
buffer_load_dwordx4 v[156:159], v1, s[44:47], s97 offen offset:2048 sc1
s_waitcnt vmcnt(7)
v_max3_u32 v120, v128, v129, v130
s_waitcnt vmcnt(6)
v_max3_u32 v120, v120, v131, v132
v_max3_u32 v120, v120, v133, v134
s_waitcnt vmcnt(5)
v_max3_u32 v120, v120, v135, v136
v_max3_u32 v120, v120, v137, v138
s_waitcnt vmcnt(4)
v_max3_u32 v120, v120, v139, v140
v_max3_u32 v120, v120, v141, v142
s_waitcnt vmcnt(3)
v_max3_u32 v120, v120, v143, v144
v_max3_u32 v120, v120, v145, v146
s_waitcnt vmcnt(2)
v_max3_u32 v120, v120, v147, v148
v_max3_u32 v120, v120, v149, v150
s_waitcnt vmcnt(1)
v_max3_u32 v120, v120, v151, v152
v_max3_u32 v120, v120, v153, v154
s_waitcnt vmcnt(0)
v_max3_u32 v120, v120, v155, v156
v_max3_u32 v120, v120, v157, v158
v_max_u32 v120, v120, v159
v_cmp_eq_u32 vcc, -1, v120
s_nop 4
s_cbranch_vccnz aretryl_70
s_branch aretryok_39
aretry_42:
s_mov_b32 s68, 0
aretryl_71:
s_add_u32 s68, s68, 1
s_cmp_lt_u32 s68, 0x30d40
s_cbranch_scc0 L_abort
s_sleep 1
buffer_load_dwordx4 v[160:163], v1, s[44:47], s60 offen offset:2048 sc1
buffer_load_dwordx4 v[164:167], v1, s[44:47], s61 offen offset:2048 sc1
buffer_load_dwordx4 v[168:171], v1, s[44:47], s86 offen offset:1024 sc1
buffer_load_dwordx4 v[172:175], v1, s[44:47], s91 offen offset:1024 sc1
buffer_load_dwordx4 v[176:179], v1, s[44:47], s94 offen offset:0 sc1
buffer_load_dwordx4 v[180:183], v1, s[44:47], s95 offen offset:0 sc1
buffer_load_dwordx4 v[184:187], v1, s[44:47], s96 offen offset:3072 sc1
buffer_load_dwordx4 v[188:191], v1, s[44:47], s97 offen offset:3072 sc1
s_waitcnt vmcnt(7)
v_max3_u32 v120, v160, v161, v162
s_waitcnt vmcnt(6)
v_max3_u32 v120, v120, v163, v164
v_max3_u32 v120, v120, v165, v166
s_waitcnt vmcnt(5)
v_max3_u32 v120, v120, v167, v168
v_max3_u32 v120, v120, v169, v170
s_waitcnt vmcnt(4)
v_max3_u32 v120, v120, v171, v172
v_max3_u32 v120, v120, v173, v174
s_waitcnt vmcnt(3)
v_max3_u32 v120, v120, v175, v176
v_max3_u32 v120, v120, v177, v178
s_waitcnt vmcnt(2)
v_max3_u32 v120, v120, v179, v180
v_max3_u32 v120, v120, v181, v182
s_waitcnt vmcnt(1)
v_max3_u32 v120, v120, v183, v184
v_max3_u32 v120, v120, v185, v186
s_waitcnt vmcnt(0)
v_max3_u32 v120, v120, v187, v188
v_max3_u32 v120, v120, v189, v190
v_max_u32 v120, v120, v191
v_cmp_eq_u32 vcc, -1, v120
s_nop 4
s_cbranch_vccnz aretryl_71
s_branch aretryok_41
aretry_44:
s_mov_b32 s68, 0
aretryl_72:
s_add_u32 s68, s68, 1
s_cmp_lt_u32 s68, 0x30d40
s_cbranch_scc0 L_abort
s_sleep 1
buffer_load_dwordx4 v[192:195], v1, s[44:47], s60 offen offset:3072 sc1
buffer_load_dwordx4 v[196:199], v1, s[44:47], s61 offen offset:3072 sc1
buffer_load_dwordx4 v[200:203], v1, s[44:47], s86 offen offset:2048 sc1
buffer_load_dwordx4 v[204:207], v1, s[44:47], s91 offen offset:2048 sc1
buffer_load_dwordx4 v[208:211], v1, s[44:47], s94 offen offset:1024 sc1
buffer_load_dwordx4 v[212:215], v1, s[44:47], s95 offen offset:1024 sc1
buffer_load_dwordx4 v[216:219], v1, s[44:47], s98 offen offset:0 sc1
buffer_load_dwordx4 v[220:223], v1, s[44:47], s99 offen offset:0 sc1
s_waitcnt vmcnt(7)
v_max3_u32 v120, v192, v193, v194
s_waitcnt vmcnt(6)
v_max3_u32 v120, v120, v195, v196
v_max3_u32 v120, v120, v197, v198
s_waitcnt vmcnt(5)
v_max3_u32 v120, v120, v199, v200
v_max3_u32 v120, v120, v201, v202
s_waitcnt vmcnt(4)
v_max3_u32 v120, v120, v203, v204
v_max3_u32 v120, v120, v205, v206
s_waitcnt vmcnt(3)
v_max3_u32 v120, v120, v207, v208
v_max3_u32 v120, v120, v209, v210
s_waitcnt vmcnt(2)
v_max3_u32 v120, v120, v211, v212
v_max3_u32 v120, v120, v213, v214
s_waitcnt vmcnt(1)
v_max3_u32 v120, v120, v215, v216
v_max3_u32 v120, v120, v217, v218
s_waitcnt vmcnt(0)
v_max3_u32 v120, v120, v219, v220
v_max3_u32 v120, v120, v221, v222
v_max_u32 v120, v120, v223
v_cmp_eq_u32 vcc, -1, v120
s_nop 4
s_cbranch_vccnz aretryl_72
s_branch aretryok_43
aretry_46:
s_mov_b32 s68, 0
aretryl_73:
s_add_u32 s68, s68, 1
s_cmp_lt_u32 s68, 0x30d40
s_cbranch_scc0 L_abort
s_sleep 1
buffer_load_dwordx4 v[82:85], v1, s[44:47], s84 offen offset:0 sc1
buffer_load_dwordx4 v[86:89], v1, s[44:47], s85 offen offset:0 sc1
buffer_load_dwordx4 v[90:93], v1, s[44:47], s86 offen offset:3072 sc1
buffer_load_dwordx4 v[94:97], v1, s[44:47], s91 offen offset:3072 sc1
buffer_load_dwordx4 v[98:101], v1, s[44:47], s94 offen offset:2048 sc1
buffer_load_dwordx4 v[102:105], v1, s[44:47], s95 offen offset:2048 sc1
buffer_load_dwordx4 v[106:109], v1, s[44:47], s98 offen offset:1024 sc1
buffer_load_dwordx4 v[110:113], v1, s[44:47], s99 offen offset:1024 sc1
s_waitcnt vmcnt(7)
v_max3_u32 v120, v82, v83, v84
s_waitcnt vmcnt(6)
v_max3_u32 v120, v120, v85, v86
v_max3_u32 v120, v120, v87, v88
s_waitcnt vmcnt(5)
v_max3_u32 v120, v120, v89, v90
v_max3_u32 v120, v120, v91, v92
s_waitcnt vmcnt(4)
v_max3_u32 v120, v120, v93, v94
v_max3_u32 v120, v120, v95, v96
s_waitcnt vmcnt(3)
v_max3_u32 v120, v120, v97, v98
v_max3_u32 v120, v120, v99, v100
s_waitcnt vmcnt(2)
v_max3_u32 v120, v120, v101, v102
v_max3_u32 v120, v120, v103, v104
s_waitcnt vmcnt(1)
v_max3_u32 v120, v120, v105, v106
v_max3_u32 v120, v120, v107, v108
s_waitcnt vmcnt(0)
v_max3_u32 v120, v120, v109, v110
v_max3_u32 v120, v120, v111, v112
v_max_u32 v120, v120, v113
v_cmp_eq_u32 vcc, -1, v120
s_nop 4
s_cbranch_vccnz aretryl_73
s_branch aretryok_45
aretry_48:
s_mov_b32 s68, 0
aretryl_74:
s_add_u32 s68, s68, 1
s_cmp_lt_u32 s68, 0x30d40
s_cbranch_scc0 L_abort
s_sleep 1
buffer_load_dwordx4 v[128:131], v1, s[44:47], s84 offen offset:1024 sc1
buffer_load_dwordx4 v[132:135], v1, s[44:47], s85 offen offset:1024 sc1
buffer_load_dwordx4 v[136:139], v1, s[44:47], s92 offen offset:0 sc1
buffer_load_dwordx4 v[140:143], v1, s[44:47], s93 offen offset:0 sc1
buffer_load_dwordx4 v[144:147], v1, s[44:47], s94 offen offset:3072 sc1
buffer_load_dwordx4 v[148:151], v1, s[44:47], s95 offen offset:3072 sc1
buffer_load_dwordx4 v[152:155], v1, s[44:47], s98 offen offset:2048 sc1
buffer_load_dwordx4 v[156:159], v1, s[44:47], s99 offen offset:2048 sc1
s_waitcnt vmcnt(7)
v_max3_u32 v120, v128, v129, v130
s_waitcnt vmcnt(6)
v_max3_u32 v120, v120, v131, v132
v_max3_u32 v120, v120, v133, v134
s_waitcnt vmcnt(5)
v_max3_u32 v120, v120, v135, v136
v_max3_u32 v120, v120, v137, v138
s_waitcnt vmcnt(4)
v_max3_u32 v120, v120, v139, v140
v_max3_u32 v120, v120, v141, v142
s_waitcnt vmcnt(3)
v_max3_u32 v120, v120, v143, v144
v_max3_u32 v120, v120, v145, v146
s_waitcnt vmcnt(2)
v_max3_u32 v120, v120, v147, v148
v_max3_u32 v120, v120, v149, v150
s_waitcnt vmcnt(1)
v_max3_u32 v120, v120, v151, v152
v_max3_u32 v120, v120, v153, v154
s_waitcnt vmcnt(0)
v_max3_u32 v120, v120, v155, v156
v_max3_u32 v120, v120, v157, v158
v_max_u32 v120, v120, v159
v_cmp_eq_u32 vcc, -1, v120
s_nop 4
s_cbranch_vccnz aretryl_74
s_branch aretryok_47
rretry_50:
s_mov_b32 s68, 0
rretryl_75:
s_add_u32 s68, s68, 1
s_cmp_lt_u32 s68, 0x30d40
s_cbranch_scc0 L_abort
s_sleep 1
s_and_b32 s54, s49, 1
s_lshl_b32 s55, s74, 1
s_add_u32 s54, s54, s55
s_lshr_b32 s55, s49, 1
s_lshl_b32 s56, s75, 1
s_add_u32 s56, s56, s55
s_lshl_b32 s57, s54, 15
s_lshl_b32 s58, s56, 11
s_add_u32 s57, s57, s58
s_add_u32 s57, s57, 0x400000
buffer_load_dwordx4 v[2:5], v1, s[44:47], s57 offen sc1
buffer_load_dwordx4 v[6:9], v1, s[44:47], s57 offen offset:1024 sc1
s_lshl_b32 s78, s54, 15
s_lshl_b32 s58, s56, 6
s_add_u32 s78, s78, s58
s_waitcnt vmcnt(1)
v_max3_u32 v40, v2, v3, v4
s_waitcnt vmcnt(0)
v_max3_u32 v40, v40, v5, v6
v_max3_u32 v40, v40, v7, v8
v_max_u32 v40, v40, v9
v_cmp_eq_u32 vcc, -1, v40
s_nop 4
s_cbranch_vccnz rretryl_75
s_branch rretryok_49
aretry_52:
s_mov_b32 s68, 0
aretryl_76:
s_add_u32 s68, s68, 1
s_cmp_lt_u32 s68, 0x30d40
s_cbranch_scc0 L_abort
s_sleep 1
buffer_load_dwordx4 v[160:163], v1, s[44:47], s84 offen offset:2048 sc1
buffer_load_dwordx4 v[164:167], v1, s[44:47], s85 offen offset:2048 sc1
buffer_load_dwordx4 v[168:171], v1, s[44:47], s92 offen offset:1024 sc1
buffer_load_dwordx4 v[172:175], v1, s[44:47], s93 offen offset:1024 sc1
buffer_load_dwordx4 v[176:179], v1, s[44:47], s96 offen offset:0 sc1
buffer_load_dwordx4 v[180:183], v1, s[44:47], s97 offen offset:0 sc1
buffer_load_dwordx4 v[184:187], v1, s[44:47], s98 offen offset:3072 sc1
buffer_load_dwordx4 v[188:191], v1, s[44:47], s99 offen offset:3072 sc1
s_waitcnt vmcnt(7)
v_max3_u32 v120, v160, v161, v162
s_waitcnt vmcnt(6)
v_max3_u32 v120, v120, v163, v164
v_max3_u32 v120, v120, v165, v166
s_waitcnt vmcnt(5)
v_max3_u32 v120, v120, v167, v168
v_max3_u32 v120, v120, v169, v170
s_waitcnt vmcnt(4)
v_max3_u32 v120, v120, v171, v172
v_max3_u32 v120, v120, v173, v174
s_waitcnt vmcnt(3)
v_max3_u32 v120, v120, v175, v176
v_max3_u32 v120, v120, v177, v178
s_waitcnt vmcnt(2)
v_max3_u32 v120, v120, v179, v180
v_max3_u32 v120, v120, v181, v182
s_waitcnt vmcnt(1)
v_max3_u32 v120, v120, v183, v184
v_max3_u32 v120, v120, v185, v186
s_waitcnt vmcnt(0)
v_max3_u32 v120, v120, v187, v188
v_max3_u32 v120, v120, v189, v190
v_max_u32 v120, v120, v191
v_cmp_eq_u32 vcc, -1, v120
s_nop 4
s_cbranch_vccnz aretryl_76
s_branch aretryok_51
dslow_37:
s_mov_b32 s68, 0
dslowl_77:
s_add_u32 s68, s68, 1
s_cmp_lt_u32 s68, 0x30d40
s_cbranch_scc0 L_abort
buffer_load_dwordx4 v[128:131], v1, s[44:47], s60 offen offset:0 sc1
buffer_load_dwordx4 v[132:135], v1, s[44:47], s61 offen offset:0 sc1
buffer_load_dwordx4 v[136:139], v1, s[44:47], s84 offen offset:3072 sc1
buffer_load_dwordx4 v[140:143], v1, s[44:47], s85 offen offset:3072 sc1
buffer_load_dwordx4 v[144:147], v1, s[44:47], s92 offen offset:2048 sc1
buffer_load_dwordx4 v[148:151], v1, s[44:47], s93 offen offset:2048 sc1
buffer_load_dwordx4 v[152:155], v1, s[44:47], s96 offen offset:1024 sc1
buffer_load_dwordx4 v[156:159], v1, s[44:47], s97 offen offset:1024 sc1
s_waitcnt vmcnt(7)
v_max3_u32 v40, v128, v129, v130
s_waitcnt vmcnt(6)
v_max3_u32 v40, v40, v131, v132
v_max3_u32 v40, v40, v133, v134
s_waitcnt vmcnt(5)
v_max3_u32 v40, v40, v135, v136
v_max3_u32 v40, v40, v137, v138
s_waitcnt vmcnt(4)
v_max3_u32 v40, v40, v139, v140
v_max3_u32 v40, v40, v141, v142
s_waitcnt vmcnt(3)
v_max3_u32 v40, v40, v143, v144
v_max3_u32 v40, v40, v145, v146
s_waitcnt vmcnt(2)
v_max3_u32 v40, v40, v147, v148
v_max3_u32 v40, v40, v149, v150
s_waitcnt vmcnt(1)
v_max3_u32 v40, v40, v151, v152
v_max3_u32 v40, v40, v153, v154
s_waitcnt vmcnt(0)
v_max3_u32 v40, v40, v155, v156
v_max3_u32 v40, v40, v157, v158
v_max_u32 v40, v40, v159
v_cmp_eq_u32 vcc, -1, v40
s_nop 4
s_cbranch_vccnz dslowl_77
v_mfma_f32_32x32x16_bf16 v[224:239], a[0:3], v[128:131], 0
v_mfma_f32_32x32x16_bf16 v[50:65], a[4:7], v[128:131], 0
v_mfma_f32_32x32x16_bf16 v[240:255], a[0:3], v[132:135], 0
v_mfma_f32_32x32x16_bf16 v[66:81], a[4:7], v[132:135], 0
v_mfma_f32_32x32x16_bf16 v[224:239], a[56:59], v[136:139], v[224:239]
v_mfma_f32_32x32x16_bf16 v[50:65], a[60:63], v[136:139], v[50:65]
v_mfma_f32_32x32x16_bf16 v[240:255], a[56:59], v[140:143], v[240:255]
v_mfma_f32_32x32x16_bf16 v[66:81], a[60:63], v[140:143], v[66:81]
v_mfma_f32_32x32x16_bf16 v[224:239], a[112:115], v[144:147], v[224:239]
v_mfma_f32_32x32x16_bf16 v[50:65], a[116:119], v[144:147], v[50:65]
v_mfma_f32_32x32x16_bf16 v[240:255], a[112:115], v[148:151], v[240:255]
v_mfma_f32_32x32x16_bf16 v[66:81], a[116:119], v[148:151], v[66:81]
v_mfma_f32_32x32x16_bf16 v[224:239], a[168:171], v[152:155], v[224:239]
v_mfma_f32_32x32x16_bf16 v[50:65], a[172:175], v[152:155], v[50:65]
v_mfma_f32_32x32x16_bf16 v[240:255], a[168:171], v[156:159], v[240:255]
v_mfma_f32_32x32x16_bf16 v[66:81], a[172:175], v[156:159], v[66:81]
s_mov_b32 s68, 0
dslowl_78:
s_add_u32 s68, s68, 1
s_cmp_lt_u32 s68, 0x30d40
s_cbranch_scc0 L_abort
buffer_load_dwordx4 v[128:131], v1, s[44:47], s60 offen offset:1024 sc1
buffer_load_dwordx4 v[132:135], v1, s[44:47], s61 offen offset:1024 sc1
buffer_load_dwordx4 v[136:139], v1, s[44:47], s86 offen offset:0 sc1
buffer_load_dwordx4 v[140:143], v1, s[44:47], s91 offen offset:0 sc1
buffer_load_dwordx4 v[144:147], v1, s[44:47], s92 offen offset:3072 sc1
buffer_load_dwordx4 v[148:151], v1, s[44:47], s93 offen offset:3072 sc1
buffer_load_dwordx4 v[152:155], v1, s[44:47], s96 offen offset:2048 sc1
buffer_load_dwordx4 v[156:159], v1, s[44:47], s97 offen offset:2048 sc1
s_waitcnt vmcnt(7)
v_max3_u32 v40, v128, v129, v130
s_waitcnt vmcnt(6)
v_max3_u32 v40, v40, v131, v132
v_max3_u32 v40, v40, v133, v134
s_waitcnt vmcnt(5)
v_max3_u32 v40, v40, v135, v136
v_max3_u32 v40, v40, v137, v138
s_waitcnt vmcnt(4)
v_max3_u32 v40, v40, v139, v140
v_max3_u32 v40, v40, v141, v142
s_waitcnt vmcnt(3)
v_max3_u32 v40, v40, v143, v144
v_max3_u32 v40, v40, v145, v146
s_waitcnt vmcnt(2)
v_max3_u32 v40, v40, v147, v148
v_max3_u32 v40, v40, v149, v150
s_waitcnt vmcnt(1)
v_max3_u32 v40, v40, v151, v152
v_max3_u32 v40, v40, v153, v154
s_waitcnt vmcnt(0)
v_max3_u32 v40, v40, v155, v156
v_max3_u32 v40, v40, v157, v158
v_max_u32 v40, v40, v159
v_cmp_eq_u32 vcc, -1, v40
s_nop 4
s_cbranch_vccnz dslowl_78
v_mfma_f32_32x32x16_bf16 v[224:239], a[8:11], v[128:131], v[224:239]
v_mfma_f32_32x32x16_bf16 v[50:65], a[12:15], v[128:131], v[50:65]
v_mfma_f32_32x32x16_bf16 v[240:255], a[8:11], v[132:135], v[240:255]
v_mfma_f32_32x32x16_bf16 v[66:81], a[12:15], v[132:135], v[66:81]
v_mfma_f32_32x32x16_bf16 v[224:239], a[64:67], v[136:139], v[224:239]
v_mfma_f32_32x32x16_bf16 v[50:65], a[68:71], v[136:139], v[50:65]
v_mfma_f32_32x32x16_bf16 v[240:255], a[64:67], v[140:143], v[240:255]
v_mfma_f32_32x32x16_bf16 v[66:81], a[68:71], v[140:143], v[66:81]
v_mfma_f32_32x32x16_bf16 v[224:239], a[120:123], v[144:147], v[224:239]
v_mfma_f32_32x32x16_bf16 v[50:65], a[124:127], v[144:147], v[50:65]
v_mfma_f32_32x32x16_bf16 v[240:255], a[120:123], v[148:151], v[240:255]
v_mfma_f32_32x32x16_bf16 v[66:81], a[124:127], v[148:151], v[66:81]
v_mfma_f32_32x32x16_bf16 v[224:239], a[176:179], v[152:155], v[224:239]
v_mfma_f32_32x32x16_bf16 v[50:65], a[180:183], v[152:155], v[50:65]
v_mfma_f32_32x32x16_bf16 v[240:255], a[176:179], v[156:159], v[240:255]
v_mfma_f32_32x32x16_bf16 v[66:81], a[180:183], v[156:159], v[66:81]
s_mov_b32 s68, 0
dslowl_79:
s_add_u32 s68, s68, 1
s_cmp_lt_u32 s68, 0x30d40
s_cbranch_scc0 L_abort
buffer_load_dwordx4 v[128:131], v1, s[44:47], s60 offen offset:2048 sc1
buffer_load_dwordx4 v[132:135], v1, s[44:47], s61 offen offset:2048 sc1
buffer_load_dwordx4 v[136:139], v1, s[44:47], s86 offen offset:1024 sc1
buffer_load_dwordx4 v[140:143], v1, s[44:47], s91 offen offset:1024 sc1
buffer_load_dwordx4 v[144:147], v1, s[44:47], s94 offen offset:0 sc1
buffer_load_dwordx4 v[148:151], v1, s[44:47], s95 offen offset:0 sc1
buffer_load_dwordx4 v[152:155], v1, s[44:47], s96 offen offset:3072 sc1
buffer_load_dwordx4 v[156:159], v1, s[44:47], s97 offen offset:3072 sc1
s_waitcnt vmcnt(7)
v_max3_u32 v40, v128, v129, v130
s_waitcnt vmcnt(6)
v_max3_u32 v40, v40, v131, v132
v_max3_u32 v40, v40, v133, v134
s_waitcnt vmcnt(5)
v_max3_u32 v40, v40, v135, v136
v_max3_u32 v40, v40, v137, v138
s_waitcnt vmcnt(4)
v_max3_u32 v40, v40, v139, v140
v_max3_u32 v40, v40, v141, v142
s_waitcnt vmcnt(3)
v_max3_u32 v40, v40, v143, v144
v_max3_u32 v40, v40, v145, v146
s_waitcnt vmcnt(2)
v_max3_u32 v40, v40, v147, v148
v_max3_u32 v40, v40, v149, v150
s_waitcnt vmcnt(1)
v_max3_u32 v40, v40, v151, v152
v_max3_u32 v40, v40, v153, v154
s_waitcnt vmcnt(0)
v_max3_u32 v40, v40, v155, v156
v_max3_u32 v40, v40, v157, v158
v_max_u32 v40, v40, v159
v_cmp_eq_u32 vcc, -1, v40
s_nop 4
s_cbranch_vccnz dslowl_79
v_mfma_f32_32x32x16_bf16 v[224:239], a[16:19], v[128:131], v[224:239]
v_mfma_f32_32x32x16_bf16 v[50:65], a[20:23], v[128:131], v[50:65]
v_mfma_f32_32x32x16_bf16 v[240:255], a[16:19], v[132:135], v[240:255]
v_mfma_f32_32x32x16_bf16 v[66:81], a[20:23], v[132:135], v[66:81]
v_mfma_f32_32x32x16_bf16 v[224:239], a[72:75], v[136:139], v[224:239]
v_mfma_f32_32x32x16_bf16 v[50:65], a[76:79], v[136:139], v[50:65]
v_mfma_f32_32x32x16_bf16 v[240:255], a[72:75], v[140:143], v[240:255]
v_mfma_f32_32x32x16_bf16 v[66:81], a[76:79], v[140:143], v[66:81]
v_mfma_f32_32x32x16_bf16 v[224:239], a[128:131], v[144:147], v[224:239]
v_mfma_f32_32x32x16_bf16 v[50:65], a[132:135], v[144:147], v[50:65]
v_mfma_f32_32x32x16_bf16 v[240:255], a[128:131], v[148:151], v[240:255]
v_mfma_f32_32x32x16_bf16 v[66:81], a[132:135], v[148:151], v[66:81]
v_mfma_f32_32x32x16_bf16 v[224:239], a[184:187], v[152:155], v[224:239]
v_mfma_f32_32x32x16_bf16 v[50:65], a[188:191], v[152:155], v[50:65]
v_mfma_f32_32x32x16_bf16 v[240:255], a[184:187], v[156:159], v[240:255]
v_mfma_f32_32x32x16_bf16 v[66:81], a[188:191], v[156:159], v[66:81]
s_mov_b32 s68, 0
dslowl_80:
s_add_u32 s68, s68, 1
s_cmp_lt_u32 s68, 0x30d40
s_cbranch_scc0 L_abort
buffer_load_dwordx4 v[128:131], v1, s[44:47], s60 offen offset:3072 sc1
buffer_load_dwordx4 v[132:135], v1, s[44:47], s61 offen offset:3072 sc1
buffer_load_dwordx4 v[136:139], v1, s[44:47], s86 offen offset:2048 sc1
buffer_load_dwordx4 v[140:143], v1, s[44:47], s91 offen offset:2048 sc1
buffer_load_dwordx4 v[144:147], v1, s[44:47], s94 offen offset:1024 sc1
buffer_load_dwordx4 v[148:151], v1, s[44:47], s95 offen offset:1024 sc1
buffer_load_dwordx4 v[152:155], v1, s[44:47], s98 offen offset:0 sc1
buffer_load_dwordx4 v[156:159], v1, s[44:47], s99 offen offset:0 sc1
s_waitcnt vmcnt(7)
v_max3_u32 v40, v128, v129, v130
s_waitcnt vmcnt(6)
v_max3_u32 v40, v40, v131, v132
v_max3_u32 v40, v40, v133, v134
s_waitcnt vmcnt(5)
v_max3_u32 v40, v40, v135, v136
v_max3_u32 v40, v40, v137, v138
s_waitcnt vmcnt(4)
v_max3_u32 v40, v40, v139, v140
v_max3_u32 v40, v40, v141, v142
s_waitcnt vmcnt(3)
v_max3_u32 v40, v40, v143, v144
v_max3_u32 v40, v40, v145, v146
s_waitcnt vmcnt(2)
v_max3_u32 v40, v40, v147, v148
v_max3_u32 v40, v40, v149, v150
s_waitcnt vmcnt(1)
v_max3_u32 v40, v40, v151, v152
v_max3_u32 v40, v40, v153, v154
s_waitcnt vmcnt(0)
v_max3_u32 v40, v40, v155, v156
v_max3_u32 v40, v40, v157, v158
v_max_u32 v40, v40, v159
v_cmp_eq_u32 vcc, -1, v40
s_nop 4
s_cbranch_vccnz dslowl_80
v_mfma_f32_32x32x16_bf16 v[224:239], a[24:27], v[128:131], v[224:239]
v_mfma_f32_32x32x16_bf16 v[50:65], a[28:31], v[128:131], v[50:65]
v_mfma_f32_32x32x16_bf16 v[240:255], a[24:27], v[132:135], v[240:255]
v_mfma_f32_32x32x16_bf16 v[66:81], a[28:31], v[132:135], v[66:81]
v_mfma_f32_32x32x16_bf16 v[224:239], a[80:83], v[136:139], v[224:239]
v_mfma_f32_32x32x16_bf16 v[50:65], a[84:87], v[136:139], v[50:65]
v_mfma_f32_32x32x16_bf16 v[240:255], a[80:83], v[140:143], v[240:255]
v_mfma_f32_32x32x16_bf16 v[66:81], a[84:87], v[140:143], v[66:81]
v_mfma_f32_32x32x16_bf16 v[224:239], a[136:139], v[144:147], v[224:239]
v_mfma_f32_32x32x16_bf16 v[50:65], a[140:143], v[144:147], v[50:65]
v_mfma_f32_32x32x16_bf16 v[240:255], a[136:139], v[148:151], v[240:255]
v_mfma_f32_32x32x16_bf16 v[66:81], a[140:143], v[148:151], v[66:81]
v_mfma_f32_32x32x16_bf16 v[224:239], a[192:195], v[152:155], v[224:239]
v_mfma_f32_32x32x16_bf16 v[50:65], a[196:199], v[152:155], v[50:65]
v_mfma_f32_32x32x16_bf16 v[240:255], a[192:195], v[156:159], v[240:255]
v_mfma_f32_32x32x16_bf16 v[66:81], a[196:199], v[156:159], v[66:81]
s_mov_b32 s68, 0
dslowl_81:
s_add_u32 s68, s68, 1
s_cmp_lt_u32 s68, 0x30d40
s_cbranch_scc0 L_abort
buffer_load_dwordx4 v[128:131], v1, s[44:47], s84 offen offset:0 sc1
buffer_load_dwordx4 v[132:135], v1, s[44:47], s85 offen offset:0 sc1
buffer_load_dwordx4 v[136:139], v1, s[44:47], s86 offen offset:3072 sc1
buffer_load_dwordx4 v[140:143], v1, s[44:47], s91 offen offset:3072 sc1
buffer_load_dwordx4 v[144:147], v1, s[44:47], s94 offen offset:2048 sc1
buffer_load_dwordx4 v[148:151], v1, s[44:47], s95 offen offset:2048 sc1
buffer_load_dwordx4 v[152:155], v1, s[44:47], s98 offen offset:1024 sc1
buffer_load_dwordx4 v[156:159], v1, s[44:47], s99 offen offset:1024 sc1
s_waitcnt vmcnt(7)
v_max3_u32 v40, v128, v129, v130
s_waitcnt vmcnt(6)
v_max3_u32 v40, v40, v131, v132
v_max3_u32 v40, v40, v133, v134
s_waitcnt vmcnt(5)
v_max3_u32 v40, v40, v135, v136
v_max3_u32 v40, v40, v137, v138
s_waitcnt vmcnt(4)
v_max3_u32 v40, v40, v139, v140
v_max3_u32 v40, v40, v141, v142
s_waitcnt vmcnt(3)
v_max3_u32 v40, v40, v143, v144
v_max3_u32 v40, v40, v145, v146
s_waitcnt vmcnt(2)
v_max3_u32 v40, v40, v147, v148
v_max3_u32 v40, v40, v149, v150
s_waitcnt vmcnt(1)
v_max3_u32 v40, v40, v151, v152
v_max3_u32 v40, v40, v153, v154
s_waitcnt vmcnt(0)
v_max3_u32 v40, v40, v155, v156
v_max3_u32 v40, v40, v157, v158
v_max_u32 v40, v40, v159
v_cmp_eq_u32 vcc, -1, v40
s_nop 4
s_cbranch_vccnz dslowl_81
v_mfma_f32_32x32x16_bf16 v[224:239], a[32:35], v[128:131], v[224:239]
v_mfma_f32_32x32x16_bf16 v[50:65], a[36:39], v[128:131], v[50:65]
v_mfma_f32_32x32x16_bf16 v[240:255], a[32:35], v[132:135], v[240:255]
v_mfma_f32_32x32x16_bf16 v[66:81], a[36:39], v[132:135], v[66:81]
v_mfma_f32_32x32x16_bf16 v[224:239], a[88:91], v[136:139], v[224:239]
v_mfma_f32_32x32x16_bf16 v[50:65], a[92:95], v[136:139], v[50:65]
v_mfma_f32_32x32x16_bf16 v[240:255], a[88:91], v[140:143], v[240:255]
v_mfma_f32_32x32x16_bf16 v[66:81], a[92:95], v[140:143], v[66:81]
v_mfma_f32_32x32x16_bf16 v[224:239], a[144:147], v[144:147], v[224:239]
v_mfma_f32_32x32x16_bf16 v[50:65], a[148:151], v[144:147], v[50:65]
v_mfma_f32_32x32x16_bf16 v[240:255], a[144:147], v[148:151], v[240:255]
v_mfma_f32_32x32x16_bf16 v[66:81], a[148:151], v[148:151], v[66:81]
v_mfma_f32_32x32x16_bf16 v[224:239], a[200:203], v[152:155], v[224:239]
v_mfma_f32_32x32x16_bf16 v[50:65], a[204:207], v[152:155], v[50:65]
v_mfma_f32_32x32x16_bf16 v[240:255], a[200:203], v[156:159], v[240:255]
v_mfma_f32_32x32x16_bf16 v[66:81], a[204:207], v[156:159], v[66:81]
s_mov_b32 s68, 0
dslowl_82:
s_add_u32 s68, s68, 1
s_cmp_lt_u32 s68, 0x30d40
s_cbranch_scc0 L_abort
buffer_load_dwordx4 v[128:131], v1, s[44:47], s84 offen offset:1024 sc1
buffer_load_dwordx4 v[132:135], v1, s[44:47], s85 offen offset:1024 sc1
buffer_load_dwordx4 v[136:139], v1, s[44:47], s92 offen offset:0 sc1
buffer_load_dwordx4 v[140:143], v1, s[44:47], s93 offen offset:0 sc1
buffer_load_dwordx4 v[144:147], v1, s[44:47], s94 offen offset:3072 sc1
buffer_load_dwordx4 v[148:151], v1, s[44:47], s95 offen offset:3072 sc1
buffer_load_dwordx4 v[152:155], v1, s[44:47], s98 offen offset:2048 sc1
buffer_load_dwordx4 v[156:159], v1, s[44:47], s99 offen offset:2048 sc1
s_waitcnt vmcnt(7)
v_max3_u32 v40, v128, v129, v130
s_waitcnt vmcnt(6)
v_max3_u32 v40, v40, v131, v132
v_max3_u32 v40, v40, v133, v134
s_waitcnt vmcnt(5)
v_max3_u32 v40, v40, v135, v136
v_max3_u32 v40, v40, v137, v138
s_waitcnt vmcnt(4)
v_max3_u32 v40, v40, v139, v140
v_max3_u32 v40, v40, v141, v142
s_waitcnt vmcnt(3)
v_max3_u32 v40, v40, v143, v144
v_max3_u32 v40, v40, v145, v146
s_waitcnt vmcnt(2)
v_max3_u32 v40, v40, v147, v148
v_max3_u32 v40, v40, v149, v150
s_waitcnt vmcnt(1)
v_max3_u32 v40, v40, v151, v152
v_max3_u32 v40, v40, v153, v154
s_waitcnt vmcnt(0)
v_max3_u32 v40, v40, v155, v156
v_max3_u32 v40, v40, v157, v158
v_max_u32 v40, v40, v159
v_cmp_eq_u32 vcc, -1, v40
s_nop 4
s_cbranch_vccnz dslowl_82
v_mfma_f32_32x32x16_bf16 v[224:239], a[40:43], v[128:131], v[224:239]
v_mfma_f32_32x32x16_bf16 v[50:65], a[44:47], v[128:131], v[50:65]
v_mfma_f32_32x32x16_bf16 v[240:255], a[40:43], v[132:135], v[240:255]
v_mfma_f32_32x32x16_bf16 v[66:81], a[44:47], v[132:135], v[66:81]
v_mfma_f32_32x32x16_bf16 v[224:239], a[96:99], v[136:139], v[224:239]
v_mfma_f32_32x32x16_bf16 v[50:65], a[100:103], v[136:139], v[50:65]
v_mfma_f32_32x32x16_bf16 v[240:255], a[96:99], v[140:143], v[240:255]
v_mfma_f32_32x32x16_bf16 v[66:81], a[100:103], v[140:143], v[66:81]
v_mfma_f32_32x32x16_bf16 v[224:239], a[152:155], v[144:147], v[224:239]
v_mfma_f32_32x32x16_bf16 v[50:65], a[156:159], v[144:147], v[50:65]
v_mfma_f32_32x32x16_bf16 v[240:255], a[152:155], v[148:151], v[240:255]
v_mfma_f32_32x32x16_bf16 v[66:81], a[156:159], v[148:151], v[66:81]
v_mfma_f32_32x32x16_bf16 v[224:239], a[208:211], v[152:155], v[224:239]
v_mfma_f32_32x32x16_bf16 v[50:65], a[212:215], v[152:155], v[50:65]
v_mfma_f32_32x32x16_bf16 v[240:255], a[208:211], v[156:159], v[240:255]
v_mfma_f32_32x32x16_bf16 v[66:81], a[212:215], v[156:159], v[66:81]
v_cmp_eq_u32 vcc, -1, v120
s_branch dgo_38
L_abort:
s_endpgm
)ASM"
    :: "s"(ka), "s"(b), "s"(w)
    : "v0", "v1", "v2", "v3", "v4", "v5", "v6", "v7", "v8", "v9", "v10", "v11", "v12", "v13", "v14", "v15", "v16", "v17", "v18", "v19", "v20", "v21", "v22", "v23", "v24", "v25", "v26", "v27", "v28", "v29", "v30", "v31", "v32", "v33", "v34", "v35", "v36", "v37", "v38", "v39", "v40", "v41", "v42", "v43", "v44", "v45", "v46", "v47", "v48", "v49", "v50", "v51", "v52", "v53", "v54", "v55", "v56", "v57", "v58", "v59", "v60", "v61", "v62", "v63", "v64", "v65", "v66", "v67", "v68", "v69", "v70", "v71", "v72", "v73", "v74", "v75", "v76", "v77", "v78", "v79", "v80", "v81", "v82", "v83", "v84", "v85", "v86", "v87", "v88", "v89", "v90", "v91", "v92", "v93", "v94", "v95", "v96", "v97", "v98", "v99", "v100", "v101", "v102", "v103", "v104", "v105", "v106", "v107", "v108", "v109", "v110", "v111", "v112", "v113", "v114", "v115", "v116", "v117", "v118", "v119", "v120", "v121", "v122", "v123", "v124", "v125", "v126", "v127", "v128", "v129", "v130", "v131", "v132", "v133", "v134", "v135", "v136", "v137", "v138", "v139", "v140", "v141", "v142", "v143", "v144", "v145", "v146", "v147", "v148", "v149", "v150", "v151", "v152", "v153", "v154", "v155", "v156", "v157", "v158", "v159", "v160", "v161", "v162", "v163", "v164", "v165", "v166", "v167", "v168", "v169", "v170", "v171", "v172", "v173", "v174", "v175", "v176", "v177", "v178", "v179", "v180", "v181", "v182", "v183", "v184", "v185", "v186", "v187", "v188", "v189", "v190", "v191", "v192", "v193", "v194", "v195", "v196", "v197", "v198", "v199", "v200", "v201", "v202", "v203", "v204", "v205", "v206", "v207", "v208", "v209", "v210", "v211", "v212", "v213", "v214", "v215", "v216", "v217", "v218", "v219", "v220", "v221", "v222", "v223", "v224", "v225", "v226", "v227", "v228", "v229", "v230", "v231", "v232", "v233", "v234", "v235", "v236", "v237", "v238", "v239", "v240", "v241", "v242", "v243", "v244", "v245", "v246", "v247", "v248", "v249", "v250", "v251", "v252", "v253", "v254", "v255", "a0", "a1", "a2", "a3", "a4", "a5", "a6", "a7", "a8", "a9", "a10", "a11", "a12", "a13", "a14", "a15", "a16", "a17", "a18", "a19", "a20", "a21", "a22", "a23", "a24", "a25", "a26", "a27", "a28", "a29", "a30", "a31", "a32", "a33", "a34", "a35", "a36", "a37", "a38", "a39", "a40", "a41", "a42", "a43", "a44", "a45", "a46", "a47", "a48", "a49", "a50", "a51", "a52", "a53", "a54", "a55", "a56", "a57", "a58", "a59", "a60", "a61", "a62", "a63", "a64", "a65", "a66", "a67", "a68", "a69", "a70", "a71", "a72", "a73", "a74", "a75", "a76", "a77", "a78", "a79", "a80", "a81", "a82", "a83", "a84", "a85", "a86", "a87", "a88", "a89", "a90", "a91", "a92", "a93", "a94", "a95", "a96", "a97", "a98", "a99", "a100", "a101", "a102", "a103", "a104", "a105", "a106", "a107", "a108", "a109", "a110", "a111", "a112", "a113", "a114", "a115", "a116", "a117", "a118", "a119", "a120", "a121", "a122", "a123", "a124", "a125", "a126", "a127", "a128", "a129", "a130", "a131", "a132", "a133", "a134", "a135", "a136", "a137", "a138", "a139", "a140", "a141", "a142", "a143", "a144", "a145", "a146", "a147", "a148", "a149", "a150", "a151", "a152", "a153", "a154", "a155", "a156", "a157", "a158", "a159", "a160", "a161", "a162", "a163", "a164", "a165", "a166", "a167", "a168", "a169", "a170", "a171", "a172", "a173", "a174", "a175", "a176", "a177", "a178", "a179", "a180", "a181", "a182", "a183", "a184", "a185", "a186", "a187", "a188", "a189", "a190", "a191", "a192", "a193", "a194", "a195", "a196", "a197", "a198", "a199", "a200", "a201", "a202", "a203", "a204", "a205", "a206", "a207", "a208", "a209", "a210", "a211", "a212", "a213", "a214", "a215", "a216", "a217", "a218", "a219", "a220", "a221", "a222", "a223", "a224", "a225", "a226", "a227", "a228", "a229", "a230", "a231", "a232", "a233", "a234", "a235", "a236", "a237", "a238", "a239", "a240", "a241", "a242", "a243", "a244", "a245", "a246", "a247", "a248", "a249", "a250", "a251", "a252", "a253", "a254", "a255", "s8", "s9", "s10", "s11", "s12", "s13", "s14", "s15", "s16", "s17", "s18", "s19", "s20", "s21", "s22", "s23", "s24", "s25", "s26", "s27", "s28", "s29", "s30", "s31", "s32", "s33", "s34", "s35", "s36", "s37", "s38", "s39", "s40", "s41", "s42", "s43", "s44", "s45", "s46", "s47", "s48", "s49", "s50", "s51", "s52", "s53", "s54", "s55", "s56", "s57", "s58", "s59", "s60", "s61", "s62", "s63", "s64", "s65", "s66", "s67", "s68", "s69", "s70", "s71", "s72", "s73", "s74", "s75", "s76", "s77", "s78", "s79", "s80", "s81", "s82", "s83", "s84", "s85", "s86", "s87", "s88", "s89", "s90", "s91", "s92", "s93", "s94", "s95", "s96", "s97", "s98", "s99", "s100", "s101", "vcc", "scc", "memory");
}


extern "C" void kernel_launch(void* const* d_in, const int* in_sizes, int n_in, void* d_out, int out_size, void* d_ws,
                              size_t ws_size, hipStream_t stream) {
  KArgs a;
  a.x = d_in[0]; a.wq = d_in[1]; a.wk = d_in[2]; a.wv = d_in[3]; a.wo = d_in[4];
  a.wg = d_in[6]; a.wu = d_in[7]; a.wd = d_in[8];
  a.out = d_out; a.ws = d_ws;
  (void)hipFuncSetAttribute((const void*)fused_block, hipFuncAttributeMaxDynamicSharedMemorySize, 163840);
  fused_block<<<256, 256, 163840, stream>>>(a);
}
```
